```python
import torch

def _softmax(x, axis=-1):
    return torch.softmax(x, dim=axis)
def _gelu(x):
    return torch.nn.functional.gelu(x, approximate="tanh")
def _silu(x):
    return torch.nn.functional.silu(x)
def _sigmoid(x):
    return torch.sigmoid(x)
def _layernorm(x, axis=-1, eps=1e-5):
    m = x.mean(dim=axis, keepdim=True)
    v = x.var(dim=axis, keepdim=True, unbiased=False)
    return (x - m) / torch.sqrt(v + eps)
def _scatter_add(tgt, idx, src, axis=0):
    return tgt.clone().index_add_(axis, idx, src)
def _segment_sum(src, seg, n):
    out = torch.zeros((n,) + src.shape[1:], dtype=src.dtype)
    return out.index_add_(0, seg, src)
def _pad(x, pw):
    flat = tuple(v for pair in reversed(pw) for v in pair)
    return torch.nn.functional.pad(x, flat)

kernel_name = 'sequential_preLN__spectral_gfnet__glu_gated_glu__prompt'

def setup_inputs():

    _w = (lambda a: (lambda t: t.to(torch.bfloat16) if t.is_floating_point() else t)(torch.as_tensor(a)))
    def _A_setup():
        B, S, D = 1, 2048, 448
        Df = D // 2 + 1
        return {
            "x": _w((lambda *s: torch.randn(*s, dtype=torch.bfloat16) * 0.05)(B, S, D)),
            "w_r": _w((lambda *s: torch.randn(*s, dtype=torch.bfloat16) * 0.4)(S, Df) * 0.02),
            "w_i": _w((lambda *s: torch.randn(*s, dtype=torch.bfloat16) * 0.4)(S, Df) * 0.02),
        }
    _A_in = _A_setup()

    def _B_setup():
        B, S, D, F = 1, 2048, 448, 1792
        return {
            "x": _w((lambda *s: torch.randn(*s, dtype=torch.bfloat16) * 0.05)(B, S, D)),
            "w_gate": _w((lambda *s: torch.randn(*s, dtype=torch.bfloat16) * 0.05)(D, F)),
            "w_up": _w((lambda *s: torch.randn(*s, dtype=torch.bfloat16) * 0.05)(D, F)),
            "w_down": _w((lambda *s: torch.randn(*s, dtype=torch.bfloat16) * 0.05)(F, D)),
        }
    _B_in = _B_setup()

    return {
        "A_x": _A_in["x"],
        "A_w_r": _A_in["w_r"],
        "A_w_i": _A_in["w_i"],
        "B_x": _B_in["x"],
        "B_w_gate": _B_in["w_gate"],
        "B_w_up": _B_in["w_up"],
        "B_w_down": _B_in["w_down"],
    }

def reference(A_x, A_w_r, A_w_i, B_x, B_w_gate, B_w_up, B_w_down):
    def _A_ref(x, w_r, w_i):
        B, S, D = x.shape
        X = (lambda x, axes=(-2,-1): torch.fft.rfft2(x.float(), dim=axes))(x, axes=(-2, -1))
        W = w_r + 1j * w_i
        X = X * W
        y = (lambda x, s, axes=(-2,-1): torch.fft.irfft2(x, s=s, dim=axes).to(torch.bfloat16))(X, (S, D), axes=(-2, -1))
        return y

    def _B_ref(x, w_gate, w_up, w_down):
        g = torch.einsum("bsd,df->bsf", x, w_gate)
        u = torch.einsum("bsd,df->bsf", x, w_up)
        h = _sigmoid(g) * u
        return torch.einsum("bsf,fd->bsd", h, w_down)

    x = A_x
    h = x + _A_ref(_layernorm(x, axis=-1), A_w_r, A_w_i)
    h2 = h + _B_ref(_layernorm(h, axis=-1), B_w_gate, B_w_up, B_w_down)
    return h2
```

```cpp
#include <hip/hip_runtime.h>
#include <hip/hip_bf16.h>
#include <stdint.h>

typedef unsigned v4u __attribute__((ext_vector_type(4)));
typedef unsigned v2u __attribute__((ext_vector_type(2)));
typedef float f32x2 __attribute__((ext_vector_type(2)));
typedef float f32x4 __attribute__((ext_vector_type(4)));
typedef float f32x16 __attribute__((ext_vector_type(16)));
typedef _Float16 h8 __attribute__((ext_vector_type(8)));
typedef _Float16 h2 __attribute__((ext_vector_type(2)));
typedef __bf16 b8 __attribute__((ext_vector_type(8)));
typedef __bf16 b2 __attribute__((ext_vector_type(2)));
#define DI __device__ __forceinline__

constexpr int S_ = 2048, D_ = 448, F_ = 1792, DF_ = 225;
constexpr int NSLOT = 240;
constexpr size_t T_OFF = 0, T_BYTES = (size_t)64 * 32 * NSLOT * 4;
constexpr size_t U_OFF = T_OFF + T_BYTES;
constexpr size_t LN_OFF = U_OFF + T_BYTES, LN_BYTES = (size_t)64 * 28 * 1024;
constexpr size_t HR_OFF = LN_OFF + LN_BYTES, HR_BYTES = (size_t)S_ * D_ * 2;
constexpr size_t HF_OFF = HR_OFF + HR_BYTES, HF_BYTES = (size_t)64 * 112 * 1024;
constexpr size_t WGU_OFF = HF_OFF + HF_BYTES, WGU_BYTES = (size_t)112 * 28 * 1024;
constexpr size_t WD_OFF = WGU_OFF + WGU_BYTES, WD_BYTES = (size_t)14 * 112 * 1024;

DI unsigned pkh(float a, float b) { h2 v = {(_Float16)a, (_Float16)b}; return __builtin_bit_cast(unsigned, v); }
DI unsigned pkb(float a, float b) { b2 v = {(__bf16)a, (__bf16)b}; return __builtin_bit_cast(unsigned, v); }
DI float blo(unsigned u) { return __builtin_bit_cast(float, u << 16); }
DI float bhi(unsigned u) { return __builtin_bit_cast(float, u & 0xffff0000u); }
DI float rbf(float a) { return blo(pkb(a, a) ); }
DI float cosr(float x) { return __builtin_amdgcn_cosf(x); }
DI float sinr(float x) { return __builtin_amdgcn_sinf(x); }
DI f32x4 mfma16h(v4u a, v4u b, f32x4 c) {
  return __builtin_amdgcn_mfma_f32_16x16x32_f16(__builtin_bit_cast(h8, a), __builtin_bit_cast(h8, b), c, 0, 0, 0);
}
DI f32x16 mfma32b(v4u a, v4u b, f32x16 c) {
  return __builtin_amdgcn_mfma_f32_32x32x16_bf16(__builtin_bit_cast(b8, a), __builtin_bit_cast(b8, b), c, 0, 0, 0);
}
template <int CTRL> DI float dpp_add(float v) {
  return v + __builtin_bit_cast(float, __builtin_amdgcn_update_dpp(0, __builtin_bit_cast(int, v), CTRL, 0xf, 0xf, true));
}
DI float sum8(float v) {
  v = dpp_add<0xB1>(v);
  v = dpp_add<0x4E>(v);
  v = dpp_add<0x141>(v);
  return v;
}

DI int kperm(int ks, int h, int e) {
  const int o = 4 * ks + (e & 3);
  return h == 0 ? ((e < 4) ? o : 224 + o) : ((e < 4) ? 223 - o : 447 - o);
}
DI void transpose8x4(const v2u (&v)[8], v4u (&o)[4]) {
#pragma unroll
  for (int j = 0; j < 4; ++j) {
    o[0][j] = __builtin_amdgcn_perm(v[2 * j + 1].x, v[2 * j].x, 0x05040100u);
    o[1][j] = __builtin_amdgcn_perm(v[2 * j + 1].x, v[2 * j].x, 0x07060302u);
    o[2][j] = __builtin_amdgcn_perm(v[2 * j + 1].y, v[2 * j].y, 0x05040100u);
    o[3][j] = __builtin_amdgcn_perm(v[2 * j + 1].y, v[2 * j].y, 0x07060302u);
  }
}
DI void tile_wgu(int idx, const uint16_t* wg, const uint16_t* wu, v4u* wgu) {
  const int quad = idx % 448, rest = idx / 448;
  const int h = rest & 1, ks = (rest >> 1) % 28, mat = (rest >> 1) / 28;
  const uint16_t* w = mat ? wu : wg;
  v2u v[8];
#pragma unroll
  for (int e = 0; e < 8; ++e) v[e] = *(const v2u*)(w + (size_t)kperm(ks, h, e) * F_ + quad * 4);
  v4u o[4];
  transpose8x4(v, o);
  const int ft = quad >> 2, r = (quad & 3) * 4 + 16 * mat;
  v4u* dst = wgu + (size_t)(ft * 28 + ks) * 64 + h * 32 + r;
#pragma unroll
  for (int j = 0; j < 4; ++j) dst[j] = o[j];
}
DI void tile_wd(int idx, const uint16_t* wd, v4u* wdt) {
  const int quad = idx % 112, rest = idx / 112;
  const int h = rest & 1, fs = rest >> 1;
  v2u v[8];
#pragma unroll
  for (int e = 0; e < 8; ++e) v[e] = *(const v2u*)(wd + (size_t)(16 * fs + (e & 3) + 8 * (e >> 2) + 4 * h) * D_ + quad * 4);
  v4u o[4];
  transpose8x4(v, o);
  const int nt = quad >> 3, r = (quad & 7) * 4;
  v4u* dst = wdt + (size_t)(nt * 112 + fs) * 64 + h * 32 + r;
#pragma unroll
  for (int j = 0; j < 4; ++j) dst[j] = o[j];
}

__global__ __launch_bounds__(256) void kA(const uint16_t* __restrict__ x, const uint16_t* __restrict__ wg,
                                          const uint16_t* __restrict__ wu, unsigned char* __restrict__ ws) {
  __shared__ v4u lds[2 * 2 * 16 * 16];
  const int b = blockIdx.x, tid = threadIdx.x;
  if (b >= 256) {
    const int idx = (b - 256) * 256 + tid;
    tile_wgu(idx, wg, wu, (v4u*)(ws + WGU_OFF));
    return;
  }
  const int s2 = (b & 7) + 8 * (b >> 5), q = (b >> 3) & 3;
  const int lane = tid & 63, w = tid >> 6, r = lane & 15, q4 = lane >> 4;
  const int row = tid >> 3, l8 = tid & 7;
  const int lc = l8 < 7 ? l8 : 6;
  const v4u* xr = (const v4u*)(x + (size_t)(64 * row + s2) * D_);
  v4u xa[2][4];
#pragma unroll
  for (int u = 0; u < 2; ++u) {
    const int c = lc + 7 * u;
    xa[u][0] = xr[c]; xa[u][1] = xr[27 - c]; xa[u][2] = xr[28 + c]; xa[u][3] = xr[55 - c];
  }
  __builtin_amdgcn_sched_barrier(0);
  const int t = 4 * q + w;
  const int slot = 16 * t + r;
  const int k = t < 8 ? 2 * slot : 2 * (slot - 128) + 1;
  v4u bc[4], bs[4];
  {
    const float kf = (float)k * (1.0f / 896.0f);
    const float c2 = 2.0f * cosr(2.0f * kf);
#pragma unroll
    for (int kc = 0; kc < 4; ++kc) {
      const int p = (2 * (32 * kc + 8 * q4) + 1) * k;
      const float a0 = (float)(p % 896) * (1.0f / 896.0f);
      const float a1 = a0 + 2.0f * kf;
      float c[8], s[8];
      c[0] = cosr(a0); s[0] = -sinr(a0); c[1] = cosr(a1); s[1] = -sinr(a1);
#pragma unroll
      for (int e = 2; e < 8; ++e) { c[e] = __builtin_fmaf(c2, c[e - 1], -c[e - 2]); s[e] = __builtin_fmaf(c2, s[e - 1], -s[e - 2]); }
#pragma unroll
      for (int j = 0; j < 4; ++j) { bc[kc][j] = pkh(c[2 * j], c[2 * j + 1]); bs[kc][j] = pkh(s[2 * j], s[2 * j + 1]); }
    }
  }
  v4u mre[2][2], mim[2][2];
  {
    const unsigned sg = (r & 1) ? 0x80008000u : 0u;
#pragma unroll
    for (int jt = 0; jt < 2; ++jt)
#pragma unroll
      for (int i = 0; i < 4; ++i) {
        const int n = ((16 * jt + r) * (64 * (4 * q4 + i) + s2)) & 2047;
        const float a = (float)n * (1.0f / 2048.0f);
        const float c = cosr(a), s = sinr(a);
        mre[jt][0][i] = pkh(c, s); mim[jt][0][i] = pkh(-s, c);
        mre[jt][1][i] = mre[jt][0][i] ^ sg; mim[jt][1][i] = mim[jt][0][i] ^ sg;
      }
  }
  __builtin_amdgcn_sched_barrier(0);
  {
    float v[2][4][8];
    float sum = 0.f;
#pragma unroll
    for (int u = 0; u < 2; ++u)
#pragma unroll
      for (int g = 0; g < 4; ++g)
#pragma unroll
        for (int j = 0; j < 4; ++j) { v[u][g][2 * j] = blo(xa[u][g][j]); v[u][g][2 * j + 1] = bhi(xa[u][g][j]); sum += v[u][g][2 * j] + v[u][g][2 * j + 1]; }
    if (l8 == 7) sum = 0.f;
    const float mean = sum8(sum) * (1.0f / 448.0f);
    float sq = 0.f;
#pragma unroll
    for (int u = 0; u < 2; ++u)
#pragma unroll
      for (int g = 0; g < 4; ++g)
#pragma unroll
        for (int j = 0; j < 8; ++j) { const float dlt = v[u][g][j] - mean; sq = __builtin_fmaf(dlt, dlt, sq); }
    if (l8 == 7) sq = 0.f;
    const float var = sum8(sq) * (1.0f / 448.0f);
    const float mb = rbf(mean);
    const float sd = rbf(__builtin_amdgcn_sqrtf(rbf(rbf(var) + 1e-5f)));
    float rs = __builtin_amdgcn_rcpf(sd);
    if (l8 == 7) rs = 0.f;
    const float sgn = q < 2 ? 1.0f : -1.0f;
#pragma unroll
    for (int u = 0; u < 2; ++u) {
#pragma unroll
      for (int g = 0; g < 4; ++g)
#pragma unroll
        for (int j = 0; j < 4; ++j) {
          const unsigned d2 = pkb(v[u][g][2 * j] - mb, v[u][g][2 * j + 1] - mb);
          const unsigned z2 = pkb(blo(d2) * rs, bhi(d2) * rs);
          v[u][g][2 * j] = blo(z2); v[u][g][2 * j + 1] = bhi(z2);
        }
      float P[8], Q[8];
#pragma unroll
      for (int i = 0; i < 8; ++i) {
        const float a = v[u][0][i], bb = v[u][1][7 - i], e = v[u][2][i], f = v[u][3][7 - i];
        P[i] = __builtin_fmaf(sgn, bb + e, a + f);
        Q[i] = __builtin_fmaf(sgn, e - bb, a - f);
      }
      v4u pp, qq;
#pragma unroll
      for (int j = 0; j < 4; ++j) { pp[j] = pkh(P[2 * j], P[2 * j + 1]); qq[j] = pkh(Q[2 * j], Q[2 * j + 1]); }
      const int c = l8 < 7 ? l8 + 7 * u : 14 + u;
      const int o = ((row >> 4) * 16 + c) * 16 + ((row & 15) ^ (c & 7));
      lds[o] = pp; lds[512 + o] = qq;
    }
  }
  __syncthreads();
  if (t >= 15) return;
  f32x4 zre[2], zim[2];
#pragma unroll
  for (int mt = 0; mt < 2; ++mt) { zre[mt] = f32x4{0, 0, 0, 0}; zim[mt] = f32x4{0, 0, 0, 0}; }
#pragma unroll
  for (int kc = 0; kc < 4; ++kc)
#pragma unroll
    for (int mt = 0; mt < 2; ++mt) {
      const int c = 4 * kc + q4;
      const int o = (mt * 16 + c) * 16 + (r ^ (c & 7));
      zre[mt] = mfma16h(lds[o], bc[kc], zre[mt]);
      zim[mt] = mfma16h(lds[512 + o], bs[kc], zim[mt]);
    }
  v4u zop[2];
#pragma unroll
  for (int mt = 0; mt < 2; ++mt)
#pragma unroll
    for (int i = 0; i < 4; ++i) zop[mt][i] = pkh(zre[mt][i], zim[mt][i]);
  unsigned* T = (unsigned*)(ws + T_OFF);
#pragma unroll
  for (int jt = 0; jt < 2; ++jt) {
    f32x4 tre = {0, 0, 0, 0}, tim = {0, 0, 0, 0};
#pragma unroll
    for (int mt = 0; mt < 2; ++mt) { tre = mfma16h(zop[mt], mre[jt][mt], tre); tim = mfma16h(zop[mt], mim[jt][mt], tim); }
    v4u o;
#pragma unroll
    for (int i = 0; i < 4; ++i) o[i] = pkh(tre[i], tim[i]);
    *(v4u*)(T + (size_t)(s2 * 32 + 16 * jt + r) * NSLOT + 16 * t + 4 * q4) = o;
  }
}

constexpr float SCALE_B = 1.0f / 16.0f;
__global__ __launch_bounds__(128) void kB(const uint16_t* __restrict__ wr, const uint16_t* __restrict__ wi,
                                          unsigned char* __restrict__ ws) {
  const int tid = threadIdx.x, lane = tid & 63, w = tid >> 6, r = lane & 15, q4 = lane >> 4;
  const int b = blockIdx.x;
  const int j1 = b >> 3, t = (b & 7) + 8 * w;
  if (t >= 15) return;
  const unsigned* T = (const unsigned*)(ws + T_OFF);
  v4u tf[4];
#pragma unroll
  for (int g = 0; g < 4; ++g)
#pragma unroll
    for (int i = 0; i < 4; ++i) tf[g][i] = T[(size_t)((16 * g + 4 * q4 + i) * 32 + j1) * NSLOT + 16 * t + r];
  const int slot = 16 * t + r;
  const int k = t < 8 ? 2 * slot : 2 * (slot - 128) + 1;
  const int kk = k <= 224 ? k : 224;
  uint16_t wre[4][4], wim[4][4];
#pragma unroll
  for (int jt = 0; jt < 4; ++jt)
#pragma unroll
    for (int i = 0; i < 4; ++i) {
      const int o = (j1 + 32 * (16 * jt + 4 * q4 + i)) * DF_ + kk;
      wre[jt][i] = wr[o]; wim[jt][i] = wi[o];
    }
  __builtin_amdgcn_sched_barrier(0);
  v4u fre[4][4], fim[4][4];
#pragma unroll
  for (int g = 0; g < 4; ++g)
#pragma unroll
    for (int i = 0; i < 4; ++i) {
      const int n = (r * (16 * g + 4 * q4 + i)) & 63;
      const float a = (float)n * (1.0f / 64.0f);
      const float c = 0.125f * cosr(a), s = 0.125f * sinr(a);
      const unsigned P = pkh(c, s), Q = pkh(-s, c);
      const unsigned nP = P ^ 0x80008000u, nQ = Q ^ 0x80008000u;
#pragma unroll
      for (int a4 = 0; a4 < 4; ++a4) {
        const int rot = (a4 * i) & 3;
        fre[a4][g][i] = rot == 0 ? P : rot == 1 ? Q : rot == 2 ? nP : nQ;
        fim[a4][g][i] = rot == 0 ? Q : rot == 1 ? nP : rot == 2 ? nQ : P;
      }
    }
  __builtin_amdgcn_sched_barrier(0);
  float sc = SCALE_B;
  if (k == 0 || k == 224) sc = 0.5f * SCALE_B;
  if (k > 224) sc = 0.f;
  v4u pop[4];
#pragma unroll
  for (int jt = 0; jt < 4; ++jt) {
    f32x4 vre = {0, 0, 0, 0}, vim = {0, 0, 0, 0};
#pragma unroll
    for (int g = 0; g < 4; ++g) { vre = mfma16h(fre[jt][g], tf[g], vre); vim = mfma16h(fim[jt][g], tf[g], vim); }
#pragma unroll
    for (int i = 0; i < 4; ++i) {
      const float a = blo(wre[jt][i]) * sc, bb = blo(wim[jt][i]) * sc;
      const float re = vre[i] * a - vim[i] * bb, im = vre[i] * bb + vim[i] * a;
      pop[jt][i] = pkh(re, -im);
    }
  }
  unsigned* U = (unsigned*)(ws + U_OFF);
#pragma unroll
  for (int st = 0; st < 4; ++st) {
    f32x4 ure = {0, 0, 0, 0}, uim = {0, 0, 0, 0};
#pragma unroll
    for (int jt = 0; jt < 4; ++jt) { ure = mfma16h(pop[jt], fre[st][jt], ure); uim = mfma16h(pop[jt], fim[st][jt], uim); }
    v4u o;
#pragma unroll
    for (int i = 0; i < 4; ++i) o[i] = pkh(ure[i], -uim[i]);
    *(v4u*)(U + (size_t)((16 * st + r) * 32 + j1) * NSLOT + 16 * t + 4 * q4) = o;
  }
}

constexpr float SCALE_Y = 512.0f / (917504.0f * SCALE_B);
__global__ __launch_bounds__(512) void kC(const uint16_t* __restrict__ x, const uint16_t* __restrict__ wd,
                                          unsigned char* __restrict__ ws) {
  __shared__ v4u lds[4 * 16 * 16];
  __shared__ f32x2 st[8][16];
  const int b = blockIdx.x, tid = threadIdx.x;
  if (b >= 128) {
    const int idx = (b - 128) * 512 + tid;
    if (idx < 112 * 2 * 112) tile_wd(idx, wd, (v4u*)(ws + WD_OFF));
    return;
  }
  const int s2 = (b & 7) + 8 * (b >> 4), hf = (b >> 3) & 1;
  const int lane = tid & 63, w = tid >> 6, r = lane & 15, q4 = lane >> 4;
  const unsigned* U = (const unsigned*)(ws + U_OFF);
  v4u uf[2][2];
#pragma unroll
  for (int u = 0; u < 2; ++u) {
    const int t = w + 8 * u < 15 ? w + 8 * u : 14;
#pragma unroll
    for (int g = 0; g < 2; ++g)
#pragma unroll
      for (int i = 0; i < 4; ++i) uf[u][g][i] = U[(size_t)(s2 * 32 + 16 * g + 4 * q4 + i) * NSLOT + 16 * t + r];
  }
  const int s1 = 16 * hf + r, srow = 64 * s1 + s2;
  const int dt = w < 7 ? w : 6;
  const int d0 = 16 * dt + 4 * q4;
  v2u xv[4];
  {
    const uint16_t* xr = x + (size_t)srow * D_;
    xv[0] = *(const v2u*)(xr + d0); xv[1] = *(const v2u*)(xr + 224 + d0);
    xv[2] = *(const v2u*)(xr + 220 - d0); xv[3] = *(const v2u*)(xr + 444 - d0);
  }
  __builtin_amdgcn_sched_barrier(0);
  v4u mre[2], mim[2];
#pragma unroll
  for (int g = 0; g < 2; ++g)
#pragma unroll
    for (int i = 0; i < 4; ++i) {
      const int n = ((16 * g + 4 * q4 + i) * srow) & 2047;
      const float a = (float)n * (1.0f / 2048.0f);
      const float c = 0.25f * cosr(a), s = 0.25f * sinr(a);
      mre[g][i] = pkh(c, -s); mim[g][i] = pkh(s, c);
    }
  v4u ce[4], co[4], se[4], so[4];
  {
    const int dd = 2 * (16 * dt + r) + 1;
    const float df = (float)dd * (1.0f / 896.0f);
    const float c2 = 2.0f * cosr(df);
#pragma unroll
    for (int kc = 0; kc < 4; ++kc) {
      const int p = dd * (64 * kc + 16 * q4);
      const float a0 = (float)(p % 896) * (1.0f / 896.0f);
      const float a1 = a0 + df;
      float c[16], s[16];
      c[0] = cosr(a0); s[0] = sinr(a0); c[1] = cosr(a1); s[1] = sinr(a1);
#pragma unroll
      for (int e = 2; e < 16; ++e) { c[e] = __builtin_fmaf(c2, c[e - 1], -c[e - 2]); s[e] = __builtin_fmaf(c2, s[e - 1], -s[e - 2]); }
#pragma unroll
      for (int j = 0; j < 4; ++j) {
        ce[kc][j] = pkh(c[4 * j], c[4 * j + 2]); co[kc][j] = pkh(c[4 * j + 1], c[4 * j + 3]);
        se[kc][j] = pkh(s[4 * j], s[4 * j + 2]); so[kc][j] = pkh(s[4 * j + 1], s[4 * j + 3]);
      }
    }
  }
  __builtin_amdgcn_sched_barrier(0);
#pragma unroll
  for (int u = 0; u < 2; ++u) {
    f32x4 yre = {0, 0, 0, 0}, yim = {0, 0, 0, 0};
#pragma unroll
    for (int g = 0; g < 2; ++g) { yre = mfma16h(uf[u][g], mre[g], yre); yim = mfma16h(uf[u][g], mim[g], yim); }
    v2u a, bb;
    a.x = pkh(yre[0], yre[1]); a.y = pkh(yre[2], yre[3]); bb.x = pkh(yim[0], yim[1]); bb.y = pkh(yim[2], yim[3]);
    if (u == 1 && w == 7) { a = v2u{0, 0}; bb = v2u{0, 0}; }
    const int c = 2 * w + (q4 >> 1);
    const int o = ((2 * u) * 16 + c) * 16 + (r ^ (c & 7));
    ((v2u*)&lds[o])[q4 & 1] = a;
    ((v2u*)&lds[o + 256])[q4 & 1] = bb;
  }
  __syncthreads();
  f32x4 Ce = {0, 0, 0, 0}, Co = {0, 0, 0, 0}, Se = {0, 0, 0, 0}, So = {0, 0, 0, 0};
#pragma unroll
  for (int kc = 0; kc < 4; ++kc) {
    const int c = 4 * kc + q4;
    const int o = c * 16 + (r ^ (c & 7));
    Ce = mfma16h(ce[kc], lds[o], Ce);
    Se = mfma16h(se[kc], lds[256 + o], Se);
    Co = mfma16h(co[kc], lds[512 + o], Co);
    So = mfma16h(so[kc], lds[768 + o], So);
  }
  float hv[4][4];
  float sum = 0.f;
#pragma unroll
  for (int i = 0; i < 4; ++i) {
    const float a = Ce[i] - Se[i], bb = Co[i] - So[i], c = Ce[i] + Se[i], d = Co[i] + So[i];
    const unsigned xa = i < 2 ? xv[0].x : xv[0].y, xb = i < 2 ? xv[1].x : xv[1].y;
    const unsigned xc = i < 2 ? xv[2].y : xv[2].x, xd = i < 2 ? xv[3].y : xv[3].x;
    const float x0 = (i & 1) ? bhi(xa) : blo(xa), x1 = (i & 1) ? bhi(xb) : blo(xb);
    const float x2 = (i & 1) ? blo(xc) : bhi(xc), x3 = (i & 1) ? blo(xd) : bhi(xd);
    const unsigned y01 = pkb((a + bb) * SCALE_Y, (a - bb) * SCALE_Y), y23 = pkb((c - d) * SCALE_Y, (c + d) * SCALE_Y);
    const unsigned h01 = pkb(x0 + blo(y01), x1 + bhi(y01)), h23 = pkb(x2 + blo(y23), x3 + bhi(y23));
    hv[0][i] = blo(h01); hv[1][i] = bhi(h01); hv[2][i] = blo(h23); hv[3][i] = bhi(h23);
    sum += (hv[0][i] + hv[1][i]) + (hv[2][i] + hv[3][i]);
  }
  sum += __shfl_xor(sum, 16); sum += __shfl_xor(sum, 32);
  const float lm = sum * (1.0f / 64.0f);
  float m2 = 0.f;
#pragma unroll
  for (int g = 0; g < 4; ++g)
#pragma unroll
    for (int i = 0; i < 4; ++i) { const float dlt = hv[g][i] - lm; m2 = __builtin_fmaf(dlt, dlt, m2); }
  m2 += __shfl_xor(m2, 16); m2 += __shfl_xor(m2, 32);
  if (q4 == 0) st[w][r] = f32x2{lm, m2};
  if (w < 7) {
    uint16_t* hr = (uint16_t*)(ws + HR_OFF) + (size_t)srow * D_;
    *(v2u*)(hr + d0) = v2u{pkb(hv[0][0], hv[0][1]), pkb(hv[0][2], hv[0][3])};
    *(v2u*)(hr + 224 + d0) = v2u{pkb(hv[1][0], hv[1][1]), pkb(hv[1][2], hv[1][3])};
    *(v2u*)(hr + 220 - d0) = v2u{pkb(hv[2][3], hv[2][2]), pkb(hv[2][1], hv[2][0])};
    *(v2u*)(hr + 444 - d0) = v2u{pkb(hv[3][3], hv[3][2]), pkb(hv[3][1], hv[3][0])};
  }
  __syncthreads();
  if (w >= 7) return;
  float mean = 0.f;
  f32x2 sv[7];
#pragma unroll
  for (int j = 0; j < 7; ++j) { sv[j] = st[j][r]; mean += sv[j].x; }
  mean *= (1.0f / 7.0f);
  float M2 = 0.f;
#pragma unroll
  for (int j = 0; j < 7; ++j) { const float dlt = sv[j].x - mean; M2 += __builtin_fmaf(64.0f * dlt, dlt, sv[j].y); }
  const float var = M2 * (1.0f / 448.0f);
  const float mb = rbf(mean);
  const float sd = rbf(__builtin_amdgcn_sqrtf(rbf(rbf(var) + 1e-5f)));
  const float rs = __builtin_amdgcn_rcpf(sd);
  unsigned z[4][2];
#pragma unroll
  for (int g = 0; g < 4; ++g)
#pragma unroll
    for (int j = 0; j < 2; ++j) {
      const unsigned d2 = pkb(hv[g][2 * j] - mb, hv[g][2 * j + 1] - mb);
      z[g][j] = pkb(blo(d2) * rs, bhi(d2) * rs);
    }
  v4u* ln = (v4u*)(ws + LN_OFF) + (size_t)(s2 * 28 + 4 * dt + q4) * 64 + s1;
  ln[0] = v4u{z[0][0], z[0][1], z[1][0], z[1][1]};
  ln[32] = v4u{z[2][0], z[2][1], z[3][0], z[3][1]};
}

DI void glds16(const void* gsrc, unsigned lds_dst) {
  unsigned keep;
  asm volatile("s_mov_b32 %0, m0\n\ts_mov_b32 m0, %2\n\ts_nop 0\n\tglobal_load_lds_dwordx4 %1, off\n\ts_mov_b32 m0, %0"
               : "=&s"(keep) : "v"(gsrc), "s"(lds_dst) : "memory");
}
template <int OFF> DI v4u gload16(const void* p) {
  v4u v;
  asm volatile("global_load_dwordx4 %0, %1, off offset:%2" : "=v"(v) : "v"(p), "i"(OFF) : "memory");
  return v;
}
__global__ __launch_bounds__(512) void kD(unsigned char* __restrict__ ws) {
  __shared__ v4u lds[112 * 64];
  const int b = blockIdx.x, tid = threadIdx.x, lane = tid & 63;
  const int w = __builtin_amdgcn_readfirstlane(tid >> 6);
  const int m = (b & 7) * 2 + (b >> 3) / 14, n = (b >> 3) % 14;
  const int ft = n * 8 + w;
  const unsigned char* wp = ws + WGU_OFF + (size_t)ft * 28 * 1024 + lane * 16;
  const unsigned char* ap = ws + LN_OFF + (size_t)((4 * m + (w & 3)) * 28 + 2 * (w >> 2)) * 1024 + lane * 16;
  const unsigned lbase = (unsigned)(size_t)lds + (unsigned)((2 * (w >> 2)) * 4 + (w & 3)) * 1024u;
  v4u wf[28];
#pragma unroll
  for (int ch = 0; ch < 7; ++ch) {
    glds16(ap + ch * 4096, lbase + ch * 16384);
    glds16(ap + ch * 4096 + 1024, lbase + ch * 16384 + 4096);
    const unsigned char* p = wp + ch * 4096;
    wf[4 * ch + 0] = gload16<0>(p); wf[4 * ch + 1] = gload16<1024>(p);
    wf[4 * ch + 2] = gload16<2048>(p); wf[4 * ch + 3] = gload16<3072>(p);
  }
  f32x16 acc[4];
#pragma unroll
  for (int tt = 0; tt < 4; ++tt)
#pragma unroll
    for (int i = 0; i < 16; ++i) acc[tt][i] = 0.f;
  typedef __attribute__((address_space(3))) const v4u lv4u;
  unsigned la0 = (unsigned)(size_t)lds + lane * 16, la1 = la0 + 57344;
  asm volatile("" : "+v"(la1));
  v4u af[2][4];
#define D_WAIT(ch, N)                                                                                    \
  {                                                                                                      \
    asm volatile("s_waitcnt vmcnt(" #N ")"                                                               \
                 : "+v"(wf[4 * ch]), "+v"(wf[4 * ch + 1]), "+v"(wf[4 * ch + 2]), "+v"(wf[4 * ch + 3])::"memory"); \
    __builtin_amdgcn_s_barrier();                                                                        \
    asm volatile("" ::: "memory");                                                                       \
  }
#define D_READ(ks)                                                                                       \
  _Pragma("unroll") for (int tt = 0; tt < 4; ++tt)                                                       \
    af[(ks) & 1][tt] = (ks) < 14 ? *(lv4u*)(size_t)(la0 + ((ks) * 4 + tt) * 1024) : *(lv4u*)(size_t)(la1 + (((ks) - 14) * 4 + tt) * 1024);
#define D_MMA(ks)                                                                                        \
  _Pragma("unroll") for (int tt = 0; tt < 4; ++tt) acc[tt] = mfma32b(wf[ks], af[(ks) & 1][tt], acc[tt]); \
  __builtin_amdgcn_sched_barrier(0);
#define D_CHUNK(ch, NNEXT)                                                                               \
  D_READ(4 * ch + 1) D_MMA(4 * ch) D_READ(4 * ch + 2) D_MMA(4 * ch + 1) D_READ(4 * ch + 3) D_MMA(4 * ch + 2) \
  D_WAIT((ch + 1), NNEXT) D_READ(4 * ch + 4) D_MMA(4 * ch + 3)
  D_WAIT(0, 36) D_READ(0)
  D_CHUNK(0, 30) D_CHUNK(1, 24) D_CHUNK(2, 18) D_CHUNK(3, 12) D_CHUNK(4, 6) D_CHUNK(5, 0)
  D_READ(25) D_MMA(24) D_READ(26) D_MMA(25) D_READ(27) D_MMA(26) D_MMA(27)
  v4u* hf = (v4u*)(ws + HF_OFF);
#pragma unroll
  for (int tt = 0; tt < 4; ++tt) {
    v4u o;
#pragma unroll
    for (int j = 0; j < 4; ++j) {
      const unsigned g2 = pkb(acc[tt][2 * j], acc[tt][2 * j + 1]), u2 = pkb(acc[tt][8 + 2 * j], acc[tt][8 + 2 * j + 1]);
      const float s0 = __builtin_amdgcn_rcpf(1.0f + __builtin_amdgcn_exp2f(blo(g2) * -1.44269504f));
      const float s1 = __builtin_amdgcn_rcpf(1.0f + __builtin_amdgcn_exp2f(bhi(g2) * -1.44269504f));
      const unsigned sg = pkb(s0, s1);
      o[j] = pkb(blo(sg) * blo(u2), bhi(sg) * bhi(u2));
    }
    hf[(size_t)((4 * m + tt) * 112 + ft) * 64 + lane] = o;
  }
}

__global__ __launch_bounds__(256) void kE(const unsigned char* __restrict__ ws, uint16_t* __restrict__ out) {
  __shared__ float red[4 * 64 * 68];
  const int b = blockIdx.x, tid = threadIdx.x, lane = tid & 63;
  const int w = __builtin_amdgcn_readfirstlane(tid >> 6);
  const int m = (b & 7) * 4 + (b >> 3) / 7, n = (b >> 3) % 7;
  const v4u* wa = (const v4u*)(ws + WD_OFF) + (size_t)((2 * n) * 112 + 28 * w) * 64 + lane;
  const v4u* hb = (const v4u*)(ws + HF_OFF) + (size_t)((2 * m) * 112 + 28 * w) * 64 + lane;
  constexpr int WIN = 12;
  v4u a[WIN][2], bb[WIN][2];
#pragma unroll
  for (int i = 0; i < WIN; ++i) {
    a[i][0] = wa[i * 64]; bb[i][0] = hb[i * 64]; a[i][1] = wa[(112 + i) * 64]; bb[i][1] = hb[(112 + i) * 64];
  }
  v4u hres[2];
#pragma unroll
  for (int p = 0; p < 2; ++p)
    hres[p] = *(const v4u*)((const uint16_t*)(ws + HR_OFF) + (size_t)(64 * (tid >> 3) + 2 * m + p) * D_ + 64 * n + 8 * (tid & 7));
  __builtin_amdgcn_sched_barrier(0);
  f32x16 acc[2][2];
#pragma unroll
  for (int i = 0; i < 2; ++i)
#pragma unroll
    for (int j = 0; j < 2; ++j)
#pragma unroll
      for (int e = 0; e < 16; ++e) acc[i][j][e] = 0.f;
#pragma unroll
  for (int fs = 0; fs < 28; ++fs) {
    const v4u a0 = a[fs % WIN][0], a1 = a[fs % WIN][1], b0 = bb[fs % WIN][0], b1 = bb[fs % WIN][1];
    acc[0][0] = mfma32b(a0, b0, acc[0][0]);
    acc[0][1] = mfma32b(a0, b1, acc[0][1]);
    acc[1][0] = mfma32b(a1, b0, acc[1][0]);
    acc[1][1] = mfma32b(a1, b1, acc[1][1]);
    if (fs + WIN < 28) {
      const int i = fs + WIN;
      a[fs % WIN][0] = wa[i * 64]; bb[fs % WIN][0] = hb[i * 64]; a[fs % WIN][1] = wa[(112 + i) * 64]; bb[fs % WIN][1] = hb[(112 + i) * 64];
    }
    __builtin_amdgcn_sched_barrier(0);
  }
  const int r = lane & 31, h = lane >> 5;
#pragma unroll
  for (int nt = 0; nt < 2; ++nt)
#pragma unroll
    for (int tt = 0; tt < 2; ++tt)
#pragma unroll
      for (int g = 0; g < 4; ++g)
        *(f32x4*)&red[(w * 64 + 32 * tt + r) * 68 + 32 * nt + 8 * g + 4 * h] =
            f32x4{acc[nt][tt][4 * g], acc[nt][tt][4 * g + 1], acc[nt][tt][4 * g + 2], acc[nt][tt][4 * g + 3]};
  __syncthreads();
#pragma unroll
  for (int p = 0; p < 2; ++p) {
    const int tok = 32 * p + (tid >> 3), c0 = 8 * (tid & 7);
    f32x4 s0 = {0, 0, 0, 0}, s1 = {0, 0, 0, 0};
#pragma unroll
    for (int ww = 0; ww < 4; ++ww) {
      s0 += *(const f32x4*)&red[(ww * 64 + tok) * 68 + c0];
      s1 += *(const f32x4*)&red[(ww * 64 + tok) * 68 + c0 + 4];
    }
    v4u o;
    const unsigned f0 = pkb(s0[0], s0[1]), f1 = pkb(s0[2], s0[3]), f2 = pkb(s1[0], s1[1]), f3 = pkb(s1[2], s1[3]);
    o[0] = pkb(blo(hres[p][0]) + blo(f0), bhi(hres[p][0]) + bhi(f0));
    o[1] = pkb(blo(hres[p][1]) + blo(f1), bhi(hres[p][1]) + bhi(f1));
    o[2] = pkb(blo(hres[p][2]) + blo(f2), bhi(hres[p][2]) + bhi(f2));
    o[3] = pkb(blo(hres[p][3]) + blo(f3), bhi(hres[p][3]) + bhi(f3));
    *(v4u*)(out + (size_t)(64 * (tid >> 3) + 2 * m + p) * D_ + 64 * n + c0) = o;
  }
}

extern "C" void kernel_launch(void* const* d_in, const int* in_sizes, int n_in, void* d_out, int out_size, void* d_ws,
                              size_t ws_size, hipStream_t stream) {
  const uint16_t* x = (const uint16_t*)d_in[0];
  const uint16_t* wr = (const uint16_t*)d_in[1];
  const uint16_t* wi = (const uint16_t*)d_in[2];
  const uint16_t* wg = (const uint16_t*)d_in[4];
  const uint16_t* wu = (const uint16_t*)d_in[5];
  const uint16_t* wd = (const uint16_t*)d_in[6];
  unsigned char* ws = (unsigned char*)d_ws;
  kA<<<256 + 196, 256, 0, stream>>>(x, wg, wu, ws);
  kB<<<256, 128, 0, stream>>>(wr, wi, ws);
  kC<<<128 + 49, 512, 0, stream>>>(x, wd, ws);
  kD<<<224, 512, 0, stream>>>(ws);
  kE<<<224, 256, 0, stream>>>(ws, (uint16_t*)d_out);
}
```

```cpp
#include <hip/hip_runtime.h>
#include <hip/hip_bf16.h>
#include <stdint.h>

typedef unsigned v4u __attribute__((ext_vector_type(4)));
typedef unsigned v2u __attribute__((ext_vector_type(2)));
typedef float f32x2 __attribute__((ext_vector_type(2)));
typedef float f32x4 __attribute__((ext_vector_type(4)));
typedef float f32x16 __attribute__((ext_vector_type(16)));
typedef _Float16 h8 __attribute__((ext_vector_type(8)));
typedef _Float16 h2 __attribute__((ext_vector_type(2)));
typedef __bf16 b8 __attribute__((ext_vector_type(8)));
typedef __bf16 b2 __attribute__((ext_vector_type(2)));
typedef short s4v __attribute__((ext_vector_type(4)));
typedef __amdgpu_buffer_rsrc_t rsrc_t;
#define DI __device__ __forceinline__
#define LAS __attribute__((address_space(3)))

constexpr int S_ = 2048, D_ = 448, F_ = 1792, DF_ = 225;
constexpr unsigned MB = 1u << 20;
constexpr unsigned T_OFF = 0;
constexpr unsigned U_OFF = 4 * MB;
constexpr unsigned LN_OFF = 6 * MB, LN_BYTES = 64 * 28 * 1024;
constexpr unsigned HR_OFF = LN_OFF + LN_BYTES;
constexpr unsigned HF_OFF = HR_OFF + LN_BYTES;
constexpr unsigned EP_OFF = 18 * MB;
constexpr unsigned WS_USED = 18 * MB + 256;
constexpr unsigned SENT = 0xffffffffu;
constexpr int SPIN_MAX = 20000;
constexpr unsigned L_BIG = 0, L_SP = 114688;
constexpr unsigned L_A = L_SP, L_BT = L_SP + 8192, L_BP = L_SP + 16384, L_C = L_SP + 24576, L_ST = L_SP + 40960;

#define DLY_T 0
#define DLY_U 0
#define DLY_L 0

#define STAMP(i) {}
#define CNT(i, v)

DI unsigned pkh(float a, float b) { h2 v = {(_Float16)a, (_Float16)b}; return __builtin_bit_cast(unsigned, v); }
DI unsigned pkb(float a, float b) { b2 v = {(__bf16)a, (__bf16)b}; return __builtin_bit_cast(unsigned, v); }
DI float blo(unsigned u) { return __builtin_bit_cast(float, u << 16); }
DI float bhi(unsigned u) { return __builtin_bit_cast(float, u & 0xffff0000u); }
DI float cosr(float x) { return __builtin_amdgcn_cosf(x); }
DI float sinr(float x) { return __builtin_amdgcn_sinf(x); }
DI unsigned hadd(unsigned a, unsigned b) { return __builtin_bit_cast(unsigned, __builtin_bit_cast(h2, a) + __builtin_bit_cast(h2, b)); }
DI unsigned hsub(unsigned a, unsigned b) { return __builtin_bit_cast(unsigned, __builtin_bit_cast(h2, a) - __builtin_bit_cast(h2, b)); }
DI f32x4 mfma16h(v4u a, v4u b, f32x4 c) {
  return __builtin_amdgcn_mfma_f32_16x16x32_f16(__builtin_bit_cast(h8, a), __builtin_bit_cast(h8, b), c, 0, 0, 0);
}
DI f32x4 mfma16b(v4u a, v4u b, f32x4 c) {
  return __builtin_amdgcn_mfma_f32_16x16x32_bf16(__builtin_bit_cast(b8, a), __builtin_bit_cast(b8, b), c, 0, 0, 0);
}
DI f32x16 mfma32b(v4u a, v4u b, f32x16 c) {
  return __builtin_amdgcn_mfma_f32_32x32x16_bf16(__builtin_bit_cast(b8, a), __builtin_bit_cast(b8, b), c, 0, 0, 0);
}
template <int CTRL> DI float dpp_add(float v) {
  return v + __builtin_bit_cast(float, __builtin_amdgcn_update_dpp(0, __builtin_bit_cast(int, v), CTRL, 0xf, 0xf, true));
}
DI float xor16_add(float v) {
  float a = v, b = v;
  asm("s_nop 1\n\tv_permlane16_swap_b32 %0, %1" : "+v"(a), "+v"(b));
  return a + b;
}
DI float xor32_add(float v) {
  float a = v, b = v;
  asm("s_nop 1\n\tv_permlane32_swap_b32 %0, %1" : "+v"(a), "+v"(b));
  return a + b;
}
DI float sum32(float v) {
  v = dpp_add<0xB1>(v); v = dpp_add<0x4E>(v); v = dpp_add<0x141>(v); v = dpp_add<0x140>(v);
  return xor16_add(v);
}
DI rsrc_t mkrs(const void* p, unsigned bytes) { return __builtin_amdgcn_make_buffer_rsrc((void*)p, (short)0, (int)bytes, 0x00020000); }
DI void dma(rsrc_t rs, unsigned voff, unsigned soff, unsigned lds) {
  asm volatile("s_mov_b32 m0, %0\n\ts_nop 0\n\tbuffer_load_dwordx4 %1, %2, %3 offen lds" ::"s"(lds), "v"(voff), "s"(rs), "s"(soff) : "memory");
}
DI void dma1(rsrc_t rs, unsigned voff, unsigned soff, unsigned lds) {
  asm volatile("s_mov_b32 m0, %0\n\ts_nop 0\n\tbuffer_load_dwordx4 %1, %2, %3 offen sc1 lds" ::"s"(lds), "v"(voff), "s"(rs), "s"(soff) : "memory");
}
DI v4u ld16(rsrc_t rs, unsigned voff, unsigned soff) { return __builtin_amdgcn_raw_buffer_load_b128(rs, voff, soff, 16); }
DI v2u ld8(rsrc_t rs, unsigned voff, unsigned soff) { return __builtin_amdgcn_raw_buffer_load_b64(rs, voff, soff, 16); }
DI void st16(v4u v, rsrc_t rs, unsigned voff, unsigned soff) { __builtin_amdgcn_raw_buffer_store_b128(v, rs, voff, soff, 16); }
DI v4u lds16(unsigned a) { return *(LAS const v4u*)(size_t)a; }
DI v2u ldtr(unsigned a) { return __builtin_bit_cast(v2u, __builtin_amdgcn_ds_read_tr16_b64_v4i16((LAS s4v*)(size_t)a)); }
DI unsigned max4(v4u v, unsigned m) { return max(max(max(max(m, v[0]), v[1]), v[2]), v[3]); }
#define BAR() asm volatile("s_waitcnt lgkmcnt(0)\n\ts_barrier" ::: "memory")
#define VMW(n) asm volatile("s_waitcnt vmcnt(" #n ")" ::: "memory")
#define SB() __builtin_amdgcn_sched_barrier(0)
#define MEMB() asm volatile("" ::: "memory")

constexpr float SCALE_B = 1.0f / 16.0f;
constexpr float SCALE_Y = 512.0f / (917504.0f * SCALE_B);

DI unsigned glu2(float g0, float g1, float u0, float u1) {
  const float s0 = __builtin_amdgcn_rcpf(1.0f + __builtin_amdgcn_exp2f(g0 * -1.44269504f));
  const float s1 = __builtin_amdgcn_rcpf(1.0f + __builtin_amdgcn_exp2f(g1 * -1.44269504f));
  return pkb(s0 * u0, s1 * u1);
}

__global__ __launch_bounds__(512, 2) void fused(const uint16_t* __restrict__ x, const uint16_t* __restrict__ wr,
                                                const uint16_t* __restrict__ wi, const uint16_t* __restrict__ wg,
                                                const uint16_t* __restrict__ wu, const uint16_t* __restrict__ wd,
                                                unsigned char* __restrict__ ws, uint16_t* __restrict__ out) {
  __shared__ __attribute__((aligned(16))) unsigned char smem[163840];
  const unsigned L0 = (unsigned)(size_t)smem;
  STAMP(0)
  const int b = blockIdx.x, tid = threadIdx.x, lane = tid & 63;
  const int w = __builtin_amdgcn_readfirstlane(tid >> 6);
  const int r = lane & 15, q4 = lane >> 4;
  const int s2 = (b & 7) + 8 * (b >> 4), q = (b >> 3) & 1;
  const int j1b = b & 15, tileb = b >> 4;
  const int yb = (b >> 3) < 28 ? (b >> 3) : 27, xc = b & 7;
  const int mD = 4 * (xc >> 1) + (yb & 3), nD = 7 * (xc & 1) + (yb >> 2);
  const int mE = 2 * xc + (yb & 1), pE = (yb >> 1) & 1, nE = yb >> 2;
  const rsrc_t rx = mkrs(x, S_ * D_ * 2), rws = mkrs(ws, WS_USED);

  v2u xa[4];
  unsigned ep;
  const int l32 = lane & 31;
  {
    const int lc = l32 < 28 ? l32 : 27;
    const unsigned rowo = (unsigned)(128 * (2 * w + (lane >> 5)) + s2) * 896u;
    const unsigned o1 = rowo + 8 * lc, o2 = rowo + 440 - 8 * lc;
    asm volatile("buffer_load_dwordx2 %0, %5, %7, 0 offen\n\t"
                 "buffer_load_dwordx2 %1, %6, %7, 0 offen\n\t"
                 "buffer_load_dwordx2 %2, %5, %7, 0 offen offset:448\n\t"
                 "buffer_load_dwordx2 %3, %6, %7, 0 offen offset:448\n\t"
                 "buffer_load_dword %4, off, %8, %9 sc1"
                 : "=&v"(xa[0]), "=&v"(xa[1]), "=&v"(xa[2]), "=&v"(xa[3]), "=&v"(ep)
                 : "v"(o1), "v"(o2), "s"(rx), "s"(rws), "s"(EP_OFF)
                 : "memory");
  }
  {
    const v4u sv = {SENT, SENT, SENT, SENT};
    const unsigned co = (unsigned)b * 8192u + (unsigned)tid * 16u;
#pragma unroll
    for (int i = 0; i < 3; ++i) st16(sv, rws, co, U_OFF + i * 2 * MB);
  }
  const int wmat = w & 1, wrange = w >> 1;
  const rsrc_t rw = mkrs(wmat ? wu : wg, D_ * F_ * 2);
  const unsigned wvoff = (unsigned)(((wrange >> 1) ? 3 - q4 : q4) * 3584 + ((r ^ (2 * (q4 + 4 * wmat))) * 16));
  const unsigned wsoff0 = (unsigned)((224 * (wrange & 1) + ((wrange >> 1) ? 220 : 0)) * 3584 + nD * 256);
  const int wstep = (wrange >> 1) ? -4 * 3584 : 4 * 3584;
#pragma unroll
  for (int ks = 0; ks < 14; ++ks) dma(rw, wvoff, wsoff0 + ks * wstep, L0 + L_BIG + (ks * 8 + w) * 1024);
  {
    const v4u sv = {SENT, SENT, SENT, SENT};
    const unsigned co = (unsigned)b * 8192u + (unsigned)tid * 16u;
#pragma unroll
    for (int i = 3; i < 7; ++i) st16(sv, rws, co, U_OFF + i * 2 * MB);
  }
  const int par = w >> 2, w3 = w & 3;
  const int kb = 16 * tileb + 2 * (r & 7) + (r >> 3);
  unsigned short fr[4], fi[4];
  {
    const int kk = kb <= 224 ? kb : 224;
#pragma unroll
    for (int i = 0; i < 4; ++i) {
      const int j2 = 16 * w3 + 4 * q4 + 2 * (i & 1) + 64 * (i >> 1) + par;
      const int o = (j1b + 16 * j2) * DF_ + kk;
      fr[i] = wr[o]; fi[i] = wi[o];
    }
  }
  SB();
  v4u bc[4], bs[4], mre, mim;
  {
    const int k = 2 * (16 * w + 8 * ((r >> 1) & 1) + 2 * (r >> 2) + (r & 1)) + q;
    const float kf = (float)k * (1.0f / 896.0f);
    const float c2 = 2.0f * cosr(2.0f * kf);
    const f32x2 c22 = {c2, c2};
#pragma unroll
    for (int kc = 0; kc < 4; ++kc) {
      const float a0 = (float)(64 * kc + 16 * q4 + 1) * kf, a1 = a0 + 2.0f * kf;
      f32x2 cs[8];
      cs[0] = f32x2{cosr(a0), -sinr(a0)}; cs[1] = f32x2{cosr(a1), -sinr(a1)};
#pragma unroll
      for (int e = 2; e < 8; ++e) cs[e] = __builtin_elementwise_fma(c22, cs[e - 1], -cs[e - 2]);
#pragma unroll
      for (int j = 0; j < 4; ++j) { bc[kc][j] = pkh(cs[2 * j].x, cs[2 * j + 1].x); bs[kc][j] = pkh(cs[2 * j].y, cs[2 * j + 1].y); }
    }
#pragma unroll
    for (int i = 0; i < 4; ++i) {
      const float a = (float)(r * (128 * (4 * q4 + i) + s2)) * (1.0f / 2048.0f);
      const float c = cosr(a), s = sinr(a);
      mre[i] = pkh(c, s); mim[i] = pkh(-s, c);
    }
#pragma unroll
    for (int kc = 0; kc < 4; ++kc) asm volatile("" : "+v"(bc[kc]), "+v"(bs[kc]));
    asm volatile("" : "+v"(mre), "+v"(mim));
  }
  STAMP(1)
  asm volatile("s_waitcnt vmcnt(29)" : "+v"(xa[0]), "+v"(xa[1]), "+v"(xa[2]), "+v"(xa[3]), "+v"(ep)::"memory");
  STAMP(2)
  const unsigned tag = __builtin_amdgcn_readfirstlane(ep) * 1664525u + 1013904223u;
  {
    const unsigned c0 = __builtin_amdgcn_readlane(xa[0].x, 0), c1 = __builtin_amdgcn_readlane(xa[0].x, 32);
    const float cc = 2.0f * blo(lane < 32 ? c0 : c1);
    float s1[4], sb[4], d1[4], d2[4];
    float sum = 0.f, sq = 0.f;
#pragma unroll
    for (int i = 0; i < 4; ++i) {
      const unsigned ua = xa[0][i >> 1], ub = xa[1][1 - (i >> 1)], ue = xa[2][i >> 1], uf = xa[3][1 - (i >> 1)];
      const float a = (i & 1) ? bhi(ua) : blo(ua), e = (i & 1) ? bhi(ue) : blo(ue);
      const float bb = (i & 1) ? blo(ub) : bhi(ub), f = (i & 1) ? blo(uf) : bhi(uf);
      s1[i] = (a + f) - cc; sb[i] = (bb + e) - cc; d1[i] = a - f; d2[i] = e - bb;
      sum += s1[i] + sb[i];
      sq = __builtin_fmaf(s1[i], s1[i], sq); sq = __builtin_fmaf(sb[i], sb[i], sq);
      sq = __builtin_fmaf(d1[i], d1[i], sq); sq = __builtin_fmaf(d2[i], d2[i], sq);
    }
    if (l32 >= 28) { sum = 0.f; sq = 0.f; }
    sum = sum32(sum); sq = sum32(sq);
    const float mu = sum * (1.0f / 448.0f);
    const float var = __builtin_fmaxf(__builtin_fmaf(-mu, mu, sq * (0.5f / 448.0f)), 0.f);
    float rs = __builtin_amdgcn_rsqf(var + 1e-5f);
    if (l32 >= 28) rs = 0.f;
    const float sgn = q ? -1.0f : 1.0f;
    const float off = q ? 0.f : -4.0f * mu * rs;
    float P[4], Q[4];
#pragma unroll
    for (int i = 0; i < 4; ++i) {
      P[i] = __builtin_fmaf(__builtin_fmaf(sgn, sb[i], s1[i]), rs, off);
      Q[i] = __builtin_fmaf(sgn, d2[i], d1[i]) * rs;
    }
    const int c = l32 >> 1, row = 2 * w + (lane >> 5);
    const unsigned o = L0 + L_A + (c * 16 + (row ^ (c & 7))) * 16 + (l32 & 1) * 8;
    *(LAS v2u*)(size_t)o = v2u{pkh(P[0], P[1]), pkh(P[2], P[3])};
    *(LAS v2u*)(size_t)(o + 4096) = v2u{pkh(Q[0], Q[1]), pkh(Q[2], Q[3])};
  }
  VMW(26);
  BAR();
  STAMP(3)
  {
    f32x4 zre = {0, 0, 0, 0}, zim = {0, 0, 0, 0};
#pragma unroll
    for (int kc = 0; kc < 4; ++kc) {
      const int c = 4 * kc + q4;
      const unsigned o = L0 + L_A + (c * 16 + (r ^ (c & 7))) * 16;
      zre = mfma16h(lds16(o), bc[kc], zre);
      zim = mfma16h(lds16(o + 4096), bs[kc], zim);
    }
    v4u zop;
#pragma unroll
    for (int i = 0; i < 4; ++i) zop[i] = pkh(zre[i], zim[i]);
    const f32x4 z4 = {0, 0, 0, 0};
    const f32x4 tre = mfma16h(zop, mre, z4), tim = mfma16h(zop, mim, z4);
    const unsigned to = (unsigned)(r * 16 * 128 * 128 + q4 * 16);
    const unsigned tso = T_OFF + (unsigned)((2 * w * 128 + s2) * 128 + q * 64);
    st16(v4u{pkh(tre[0], tim[0]), tag, pkh(tre[1], tim[1]), tag}, rws, to, tso);
    st16(v4u{pkh(tre[2], tim[2]), tag, pkh(tre[3], tim[3]), tag}, rws, to, tso + 128 * 128);
  }
  STAMP(4)
  v4u wf[28];
  const unsigned trb = L0 + L_BIG + (unsigned)((((lane >> 5) * 4 + ((lane >> 4) & 1)) * 1024) + ((lane >> 2) & 3) * 256 +
                                               (((2 * w + ((lane >> 1) & 1)) ^ (2 * (((lane >> 2) & 3) + 4 * ((lane >> 4) & 1)))) * 16) + (lane & 1) * 8);
  VMW(2);
  BAR();
  {
    unsigned t0 = trb, t1 = trb + 57344;
    asm volatile("" : "+v"(t1));
#pragma unroll
    for (int ks = 0; ks < 14; ++ks) {
      const unsigned a = ks < 7 ? t0 + ks * 8192 : t1 + (ks - 7) * 8192;
      const v2u lo = ldtr(a), hi = ldtr(a + 2048);
      wf[ks] = v4u{lo.x, lo.y, hi.x, hi.y};
    }
#pragma unroll
    for (int ks = 0; ks < 14; ++ks) asm volatile("" : "+v"(wf[ks]));
  }
  BAR();
#pragma unroll
  for (int ks = 0; ks < 14; ++ks) dma(rw, wvoff, wsoff0 + (14 + ks) * wstep, L0 + L_BIG + (ks * 8 + w) * 1024);
  STAMP(5)
  v4u fre[4], fim[4];
  const int arow = 16 * w3 + 4 * (r >> 2) + 2 * (r & 1) + 64 * ((r >> 1) & 1) + par;
  {
    const float af = (float)arow * (1.0f / 128.0f);
#pragma unroll
    for (int g = 0; g < 4; ++g)
#pragma unroll
      for (int i = 0; i < 4; ++i) {
        const float a = (float)(16 * g + 4 * q4 + 2 * (i & 1) + (i >> 1)) * af;
        const float c = cosr(a), s = sinr(a);
        fre[g][i] = pkh(c, s); fim[g][i] = pkh(-s, c);
      }
#pragma unroll
    for (int g = 0; g < 4; ++g) asm volatile("" : "+v"(fre[g]), "+v"(fim[g]));
  }
  if (DLY_T) __builtin_amdgcn_s_sleep(DLY_T);
  STAMP(6)
  {
    const unsigned tso = T_OFF + (unsigned)((j1b * 16 + tileb) * 16384 + w * 1024);
    v4u g0, g1;
    int spins = 0;
    for (;;) {
      MEMB();
      g0 = ld16(rws, lane * 16, tso); g1 = ld16(rws, lane * 16, tso + 8192);
      const bool ok = g0.y == tag && g0.w == tag && g1.y == tag && g1.w == tag;
      if (__all(ok) || ++spins > SPIN_MAX) break;
    }
    CNT(20, spins)
    VMW(0);
    const int m = 8 * w + (lane >> 3);
    const unsigned o = L0 + L_BT + (unsigned)(((m >> 4) * 64 + ((m >> 2) & 3) * 16 + 2 * (lane & 7)) * 16 + (((m >> 1) & 1) + 2 * (m & 1)) * 4);
    *(LAS unsigned*)(size_t)o = hadd(g0.x, g1.x);
    *(LAS unsigned*)(size_t)(o + 16) = hadd(g0.z, g1.z);
    *(LAS unsigned*)(size_t)(o + 4096) = hsub(g0.x, g1.x);
    *(LAS unsigned*)(size_t)(o + 4096 + 16) = hsub(g0.z, g1.z);
    STAMP(7)
    BAR();
    if (b == 0 && tid == 0) __builtin_amdgcn_raw_buffer_store_b32(tag, rws, 0, EP_OFF, 16);
    f32x4 vre = {0, 0, 0, 0}, vim = {0, 0, 0, 0};
#pragma unroll
    for (int g = 0; g < 4; ++g) {
      const v4u tf = lds16(L0 + L_BT + par * 4096 + g * 1024 + lane * 16);
      vre = mfma16h(fre[g], tf, vre); vim = mfma16h(fim[g], tf, vim);
    }
    float sc = SCALE_B * 0.125f;
    if (kb == 0 || kb == 224) sc = 0.5f * SCALE_B * 0.125f;
    if (kb > 224) sc = 0.f;
    float pr[4], pi[4];
#pragma unroll
    for (int i = 0; i < 4; ++i) {
      const float a = blo(fr[i]) * sc, bb = blo(fi[i]) * sc;
      pr[i] = vre[i] * a - vim[i] * bb; pi[i] = -(vre[i] * bb + vim[i] * a);
    }
    const unsigned po = L0 + L_BP + w3 * 1024 + lane * 16 + par * 8;
    *(LAS v2u*)(size_t)po = v2u{pkh(pr[0] + pr[2], pi[0] + pi[2]), pkh(pr[1] + pr[3], pi[1] + pi[3])};
    *(LAS v2u*)(size_t)(po + 4096) = v2u{pkh(pr[0] - pr[2], pi[0] - pi[2]), pkh(pr[1] - pr[3], pi[1] - pi[3])};
    BAR();
    f32x4 ure = {0, 0, 0, 0}, uim = {0, 0, 0, 0};
#pragma unroll
    for (int g = 0; g < 4; ++g) {
      const v4u pf = lds16(L0 + L_BP + par * 4096 + g * 1024 + lane * 16);
      ure = mfma16h(pf, fre[g], ure); uim = mfma16h(pf, fim[g], uim);
    }
    v4u o4;
#pragma unroll
    for (int i = 0; i < 4; ++i) o4[i] = pkh(ure[i] * 0.125f, uim[i] * -0.125f);
    st16(o4, rws, (unsigned)(arow * 16384 + q4 * 16), U_OFF + (unsigned)((tileb * 16 + j1b) * 64));
  }
  STAMP(8)
  {
    unsigned t0 = trb, t1 = trb + 57344;
    asm volatile("" : "+v"(t0), "+v"(t1));
#pragma unroll
    for (int ks = 0; ks < 14; ++ks) {
      const unsigned a = ks < 7 ? t0 + ks * 8192 : t1 + (ks - 7) * 8192;
      const v2u lo = ldtr(a), hi = ldtr(a + 2048);
      wf[14 + ks] = v4u{lo.x, lo.y, hi.x, hi.y};
    }
#pragma unroll
    for (int ks = 0; ks < 14; ++ks) asm volatile("" : "+v"(wf[14 + ks]));
  }
  BAR();
  const rsrc_t rd = mkrs(wd, F_ * D_ * 2);
  const unsigned dvoff = (unsigned)((lane >> 3) * 896 + (((lane & 7) ^ (4 * ((lane >> 4) & 1))) * 16));
  const unsigned dsoff = (unsigned)(224 * w * 896 + nE * 128);
#define WD_DMA(u, ldsaddr)                                                            \
  {                                                                                   \
    dma(rd, dvoff, dsoff + (32 * ((u) >> 1) + 8 * ((u) & 1)) * 896, (ldsaddr));       \
    dma(rd, dvoff, dsoff + (32 * ((u) >> 1) + 8 * ((u) & 1) + 16) * 896, (ldsaddr) + 1024); \
  }
  const int srow = 128 * r + s2;
  const int dt = w < 7 ? w : 6;
  const int d0 = 16 * dt + 4 * q4;
  v2u xv[4];
  {
    const uint16_t* xr = x + (size_t)srow * D_;
    xv[0] = *(const v2u*)(xr + d0); xv[1] = *(const v2u*)(xr + 224 + d0);
    xv[2] = *(const v2u*)(xr + 220 - d0); xv[3] = *(const v2u*)(xr + 444 - d0);
  }
#pragma unroll
  for (int u = 0; u < 7; ++u) WD_DMA(u, L0 + L_BIG + w * 14336 + u * 2048)
  STAMP(9)
  v4u ce[4], co[4], se[4], so[4];
  {
#pragma unroll
    for (int i = 0; i < 4; ++i) {
      const float a = (float)((4 * q4 + i) * srow) * (1.0f / 2048.0f);
      const float c = 0.25f * cosr(a), s = 0.25f * sinr(a);
      mre[i] = pkh(c, -s); mim[i] = pkh(s, c);
    }
    const float df = (float)(2 * (16 * dt + r) + 1) * (1.0f / 896.0f);
    const float c2 = 2.0f * cosr(df);
    const f32x2 c22 = {c2, c2};
#pragma unroll
    for (int kc = 0; kc < 4; ++kc) {
      const float a0 = (float)(64 * kc + 16 * q4) * df, a1 = a0 + df;
      f32x2 cs[16];
      cs[0] = f32x2{cosr(a0), sinr(a0)}; cs[1] = f32x2{cosr(a1), sinr(a1)};
#pragma unroll
      for (int e = 2; e < 16; ++e) cs[e] = __builtin_elementwise_fma(c22, cs[e - 1], -cs[e - 2]);
#pragma unroll
      for (int j = 0; j < 4; ++j) {
        ce[kc][j] = pkh(cs[4 * j].x, cs[4 * j + 2].x); co[kc][j] = pkh(cs[4 * j + 1].x, cs[4 * j + 3].x);
        se[kc][j] = pkh(cs[4 * j].y, cs[4 * j + 2].y); so[kc][j] = pkh(cs[4 * j + 1].y, cs[4 * j + 3].y);
      }
      asm volatile("" : "+v"(ce[kc]), "+v"(co[kc]), "+v"(se[kc]), "+v"(so[kc]));
    }
    asm volatile("" : "+v"(mre), "+v"(mim));
  }
  if (DLY_U) __builtin_amdgcn_s_sleep(DLY_U);
  STAMP(10)
  {
    v4u uf[2];
    const unsigned uso = U_OFF + (unsigned)((s2 * 16 + w) * 1024);
    int spins = 0;
    for (;;) {
      MEMB();
#pragma unroll
      for (int u = 0; u < 2; ++u)
#pragma unroll
        for (int i = 0; i < 4; ++i) uf[u][i] = __builtin_amdgcn_raw_buffer_load_b32(rws, q4 * 256 + r * 4 + i * 64, uso + u * 8192, 16);
      const unsigned mx = max4(uf[1], max4(uf[0], 0));
      if (__all(mx != SENT) || ++spins > SPIN_MAX) break;
    }
    CNT(21, spins)
    STAMP(11)
    const f32x4 z4 = {0, 0, 0, 0};
#pragma unroll
    for (int u = 0; u < 2; ++u) {
      const f32x4 yre = mfma16h(uf[u], mre, z4), yim = mfma16h(uf[u], mim, z4);
      const int c = w + 8 * u;
      const unsigned o = L0 + L_C + (q4 >> 1) * 8192 + (c * 16 + (r ^ (c & 7))) * 16 + (q4 & 1) * 8;
      *(LAS v2u*)(size_t)o = v2u{pkh(yre[0], yre[1]), pkh(yre[2], yre[3])};
      *(LAS v2u*)(size_t)(o + 4096) = v2u{pkh(yim[0], yim[1]), pkh(yim[2], yim[3])};
    }
    BAR();
    f32x4 Ce = z4, Co = z4, Se = z4, So = z4;
#pragma unroll
    for (int kc = 0; kc < 4; ++kc) {
      const int c = 4 * kc + q4;
      const unsigned o = L0 + L_C + (c * 16 + (r ^ (c & 7))) * 16;
      Ce = mfma16h(ce[kc], lds16(o), Ce);
      Se = mfma16h(se[kc], lds16(o + 4096), Se);
      Co = mfma16h(co[kc], lds16(o + 8192), Co);
      So = mfma16h(so[kc], lds16(o + 12288), So);
    }
    float hv[4][4];
    float sum = 0.f;
#pragma unroll
    for (int i = 0; i < 4; ++i) {
      const float a = Ce[i] - Se[i], bb = Co[i] - So[i], c = Ce[i] + Se[i], d = Co[i] + So[i];
      const unsigned xa_ = i < 2 ? xv[0].x : xv[0].y, xb = i < 2 ? xv[1].x : xv[1].y;
      const unsigned xc_ = i < 2 ? xv[2].y : xv[2].x, xd = i < 2 ? xv[3].y : xv[3].x;
      const float x0 = (i & 1) ? bhi(xa_) : blo(xa_), x1 = (i & 1) ? bhi(xb) : blo(xb);
      const float x2 = (i & 1) ? blo(xc_) : bhi(xc_), x3 = (i & 1) ? blo(xd) : bhi(xd);
      const unsigned h01 = pkb(__builtin_fmaf(a + bb, SCALE_Y, x0), __builtin_fmaf(a - bb, SCALE_Y, x1));
      const unsigned h23 = pkb(__builtin_fmaf(c - d, SCALE_Y, x2), __builtin_fmaf(c + d, SCALE_Y, x3));
      hv[0][i] = blo(h01); hv[1][i] = bhi(h01); hv[2][i] = blo(h23); hv[3][i] = bhi(h23);
      sum += (hv[0][i] + hv[1][i]) + (hv[2][i] + hv[3][i]);
    }
    sum = xor32_add(xor16_add(sum));
    const float lm = sum * (1.0f / 64.0f);
    float m2 = 0.f;
#pragma unroll
    for (int g = 0; g < 4; ++g)
#pragma unroll
      for (int i = 0; i < 4; ++i) { const float dlt = hv[g][i] - lm; m2 = __builtin_fmaf(dlt, dlt, m2); }
    m2 = xor32_add(xor16_add(m2));
    if (q4 == 0) *(LAS f32x2*)(size_t)(L0 + L_ST + (r * 8 + w) * 8) = f32x2{lm, m2};
    const unsigned lo = (unsigned)((q4 * 64 + r) * 16);
    const unsigned lso = (unsigned)((((s2 >> 1) * 28 + 4 * dt) * 64 + 16 * (s2 & 1)) * 16);
    if (q == 1 && w < 7) {
      st16(v4u{pkb(hv[0][0], hv[0][1]), pkb(hv[0][2], hv[0][3]), pkb(hv[1][0], hv[1][1]), pkb(hv[1][2], hv[1][3])}, rws, lo, HR_OFF + lso);
      st16(v4u{pkb(hv[2][3], hv[2][2]), pkb(hv[2][1], hv[2][0]), pkb(hv[3][3], hv[3][2]), pkb(hv[3][1], hv[3][0])}, rws, lo + 512, HR_OFF + lso);
    }
    BAR();
    if (q == 0 && w < 7) {
      f32x4 sv[4];
#pragma unroll
      for (int j = 0; j < 4; ++j) sv[j] = *(LAS const f32x4*)(size_t)(L0 + L_ST + r * 64 + j * 16);
      const float mean = (((sv[0].x + sv[0].z) + (sv[1].x + sv[1].z)) + ((sv[2].x + sv[2].z) + sv[3].x)) * (1.0f / 7.0f);
      float M2 = 0.f;
#pragma unroll
      for (int j = 0; j < 7; ++j) {
        const float dlt = sv[j >> 1][2 * (j & 1)] - mean;
        M2 += __builtin_fmaf(64.0f * dlt, dlt, sv[j >> 1][2 * (j & 1) + 1]);
      }
      const float rs = __builtin_amdgcn_rsqf(M2 * (1.0f / 448.0f) + 1e-5f);
      const float off = -mean * rs;
      unsigned z[4][2];
#pragma unroll
      for (int g = 0; g < 4; ++g)
#pragma unroll
        for (int j = 0; j < 2; ++j) z[g][j] = pkb(__builtin_fmaf(hv[g][2 * j], rs, off), __builtin_fmaf(hv[g][2 * j + 1], rs, off));
      st16(v4u{z[0][0], z[0][1], z[1][0], z[1][1]}, rws, lo, LN_OFF + lso);
      st16(v4u{z[2][0], z[2][1], z[3][0], z[3][1]}, rws, lo + 512, LN_OFF + lso);
    }
  }
  STAMP(12)
  if ((b >> 3) < 28) {
    v4u wdf[7][4];
    const unsigned tdb = (unsigned)((q4 >> 1) * 1024 + (4 * (q4 & 1) + ((lane >> 2) & 3)) * 128 + ((lane >> 3) & 1) * 64 + (lane & 3) * 16);
#define WD_READ(u, base0, base1, off)                                                                  \
  _Pragma("unroll") for (int nt = 0; nt < 4; ++nt) {                                                   \
    const v2u t_ = ldtr(((nt >> 1) ? (base1) : (base0)) + (off) + (nt & 1) * 8);                       \
    wdf[(u) >> 1][nt][2 * ((u) & 1)] = t_.x; wdf[(u) >> 1][nt][2 * ((u) & 1) + 1] = t_.y;              \
  }
    {
      const unsigned a0 = L0 + L_BIG + w * 14336 + tdb, a1 = L0 + L_BIG + w * 14336 + (tdb ^ 64);
#pragma unroll
      for (int u = 0; u < 7; ++u) WD_READ(u, a0, a1, u * 2048)
#pragma unroll
      for (int i = 0; i < 3; ++i) asm volatile("" : "+v"(wdf[i][0]), "+v"(wdf[i][1]), "+v"(wdf[i][2]), "+v"(wdf[i][3]));
    }
    BAR();
    if (DLY_L) __builtin_amdgcn_s_sleep(DLY_L);
    STAMP(13)
    const unsigned lnso = LN_OFF + (unsigned)(mD * 114688 + w * 1024);
    const unsigned la = L0 + L_BIG + lane * 16;
#pragma unroll
    for (int i = 0; i < 14; ++i) dma1(rws, lane * 16, lnso + i * 8192, L0 + L_BIG + w * 1024 + i * 8192);
#pragma unroll
    for (int u = 7; u < 10; ++u) WD_DMA(u, L0 + L_SP + w * 6144 + (u - 7) * 2048)
    VMW(16);
    {
      int spins = 0;
      for (;;) {
        unsigned mx = 0;
#pragma unroll
        for (int i = 0; i < 4; ++i) mx = max4(lds16(la + w * 1024 + i * 8192), mx);
        if (__all(mx != SENT) || ++spins > SPIN_MAX) break;
#pragma unroll
        for (int i = 0; i < 4; ++i) dma1(rws, lane * 16, lnso + i * 8192, L0 + L_BIG + w * 1024 + i * 8192);
        VMW(0);
      }
      CNT(22, spins)
    }
    BAR();
    STAMP(14)
    f32x16 acc, accp;
    v4u af[4], ho;
    v4u hb[4][7];
    const unsigned hso = HF_OFF + (unsigned)(4 * mD * 114688 + (8 * nD + w) * 1024);
    const unsigned hvo = (unsigned)(q4 * 512 + pE * 256 + r * 16);
    const unsigned hlo = HF_OFF + (unsigned)(4 * mE * 114688 + 14 * w * 1024);
    unsigned lb[4];
#pragma unroll
    for (int tt = 0; tt < 4; ++tt) lb[tt] = la + tt * 28672;
#define H_LOAD(tt, i) hb[tt][i] = ld16(rws, hvo, hlo + (tt) * 114688 + (i) * 2048);
#pragma unroll
    for (int tt = 0; tt < 4; ++tt) {
      unsigned lbt = lb[tt];
      asm volatile("" : "+v"(lbt));
#pragma unroll
      for (int i = 0; i < 3; ++i) af[i] = lds16(lbt + i * 1024);
#pragma unroll
      for (int i = 0; i < 16; ++i) acc[i] = 0.f;
      SB();
#pragma unroll
      for (int ks = 0; ks < 28; ++ks) {
        if (ks + 3 < 28) af[(ks + 3) & 3] = lds16(lbt + (ks + 3) * 1024);
        acc = mfma32b(wf[ks], af[ks & 3], acc);
        if (tt > 0) {
          if (ks >= 2 && ks < 18 && (ks & 3) == 2) {
            const int j = (ks - 2) >> 2;
            ho[j] = glu2(accp[2 * j], accp[2 * j + 1], accp[8 + 2 * j], accp[8 + 2 * j + 1]);
          }
          if (ks == 16) st16(ho, rws, lane * 16, hso + (tt - 1) * 114688);
        }
        if (tt == 3) {
          if (ks < 7) H_LOAD(0, ks)
          if (ks == 7) VMW(7);
          if (ks >= 8 && ks < 22 && (ks & 1) == 0) {
            const int u = 7 + ((ks - 8) >> 1);
            const unsigned ub = u < 10 ? L_SP + (u - 7) * 2048 : u == 10 ? 0 : u < 13 ? 16384 + (u - 11) * 2048 : 57344;
            const unsigned wb = u < 10 ? w * 6144 : u == 10 ? w * 2048 : u < 13 ? w * 4096 : w * 2048;
            const unsigned a0 = L0 + ub + wb + tdb, a1 = L0 + ub + wb + (tdb ^ 64);
            WD_READ(u, a0, a1, 0)
          }
          if (ks >= 21) H_LOAD(1, ks - 21)
        }
        SB();
      }
      accp = acc;
      if (tt == 0) {
        VMW(0);
        int spins = 0;
        for (;;) {
          unsigned mx = 0;
#pragma unroll
          for (int i = 4; i < 14; ++i) mx = max4(lds16((i < 7 ? la : la + 57344) + w * 1024 + (i < 7 ? i : i - 7) * 8192), mx);
          if (__all(mx != SENT) || ++spins > SPIN_MAX) break;
#pragma unroll
          for (int i = 4; i < 14; ++i) dma1(rws, lane * 16, lnso + i * 8192, L0 + L_BIG + w * 1024 + i * 8192);
          VMW(0);
        }
      }
      if (tt < 3) BAR();
      if (tt == 0) WD_DMA(10, L0 + w * 2048)
      if (tt == 1) { WD_DMA(11, L0 + 16384 + w * 4096) WD_DMA(12, L0 + 16384 + w * 4096 + 2048) }
      if (tt == 2) WD_DMA(13, L0 + 57344 + w * 2048)
      if (tt == 0) STAMP(15)
    }
    STAMP(16)
#pragma unroll
    for (int j = 0; j < 4; ++j) ho[j] = glu2(accp[2 * j], accp[2 * j + 1], accp[8 + 2 * j], accp[8 + 2 * j + 1]);
    st16(ho, rws, lane * 16, hso + 3 * 114688);
#pragma unroll
    for (int i = 0; i < 7; ++i) H_LOAD(2, i)
    const int ttf = w >> 1, c0 = 64 * nE + 32 * (w & 1) + 8 * q4;
    v2u hr0, hr1;
    unsigned hro0, hro1;
    const unsigned hrs = HR_OFF + (unsigned)((4 * mE + ttf) * 28672 + pE * 256);
    {
      const int seg = c0 / 112, hi = seg >> 1, mir = seg & 1, cc = c0 - 224 * hi;
      const int ksa = mir ? (220 - cc) >> 2 : cc >> 2;
      hro0 = (unsigned)((ksa * 64 + 32 * mir + r) * 16 + 8 * hi);
      hro1 = mir ? hro0 - 1024 : hro0 + 1024;
      hr0 = ld8(rws, hro0, hrs); hr1 = ld8(rws, hro1, hrs);
    }
    SB();
    STAMP(17)
    int espins = 0;
#pragma unroll
    for (int tt = 0; tt < 4; ++tt) {
      {
        unsigned mx = 0;
#pragma unroll
        for (int i = 0; i < 7; ++i) mx = max4(hb[tt][i], mx);
        if (__any(mx == SENT)) {
          int spins = 0;
          do {
            MEMB();
#pragma unroll
            for (int i = 0; i < 7; ++i) H_LOAD(tt, i)
            mx = 0;
#pragma unroll
            for (int i = 0; i < 7; ++i) mx = max4(hb[tt][i], mx);
            espins += 1 << (8 * tt);
          } while (__any(mx == SENT) && ++spins < SPIN_MAX);
        }
      }
      f32x4 ea[4];
#pragma unroll
      for (int nt = 0; nt < 4; ++nt) ea[nt] = f32x4{0, 0, 0, 0};
#pragma unroll
      for (int i = 0; i < 7; ++i)
#pragma unroll
        for (int nt = 0; nt < 4; ++nt) ea[nt] = mfma16b(wdf[i][nt], hb[tt][i], ea[nt]);
      SB();
      if (tt == 1) {
#pragma unroll
        for (int i = 0; i < 7; ++i) H_LOAD(3, i)
      }
      if (tt == 0) BAR();
#pragma unroll
      for (int nt = 0; nt < 4; ++nt) *(LAS f32x4*)(size_t)(la + w * 16384 + (tt * 4 + nt) * 1024) = ea[nt];
      SB();
    }
    CNT(23, espins)
    STAMP(18)
    if (__any(max(max(hr0.x, hr0.y), max(hr1.x, hr1.y)) == SENT)) {
      int spins = 0;
      do {
        MEMB();
        hr0 = ld8(rws, hro0, hrs); hr1 = ld8(rws, hro1, hrs);
      } while (__any(max(max(hr0.x, hr0.y), max(hr1.x, hr1.y)) == SENT) && ++spins < SPIN_MAX);
    }
    BAR();
    {
      f32x4 s0 = {0, 0, 0, 0}, s1 = {0, 0, 0, 0};
      unsigned ra = la + w * 2048, rb = ra + 65536;
      asm volatile("" : "+v"(rb));
#pragma unroll
      for (int ww = 0; ww < 8; ++ww) {
        const unsigned a = (ww < 4 ? ra : rb) + (ww & 3) * 16384;
        s0 += *(LAS const f32x4*)(size_t)a;
        s1 += *(LAS const f32x4*)(size_t)(a + 1024);
      }
      const unsigned f0 = pkb(s0[0], s0[1]), f1 = pkb(s0[2], s0[3]), f2 = pkb(s1[0], s1[1]), f3 = pkb(s1[2], s1[3]);
      v4u o;
      o[0] = pkb(blo(hr0.x) + blo(f0), bhi(hr0.x) + bhi(f0));
      o[1] = pkb(blo(hr0.y) + blo(f1), bhi(hr0.y) + bhi(f1));
      o[2] = pkb(blo(hr1.x) + blo(f2), bhi(hr1.x) + bhi(f2));
      o[3] = pkb(blo(hr1.y) + blo(f3), bhi(hr1.y) + bhi(f3));
      *(v4u*)(out + (size_t)(128 * r + 8 * mE + 2 * ttf + pE) * D_ + c0) = o;
    }
  }
}

extern "C" void kernel_launch(void* const* d_in, const int* in_sizes, int n_in, void* d_out, int out_size, void* d_ws,
                              size_t ws_size, hipStream_t stream) {
  fused<<<256, 512, 0, stream>>>((const uint16_t*)d_in[0], (const uint16_t*)d_in[1], (const uint16_t*)d_in[2],
                                 (const uint16_t*)d_in[4], (const uint16_t*)d_in[5], (const uint16_t*)d_in[6],
                                 (unsigned char*)d_ws, (uint16_t*)d_out);
}
```

```cpp
#include <hip/hip_runtime.h>
#include <hip/hip_bf16.h>
#include <stdint.h>

typedef unsigned v4u __attribute__((ext_vector_type(4)));
typedef unsigned v2u __attribute__((ext_vector_type(2)));
typedef float f32x2 __attribute__((ext_vector_type(2)));
typedef float f32x4 __attribute__((ext_vector_type(4)));
typedef float f32x16 __attribute__((ext_vector_type(16)));
typedef _Float16 h8 __attribute__((ext_vector_type(8)));
typedef _Float16 h2 __attribute__((ext_vector_type(2)));
typedef __bf16 b8 __attribute__((ext_vector_type(8)));
typedef __bf16 b2 __attribute__((ext_vector_type(2)));
typedef short s4v __attribute__((ext_vector_type(4)));
typedef __amdgpu_buffer_rsrc_t rsrc_t;
#define DI __device__ __forceinline__
template <int N> struct IC { static constexpr int value = N; };
#define LAS __attribute__((address_space(3)))

constexpr int S_ = 2048, D_ = 448, F_ = 1792, DF_ = 225;
constexpr unsigned MB = 1u << 20;
constexpr unsigned T_OFF = 0;
constexpr unsigned U_OFF = 4 * MB;
constexpr unsigned LN_OFF = 8 * MB;
constexpr unsigned HR_OFF = 10 * MB;
constexpr unsigned HF_OFF = 12 * MB;
constexpr unsigned REG_BYTES = 20 * MB;
#define NROT 8
constexpr unsigned EP_OFF = NROT * REG_BYTES;
constexpr unsigned SENT = 0xffffffffu;
constexpr int SPIN_MAX = 20000;
constexpr unsigned L_BIG = 0, L_SP = 114688;
constexpr unsigned L_A = L_SP, L_BT = L_SP + 8192, L_BP = L_SP + 16384, L_C = L_SP + 24576, L_ST = L_SP + 40960;

#define DLY_T 0
#define DLY_U 0
#define DLY_L 0

#define STAMP_TID 0
#define STAMP(i) {}
#define CNT(i, v)

DI unsigned pkh(float a, float b) { h2 v = {(_Float16)a, (_Float16)b}; return __builtin_bit_cast(unsigned, v); }
DI unsigned pkb(float a, float b) { b2 v = {(__bf16)a, (__bf16)b}; return __builtin_bit_cast(unsigned, v); }
DI float blo(unsigned u) { return __builtin_bit_cast(float, u << 16); }
DI float bhi(unsigned u) { return __builtin_bit_cast(float, u & 0xffff0000u); }
DI float cosr(float x) { return __builtin_amdgcn_cosf(x); }
DI float sinr(float x) { return __builtin_amdgcn_sinf(x); }
DI unsigned hadd(unsigned a, unsigned b) { return __builtin_bit_cast(unsigned, __builtin_bit_cast(h2, a) + __builtin_bit_cast(h2, b)); }
DI unsigned hsub(unsigned a, unsigned b) { return __builtin_bit_cast(unsigned, __builtin_bit_cast(h2, a) - __builtin_bit_cast(h2, b)); }
DI f32x4 mfma16h(v4u a, v4u b, f32x4 c) {
  return __builtin_amdgcn_mfma_f32_16x16x32_f16(__builtin_bit_cast(h8, a), __builtin_bit_cast(h8, b), c, 0, 0, 0);
}
DI f32x4 mfma16b(v4u a, v4u b, f32x4 c) {
  return __builtin_amdgcn_mfma_f32_16x16x32_bf16(__builtin_bit_cast(b8, a), __builtin_bit_cast(b8, b), c, 0, 0, 0);
}
DI f32x16 mfma32b(v4u a, v4u b, f32x16 c) {
  return __builtin_amdgcn_mfma_f32_32x32x16_bf16(__builtin_bit_cast(b8, a), __builtin_bit_cast(b8, b), c, 0, 0, 0);
}
template <int CTRL> DI float dpp_add(float v) {
  return v + __builtin_bit_cast(float, __builtin_amdgcn_update_dpp(0, __builtin_bit_cast(int, v), CTRL, 0xf, 0xf, true));
}
DI float xor16_add(float v) {
  float a = v, b = v;
  asm("s_nop 1\n\tv_permlane16_swap_b32 %0, %1" : "+v"(a), "+v"(b));
  return a + b;
}
DI float xor32_add(float v) {
  float a = v, b = v;
  asm("s_nop 1\n\tv_permlane32_swap_b32 %0, %1" : "+v"(a), "+v"(b));
  return a + b;
}
DI float sum32(float v) {
  v = dpp_add<0xB1>(v); v = dpp_add<0x4E>(v); v = dpp_add<0x141>(v); v = dpp_add<0x140>(v);
  return xor16_add(v);
}
DI rsrc_t mkrs(const void* p, unsigned bytes) { return __builtin_amdgcn_make_buffer_rsrc((void*)p, (short)0, (int)bytes, 0x00020000); }
DI void dma(rsrc_t rs, unsigned voff, unsigned soff, unsigned lds) {
  asm volatile("s_mov_b32 m0, %0\n\ts_nop 0\n\tbuffer_load_dwordx4 %1, %2, %3 offen lds" ::"s"(lds), "v"(voff), "s"(rs), "s"(soff) : "memory");
}
DI void dma1(rsrc_t rs, unsigned voff, unsigned soff, unsigned lds) {
  asm volatile("s_mov_b32 m0, %0\n\ts_nop 0\n\tbuffer_load_dwordx4 %1, %2, %3 offen sc1 lds" ::"s"(lds), "v"(voff), "s"(rs), "s"(soff) : "memory");
}
DI v4u ld16(rsrc_t rs, unsigned voff, unsigned soff) { return __builtin_amdgcn_raw_buffer_load_b128(rs, voff, soff, 16); }
DI v2u ld8(rsrc_t rs, unsigned voff, unsigned soff) { return __builtin_amdgcn_raw_buffer_load_b64(rs, voff, soff, 16); }
DI void st16(v4u v, rsrc_t rs, unsigned voff, unsigned soff) { __builtin_amdgcn_raw_buffer_store_b128(v, rs, voff, soff, 16); }
DI v4u lds16(unsigned a) { return *(LAS const v4u*)(size_t)a; }
DI v2u ldtr(unsigned a) { return __builtin_bit_cast(v2u, __builtin_amdgcn_ds_read_tr16_b64_v4i16((LAS s4v*)(size_t)a)); }
DI unsigned max4(v4u v, unsigned m) { return max(max(max(max(m, v[0]), v[1]), v[2]), v[3]); }
#define BAR() asm volatile("s_waitcnt lgkmcnt(0)\n\ts_barrier" ::: "memory")
#define VMW(n) asm volatile("s_waitcnt vmcnt(" #n ")" ::: "memory")
#define SB() __builtin_amdgcn_sched_barrier(0)
#define MEMB() asm volatile("" ::: "memory")

constexpr float SCALE_B = 1.0f / 16.0f;
constexpr float SCALE_Y = 512.0f / (917504.0f * SCALE_B);

DI unsigned glu2(float g0, float g1, float u0, float u1) {
  const float s0 = __builtin_amdgcn_rcpf(1.0f + __builtin_amdgcn_exp2f(g0 * -1.44269504f));
  const float s1 = __builtin_amdgcn_rcpf(1.0f + __builtin_amdgcn_exp2f(g1 * -1.44269504f));
  return pkb(s0 * u0, s1 * u1);
}

__global__ __launch_bounds__(512, 2) void fused(const uint16_t* __restrict__ x, const uint16_t* __restrict__ wr,
                                                const uint16_t* __restrict__ wi, const uint16_t* __restrict__ wg,
                                                const uint16_t* __restrict__ wu, const uint16_t* __restrict__ wd,
                                                unsigned char* __restrict__ ws, uint16_t* __restrict__ out) {
  __shared__ __attribute__((aligned(16))) unsigned char smem[163840];
  const unsigned L0 = (unsigned)(size_t)smem;
  STAMP(0)
  const int b = blockIdx.x, tid = threadIdx.x, lane = tid & 63;
  const int w = __builtin_amdgcn_readfirstlane(tid >> 6);
  const int r = lane & 15, q4 = lane >> 4;
  const int s2 = (b & 7) + 8 * (b >> 4), q = (b >> 3) & 1;
  const int j1b = b & 15, tileb = b >> 4;
  const int yb = (b >> 3) < 28 ? (b >> 3) : 27;
  const int mG = 2 * (b & 7) + (yb & 1), nD = yb >> 1, pE = nD & 1, nE = nD >> 1;
  const rsrc_t rx = mkrs(x, S_ * D_ * 2), rep_ = mkrs(ws + EP_OFF, 64);

  v2u xa[4];
  unsigned ep;
  const int l32 = lane & 31;
  {
    const int lc = l32 < 28 ? l32 : 27;
    const unsigned rowo = (unsigned)(128 * (2 * w + (lane >> 5)) + s2) * 896u;
    const unsigned o1 = rowo + 8 * lc, o2 = rowo + 440 - 8 * lc;
    asm volatile("buffer_load_dword %4, off, %8, 0 sc1\n\t"
                 "buffer_load_dwordx2 %0, %5, %7, 0 offen\n\t"
                 "buffer_load_dwordx2 %1, %6, %7, 0 offen\n\t"
                 "buffer_load_dwordx2 %2, %5, %7, 0 offen offset:448\n\t"
                 "buffer_load_dwordx2 %3, %6, %7, 0 offen offset:448"
                 : "=&v"(xa[0]), "=&v"(xa[1]), "=&v"(xa[2]), "=&v"(xa[3]), "=&v"(ep)
                 : "v"(o1), "v"(o2), "s"(rx), "s"(rep_)
                 : "memory");
  }
  const int wmat = w & 1, wrange = w >> 1;
  const rsrc_t rw = mkrs(wmat ? wu : wg, D_ * F_ * 2);
  const unsigned wvoff = (unsigned)(((wrange >> 1) ? 3 - q4 : q4) * 3584 + ((r ^ (2 * (q4 + 4 * wmat))) * 16));
  const unsigned wsoff0 = (unsigned)((224 * (wrange & 1) + ((wrange >> 1) ? 220 : 0)) * 3584 + nD * 256);
  const int wstep = (wrange >> 1) ? -4 * 3584 : 4 * 3584;
  const int rot = (7 * mG) >> 2;
#define KROT(j) ((j) + rot >= 28 ? (j) + rot - 28 : (j) + rot)
#define W_DMA(n0, n1) \
  _Pragma("unroll") for (int n_ = (n0); n_ < (n1); ++n_) dma(rw, wvoff, wsoff0 + KROT(n_) * wstep, L0 + L_BIG + ((n_ % 14) * 8 + w) * 1024);
  W_DMA(0, 4)
  const int par = w >> 2, w3 = w & 3;
  const int kb = 16 * tileb + 2 * (r & 7) + (r >> 3);
  unsigned short fr[4], fi[4];
  {
    const int kk = kb <= 224 ? kb : 224;
    const rsrc_t rr = mkrs(wr, S_ * DF_ * 2), ri = mkrs(wi, S_ * DF_ * 2);
    const unsigned fo = (unsigned)(((j1b + 16 * (16 * w3 + 4 * q4 + par)) * DF_ + kk) * 2);
#pragma unroll
    for (int i = 0; i < 4; ++i) {
      const unsigned so = (2 * (i & 1) + 64 * (i >> 1)) * 16 * DF_ * 2;
      fr[i] = __builtin_amdgcn_raw_buffer_load_b16(rr, fo, so, 0);
      fi[i] = __builtin_amdgcn_raw_buffer_load_b16(ri, fo, so, 0);
    }
  }
  MEMB();
  SB();
  v4u bc[4], bs[4], mre, mim;
  {
    const int k = 2 * (16 * w + 8 * ((r >> 1) & 1) + 2 * (r >> 2) + (r & 1)) + q;
    const float kf = (float)k * (1.0f / 896.0f);
    const float c2 = 2.0f * cosr(2.0f * kf);
    const f32x2 c22 = {c2, c2};
#pragma unroll
    for (int kc = 0; kc < 4; ++kc) {
      const float a0 = (float)(64 * kc + 16 * q4 + 1) * kf, a1 = a0 + 2.0f * kf;
      f32x2 cs[8];
      cs[0] = f32x2{cosr(a0), -sinr(a0)}; cs[1] = f32x2{cosr(a1), -sinr(a1)};
#pragma unroll
      for (int e = 2; e < 8; ++e) cs[e] = __builtin_elementwise_fma(c22, cs[e - 1], -cs[e - 2]);
#pragma unroll
      for (int j = 0; j < 4; ++j) { bc[kc][j] = pkh(cs[2 * j].x, cs[2 * j + 1].x); bs[kc][j] = pkh(cs[2 * j].y, cs[2 * j + 1].y); }
    }
#pragma unroll
    for (int i = 0; i < 4; ++i) {
      const float a = (float)(r * (128 * (4 * q4 + i) + s2)) * (1.0f / 2048.0f);
      const float c = cosr(a), s = sinr(a);
      mre[i] = pkh(c, s); mim[i] = pkh(-s, c);
    }
#pragma unroll
    for (int kc = 0; kc < 4; ++kc) asm volatile("" : "+v"(bc[kc]), "+v"(bs[kc]));
    asm volatile("" : "+v"(mre), "+v"(mim));
  }
  W_DMA(4, 8)
  STAMP(1)
  asm volatile("s_waitcnt vmcnt(16)" : "+v"(xa[0]), "+v"(xa[1]), "+v"(xa[2]), "+v"(xa[3]), "+v"(ep)::"memory");
  STAMP(2)
  const unsigned tag = __builtin_amdgcn_readfirstlane(ep) * 1664525u + 1013904223u;
  const rsrc_t rws = mkrs(ws + (size_t)(tag & (NROT - 1)) * REG_BYTES, REG_BYTES);
  const v4u sent4 = {SENT, SENT, SENT, SENT};
  st16(sent4, rws, tid * 16, LN_OFF + b * 8192);
  st16(sent4, rws, tid * 16, HR_OFF + b * 8192);
  {
    const unsigned c0 = __builtin_amdgcn_readlane(xa[0].x, 0), c1 = __builtin_amdgcn_readlane(xa[0].x, 32);
    const float cc = 2.0f * blo(lane < 32 ? c0 : c1);
    float s1[4], sb[4], d1[4], d2[4];
    float sum = 0.f, sq = 0.f;
#pragma unroll
    for (int i = 0; i < 4; ++i) {
      const unsigned ua = xa[0][i >> 1], ub = xa[1][1 - (i >> 1)], ue = xa[2][i >> 1], uf = xa[3][1 - (i >> 1)];
      const float a = (i & 1) ? bhi(ua) : blo(ua), e = (i & 1) ? bhi(ue) : blo(ue);
      const float bb = (i & 1) ? blo(ub) : bhi(ub), f = (i & 1) ? blo(uf) : bhi(uf);
      s1[i] = (a + f) - cc; sb[i] = (bb + e) - cc; d1[i] = a - f; d2[i] = e - bb;
      sum += s1[i] + sb[i];
      sq = __builtin_fmaf(s1[i], s1[i], sq); sq = __builtin_fmaf(sb[i], sb[i], sq);
      sq = __builtin_fmaf(d1[i], d1[i], sq); sq = __builtin_fmaf(d2[i], d2[i], sq);
    }
    if (l32 >= 28) { sum = 0.f; sq = 0.f; }
    sum = sum32(sum); sq = sum32(sq);
    const float mu = sum * (1.0f / 448.0f);
    const float var = __builtin_fmaxf(__builtin_fmaf(-mu, mu, sq * (0.5f / 448.0f)), 0.f);
    float rs = __builtin_amdgcn_rsqf(var + 1e-5f);
    if (l32 >= 28) rs = 0.f;
    const float sgn = q ? -1.0f : 1.0f;
    const float off = q ? 0.f : -4.0f * mu * rs;
    float P[4], Q[4];
#pragma unroll
    for (int i = 0; i < 4; ++i) {
      P[i] = __builtin_fmaf(__builtin_fmaf(sgn, sb[i], s1[i]), rs, off);
      Q[i] = __builtin_fmaf(sgn, d2[i], d1[i]) * rs;
    }
    const int c = l32 >> 1, row = 2 * w + (lane >> 5);
    const unsigned o = L0 + L_A + (c * 16 + (row ^ (c & 7))) * 16 + (l32 & 1) * 8;
    *(LAS v2u*)(size_t)o = v2u{pkh(P[0], P[1]), pkh(P[2], P[3])};
    *(LAS v2u*)(size_t)(o + 4096) = v2u{pkh(Q[0], Q[1]), pkh(Q[2], Q[3])};
  }
  W_DMA(8, 12)
  BAR();
  STAMP(3)
  {
    f32x4 zre = {0, 0, 0, 0}, zim = {0, 0, 0, 0};
#pragma unroll
    for (int kc = 0; kc < 4; ++kc) {
      const int c = 4 * kc + q4;
      const unsigned o = L0 + L_A + (c * 16 + (r ^ (c & 7))) * 16;
      zre = mfma16h(lds16(o), bc[kc], zre);
      zim = mfma16h(lds16(o + 4096), bs[kc], zim);
    }
    v4u zop;
#pragma unroll
    for (int i = 0; i < 4; ++i) zop[i] = pkh(zre[i], zim[i]);
    const f32x4 z4 = {0, 0, 0, 0};
    const f32x4 tre = mfma16h(zop, mre, z4), tim = mfma16h(zop, mim, z4);
    const unsigned to = (unsigned)(r * 16 * 128 * 128 + q4 * 16);
    const unsigned tso = T_OFF + (unsigned)((2 * w * 128 + s2) * 128 + q * 64);
    st16(v4u{pkh(tre[0], tim[0]), tag, pkh(tre[1], tim[1]), tag}, rws, to, tso);
    st16(v4u{pkh(tre[2], tim[2]), tag, pkh(tre[3], tim[3]), tag}, rws, to, tso + 128 * 128);
  }
  STAMP(4)
  v4u wf[28];
  const unsigned trb = L0 + L_BIG + (unsigned)((((lane >> 5) * 4 + ((lane >> 4) & 1)) * 1024) + ((lane >> 2) & 3) * 256 +
                                               (((2 * w + ((lane >> 1) & 1)) ^ (2 * (((lane >> 2) & 3) + 4 * ((lane >> 4) & 1)))) * 16) + (lane & 1) * 8);
#define W_READ(n0, n1)                                                                   \
  {                                                                                      \
    unsigned t0 = trb, t1 = trb + 57344;                                                 \
    asm volatile("" : "+v"(t0), "+v"(t1));                                               \
    _Pragma("unroll") for (int n_ = (n0); n_ < (n1); ++n_) {                             \
      const int sl = n_ % 14;                                                            \
      const unsigned a = sl < 7 ? t0 + sl * 8192 : t1 + (sl - 7) * 8192;                 \
      const v2u lo = ldtr(a), hi = ldtr(a + 2048);                                       \
      wf[n_] = v4u{lo.x, lo.y, hi.x, hi.y};                                              \
    }                                                                                    \
    _Pragma("unroll") for (int n_ = (n0); n_ < (n1); ++n_) asm volatile("" : "+v"(wf[n_])); \
  }
  W_DMA(12, 14)
  VMW(4);
  BAR();
  W_READ(0, 12)
  BAR();
  W_DMA(14, 20)
  STAMP(5)
  v4u fre[4], fim[4];
  const int arow = 16 * w3 + 4 * (r >> 2) + 2 * (r & 1) + 64 * ((r >> 1) & 1) + par;
  {
    const float af = (float)arow * (1.0f / 128.0f);
#pragma unroll
    for (int g = 0; g < 4; ++g)
#pragma unroll
      for (int i = 0; i < 4; ++i) {
        const float a = (float)(16 * g + 4 * q4 + 2 * (i & 1) + (i >> 1)) * af;
        const float c = cosr(a), s = sinr(a);
        fre[g][i] = pkh(c, s); fim[g][i] = pkh(-s, c);
      }
#pragma unroll
    for (int g = 0; g < 4; ++g) asm volatile("" : "+v"(fre[g]), "+v"(fim[g]));
  }
  if (DLY_T) __builtin_amdgcn_s_sleep(DLY_T);
  STAMP(6)
  const unsigned hclr = HF_OFF + (unsigned)((2 * s2 + q) * 28672);
  {
    const unsigned tso = T_OFF + (unsigned)((j1b * 16 + tileb) * 16384 + w * 1024);
    v4u g0, g1;
    int spins = 0;
    for (;;) {
      MEMB();
      g0 = ld16(rws, lane * 16, tso); g1 = ld16(rws, lane * 16, tso + 8192);
      const bool ok = g0.y == tag && g0.w == tag && g1.y == tag && g1.w == tag;
      if (__all(ok) || ++spins > SPIN_MAX) break;
    }
    CNT(20, spins)
    VMW(0);
    const int m = 8 * w + (lane >> 3);
    const unsigned o = L0 + L_BT + (unsigned)(((m >> 4) * 64 + ((m >> 2) & 3) * 16 + 2 * (lane & 7)) * 16 + (((m >> 1) & 1) + 2 * (m & 1)) * 4);
    *(LAS unsigned*)(size_t)o = hadd(g0.x, g1.x);
    *(LAS unsigned*)(size_t)(o + 16) = hadd(g0.z, g1.z);
    *(LAS unsigned*)(size_t)(o + 4096) = hsub(g0.x, g1.x);
    *(LAS unsigned*)(size_t)(o + 4096 + 16) = hsub(g0.z, g1.z);
    W_DMA(20, 24)
    st16(sent4, rws, tid * 16, hclr);
    st16(sent4, rws, tid * 16, hclr + 8192);
    STAMP(7)
    BAR();
    f32x4 vre = {0, 0, 0, 0}, vim = {0, 0, 0, 0};
#pragma unroll
    for (int g = 0; g < 4; ++g) {
      const v4u tf = lds16(L0 + L_BT + par * 4096 + g * 1024 + lane * 16);
      vre = mfma16h(fre[g], tf, vre); vim = mfma16h(fim[g], tf, vim);
    }
    float sc = SCALE_B * 0.125f;
    if (kb == 0 || kb == 224) sc = 0.5f * SCALE_B * 0.125f;
    if (kb > 224) sc = 0.f;
    float pr[4], pi[4];
#pragma unroll
    for (int i = 0; i < 4; ++i) {
      const float a = blo(fr[i]) * sc, bb = blo(fi[i]) * sc;
      pr[i] = vre[i] * a - vim[i] * bb; pi[i] = -(vre[i] * bb + vim[i] * a);
    }
    const unsigned po = L0 + L_BP + w3 * 1024 + lane * 16 + par * 8;
    *(LAS v2u*)(size_t)po = v2u{pkh(pr[0] + pr[2], pi[0] + pi[2]), pkh(pr[1] + pr[3], pi[1] + pi[3])};
    *(LAS v2u*)(size_t)(po + 4096) = v2u{pkh(pr[0] - pr[2], pi[0] - pi[2]), pkh(pr[1] - pr[3], pi[1] - pi[3])};
    BAR();
    f32x4 ure = {0, 0, 0, 0}, uim = {0, 0, 0, 0};
#pragma unroll
    for (int g = 0; g < 4; ++g) {
      const v4u pf = lds16(L0 + L_BP + par * 4096 + g * 1024 + lane * 16);
      ure = mfma16h(pf, fre[g], ure); uim = mfma16h(pf, fim[g], uim);
    }
    const unsigned uo = (unsigned)(arow * 32768 + q4 * 16), uso = U_OFF + (unsigned)((tileb * 16 + j1b) * 128);
    st16(v4u{pkh(ure[0] * 0.125f, uim[0] * -0.125f), tag, pkh(ure[1] * 0.125f, uim[1] * -0.125f), tag}, rws, uo, uso);
    st16(v4u{pkh(ure[2] * 0.125f, uim[2] * -0.125f), tag, pkh(ure[3] * 0.125f, uim[3] * -0.125f), tag}, rws, uo + 64, uso);
  }
  STAMP(8)
  W_DMA(24, 26)
  VMW(6);
  BAR();
  if (b == 0 && tid == 0) __builtin_amdgcn_raw_buffer_store_b32(tag, rep_, 0, 0, 16);
  W_READ(12, 24)
  BAR();
  const rsrc_t rd = mkrs(wd, F_ * D_ * 2);
  const unsigned dvoff = (unsigned)((lane >> 3) * 896 + (((lane & 7) ^ (4 * ((lane >> 4) & 1))) * 16));
  const unsigned dsoff = (unsigned)(224 * w * 896 + nE * 128);
#define ER(i) ((i) + nE >= 7 ? (i) + nE - 7 : (i) + nE)
#define WD_DMA1(j, ldsaddr) dma(rd, dvoff, dsoff + (32 * ER((j) >> 2) + 8 * (((j) >> 1) & 1) + 16 * ((j) & 1)) * 896, (ldsaddr));
#define WD_PRE(j0, j1) _Pragma("unroll") for (int j_ = (j0); j_ < (j1); ++j_) WD_DMA1(j_, L0 + L_BIG + (j_ * 8 + w) * 1024)
  const int srow = 128 * r + s2;
  const int dt = w < 7 ? w : 6;
  const int d0 = 16 * dt + 4 * q4;
  v2u xv[4];
  {
    const unsigned o1 = (unsigned)(srow * 896 + 2 * d0), o2 = (unsigned)(srow * 896 + 440 - 2 * d0);
    xv[0] = __builtin_amdgcn_raw_buffer_load_b64(rx, o1, 0, 0); xv[1] = __builtin_amdgcn_raw_buffer_load_b64(rx, o1 + 448, 0, 0);
    xv[2] = __builtin_amdgcn_raw_buffer_load_b64(rx, o2, 0, 0); xv[3] = __builtin_amdgcn_raw_buffer_load_b64(rx, o2 + 448, 0, 0);
  }
  W_DMA(26, 28)
  WD_PRE(0, 4)
  STAMP(9)
  v4u ce[4], co[4], se[4], so[4];
  {
#pragma unroll
    for (int i = 0; i < 4; ++i) {
      const float a = (float)((4 * q4 + i) * srow) * (1.0f / 2048.0f);
      const float c = 0.25f * cosr(a), s = 0.25f * sinr(a);
      mre[i] = pkh(c, -s); mim[i] = pkh(s, c);
    }
    const float df = (float)(2 * (16 * dt + r) + 1) * (1.0f / 896.0f);
    const float c2 = 2.0f * cosr(df);
    const f32x2 c22 = {c2, c2};
#pragma unroll
    for (int kc = 0; kc < 4; ++kc) {
      const float a0 = (float)(64 * kc + 16 * q4) * df, a1 = a0 + df;
      f32x2 cs[16];
      cs[0] = f32x2{cosr(a0), sinr(a0)}; cs[1] = f32x2{cosr(a1), sinr(a1)};
#pragma unroll
      for (int e = 2; e < 16; ++e) cs[e] = __builtin_elementwise_fma(c22, cs[e - 1], -cs[e - 2]);
#pragma unroll
      for (int j = 0; j < 4; ++j) {
        ce[kc][j] = pkh(cs[4 * j].x, cs[4 * j + 2].x); co[kc][j] = pkh(cs[4 * j + 1].x, cs[4 * j + 3].x);
        se[kc][j] = pkh(cs[4 * j].y, cs[4 * j + 2].y); so[kc][j] = pkh(cs[4 * j + 1].y, cs[4 * j + 3].y);
      }
      asm volatile("" : "+v"(ce[kc]), "+v"(co[kc]), "+v"(se[kc]), "+v"(so[kc]));
    }
    asm volatile("" : "+v"(mre), "+v"(mim));
  }
  if (DLY_U) __builtin_amdgcn_s_sleep(DLY_U);
  STAMP(10)
  {
    v4u uf[2];
    const unsigned uso = U_OFF + (unsigned)((s2 * 16 + w) * 2048);
    const unsigned uo = (unsigned)(q4 * 512 + ((r >> 1) & 1) * 64 + (r >> 2) * 16 + (r & 1) * 8);
    int spins = 0;
    for (;;) {
      MEMB();
      bool ok = true;
#pragma unroll
      for (int u = 0; u < 2; ++u)
#pragma unroll
        for (int i = 0; i < 4; ++i) {
          const v2u g = ld8(rws, uo + i * 128, uso + u * 16384);
          uf[u][i] = g.x; ok = ok && g.y == tag;
        }
      if (__all(ok) || ++spins > SPIN_MAX) break;
    }
    CNT(21, spins)
    VMW(0);
    if (w < 4) st16(sent4, rws, tid * 16, hclr + 24576);
    st16(sent4, rws, tid * 16, hclr + 16384);
    WD_PRE(4, 8)
    STAMP(11)
    const f32x4 z4 = {0, 0, 0, 0};
#pragma unroll
    for (int u = 0; u < 2; ++u) {
      const f32x4 yre = mfma16h(uf[u], mre, z4), yim = mfma16h(uf[u], mim, z4);
      const int c = w + 8 * u;
      const unsigned o = L0 + L_C + (q4 >> 1) * 8192 + (c * 16 + (r ^ (c & 7))) * 16 + (q4 & 1) * 8;
      *(LAS v2u*)(size_t)o = v2u{pkh(yre[0], yre[1]), pkh(yre[2], yre[3])};
      *(LAS v2u*)(size_t)(o + 4096) = v2u{pkh(yim[0], yim[1]), pkh(yim[2], yim[3])};
    }
    BAR();
    f32x4 Ce = z4, Co = z4, Se = z4, So = z4;
#pragma unroll
    for (int kc = 0; kc < 4; ++kc) {
      const int c = 4 * kc + q4;
      const unsigned o = L0 + L_C + (c * 16 + (r ^ (c & 7))) * 16;
      Ce = mfma16h(ce[kc], lds16(o), Ce);
      Se = mfma16h(se[kc], lds16(o + 4096), Se);
      Co = mfma16h(co[kc], lds16(o + 8192), Co);
      So = mfma16h(so[kc], lds16(o + 12288), So);
    }
    WD_PRE(8, 10)
    float hv[4][4];
    float sum = 0.f;
#pragma unroll
    for (int i = 0; i < 4; ++i) {
      const float a = Ce[i] - Se[i], bb = Co[i] - So[i], c = Ce[i] + Se[i], d = Co[i] + So[i];
      const unsigned xa_ = i < 2 ? xv[0].x : xv[0].y, xb = i < 2 ? xv[1].x : xv[1].y;
      const unsigned xc_ = i < 2 ? xv[2].y : xv[2].x, xd = i < 2 ? xv[3].y : xv[3].x;
      const float x0 = (i & 1) ? bhi(xa_) : blo(xa_), x1 = (i & 1) ? bhi(xb) : blo(xb);
      const float x2 = (i & 1) ? blo(xc_) : bhi(xc_), x3 = (i & 1) ? blo(xd) : bhi(xd);
      const unsigned h01 = pkb(__builtin_fmaf(a + bb, SCALE_Y, x0), __builtin_fmaf(a - bb, SCALE_Y, x1));
      const unsigned h23 = pkb(__builtin_fmaf(c - d, SCALE_Y, x2), __builtin_fmaf(c + d, SCALE_Y, x3));
      hv[0][i] = blo(h01); hv[1][i] = bhi(h01); hv[2][i] = blo(h23); hv[3][i] = bhi(h23);
      sum += (hv[0][i] + hv[1][i]) + (hv[2][i] + hv[3][i]);
    }
    sum = xor32_add(xor16_add(sum));
    const float lm = sum * (1.0f / 64.0f);
    float m2 = 0.f;
#pragma unroll
    for (int g = 0; g < 4; ++g)
#pragma unroll
      for (int i = 0; i < 4; ++i) { const float dlt = hv[g][i] - lm; m2 = __builtin_fmaf(dlt, dlt, m2); }
    m2 = xor32_add(xor16_add(m2));
    if (q4 == 0) *(LAS f32x2*)(size_t)(L0 + L_ST + (r * 8 + w) * 8) = f32x2{lm, m2};
    VMW(6);
    BAR();
    if (w < 7) {
      f32x4 sv[4];
#pragma unroll
      for (int j = 0; j < 4; ++j) sv[j] = *(LAS const f32x4*)(size_t)(L0 + L_ST + r * 64 + j * 16);
      const float mean = (((sv[0].x + sv[0].z) + (sv[1].x + sv[1].z)) + ((sv[2].x + sv[2].z) + sv[3].x)) * (1.0f / 7.0f);
      float M2 = 0.f;
#pragma unroll
      for (int j = 0; j < 7; ++j) {
        const float dlt = sv[j >> 1][2 * (j & 1)] - mean;
        M2 += __builtin_fmaf(64.0f * dlt, dlt, sv[j >> 1][2 * (j & 1) + 1]);
      }
      const float rs = __builtin_amdgcn_rsqf(M2 * (1.0f / 448.0f) + 1e-5f);
      const float off = -mean * rs;
      const unsigned lo = (unsigned)((q4 * 64 + r) * 16);
      const unsigned lso = (unsigned)((((s2 >> 1) * 28 + 4 * dt) * 64 + 16 * (s2 & 1)) * 16);
#define NRM(g, i) __builtin_fmaf(hv[g][i], rs, off)
      if (q == 0) {
        st16(v4u{pkb(NRM(0, 0), NRM(0, 1)), pkb(NRM(0, 2), NRM(0, 3)), pkb(NRM(1, 0), NRM(1, 1)), pkb(NRM(1, 2), NRM(1, 3))}, rws, lo, LN_OFF + lso);
        st16(v4u{pkb(hv[2][3], hv[2][2]), pkb(hv[2][1], hv[2][0]), pkb(hv[3][3], hv[3][2]), pkb(hv[3][1], hv[3][0])}, rws, lo + 512, HR_OFF + lso);
      } else {
        st16(v4u{pkb(NRM(2, 0), NRM(2, 1)), pkb(NRM(2, 2), NRM(2, 3)), pkb(NRM(3, 0), NRM(3, 1)), pkb(NRM(3, 2), NRM(3, 3))}, rws, lo + 512, LN_OFF + lso);
        st16(v4u{pkb(hv[0][0], hv[0][1]), pkb(hv[0][2], hv[0][3]), pkb(hv[1][0], hv[1][1]), pkb(hv[1][2], hv[1][3])}, rws, lo, HR_OFF + lso);
      }
    }
  }
  STAMP(12)
  if ((b >> 3) >= 28) {
    VMW(0);
  } else {
    v4u wdf[7][4];
    const unsigned tdb = (unsigned)((q4 >> 1) * 1024 + (4 * (q4 & 1) + ((lane >> 2) & 3)) * 128 + ((lane >> 3) & 1) * 64 + (lane & 3) * 16);
#define WD_READ(u, base0, base1, off)                                                                  \
  _Pragma("unroll") for (int nt = 0; nt < 4; ++nt) {                                                   \
    const v2u t_ = ldtr(((nt >> 1) ? (base1) : (base0)) + (off) + (nt & 1) * 8);                       \
    wdf[(u) >> 1][nt][2 * ((u) & 1)] = t_.x; wdf[(u) >> 1][nt][2 * ((u) & 1) + 1] = t_.y;              \
  }
    W_READ(24, 28)
    if (w < 7) { VMW(2); } else { VMW(0); }
    {
      const unsigned a0 = L0 + L_BIG + w * 1024 + tdb + (q4 >> 1) * 7168, a1 = a0 ^ 64;
#pragma unroll
      for (int u = 0; u < 4; ++u) WD_READ(u, a0, a1, u * 16384)
      WD_READ(4, a0 + 65536, a1 + 65536, 0)
    }
    BAR();
    if (DLY_L) __builtin_amdgcn_s_sleep(DLY_L);
    STAMP(13)
    const unsigned lnso = LN_OFF + (unsigned)(mG * 114688);
    const unsigned la = L0 + L_BIG + lane * 16;
#define LN_DMA(i)                                                                                      \
  {                                                                                                    \
    const int pi_ = 8 * (i) + w, tt_ = pi_ / 28, j_ = pi_ - 28 * tt_;                                  \
    dma1(rws, lane * 16, lnso + (28 * tt_ + KROT(j_)) * 1024, L0 + L_BIG + pi_ * 1024);                \
  }
#define LN_MAX(i0, i1) \
  _Pragma("unroll") for (int i_ = (i0); i_ < (i1); ++i_) mx = max4(lds16((i_ < 7 ? la : la + 57344) + w * 1024 + (i_ < 7 ? i_ : i_ - 7) * 8192), mx);
#define LN_FIX(i0, i1)                                                                                 \
  if (__any(mx == SENT)) {                                                                             \
    int spins = 0;                                                                                     \
    do {                                                                                               \
      _Pragma("unroll") for (int i_ = (i0); i_ < (i1); ++i_) LN_DMA(i_)                                \
      VMW(0);                                                                                          \
      mx = 0;                                                                                          \
      LN_MAX(i0, i1)                                                                                   \
      lspins++;                                                                                        \
    } while (__any(mx == SENT) && ++spins < SPIN_MAX);                                                 \
  }
    unsigned mx = 0;
    int lspins = 0;
#pragma unroll
    for (int i = 0; i < 4; ++i) LN_DMA(i)
    VMW(0);
    LN_MAX(0, 4)
    LN_FIX(0, 4)
    BAR();
    STAMP(14)
    f32x16 acc, accp;
    v4u af[4], ho;
    v4u hb[4][7];
    const unsigned hso = HF_OFF + (unsigned)(4 * mG * 114688 + (8 * nD + w) * 1024);
    const unsigned hvo = (unsigned)(q4 * 512 + pE * 256 + r * 16);
    const unsigned hlo = HF_OFF + (unsigned)(4 * mG * 114688 + 14 * w * 1024);
    const unsigned spw = L0 + L_SP + w * 6144;
#define H_LOAD(tt, i) hb[tt][i] = ld16(rws, hvo, hlo + (tt) * 114688 + ER(i) * 2048);
    auto dpass = [&](auto TT) __attribute__((always_inline)) {
      constexpr int tt = decltype(TT)::value;
      unsigned lbt = la + tt * 28672;
      asm volatile("" : "+v"(lbt));
#pragma unroll
      for (int i = 0; i < 3; ++i) af[i] = lds16(lbt + i * 1024);
#pragma unroll
      for (int i = 0; i < 16; ++i) acc[i] = 0.f;
      mx = 0;
      SB();
#pragma unroll
      for (int ks = 0; ks < 28; ++ks) {
        if (ks + 3 < 28) af[(ks + 3) & 3] = lds16(lbt + (ks + 3) * 1024);
        acc = mfma32b(wf[ks], af[ks & 3], acc);
        if (tt > 0) {
          if (ks < 8 && (ks & 1) == 1) {
            const int j = ks >> 1;
            ho[j] = glu2(accp[2 * j], accp[2 * j + 1], accp[8 + 2 * j], accp[8 + 2 * j + 1]);
          }
          if (ks == 8) st16(ho, rws, lane * 16, hso + (tt - 1) * 114688);
        }
        if (tt == 0) {
          if (ks < 6 && (ks & 1) == 0) LN_DMA(4 + (ks >> 1))
          if (ks >= 6 && ks < 18 && (ks & 1) == 0) WD_DMA1(10 + ((ks - 6) >> 1), spw + ((ks - 6) >> 1) * 1024)
          if (ks == 22) { VMW(6); LN_MAX(4, 7) }
        }
        if (tt == 1) {
          if (ks < 8 && (ks & 1) == 0) LN_DMA(7 + (ks >> 1))
          if (ks == 10 || ks == 12) WD_DMA1(20 + ((ks - 10) >> 1), L0 + w * 2048 + ((ks - 10) >> 1) * 1024)
          if (ks == 16) { VMW(3); WD_READ(5, spw + tdb, spw + (tdb ^ 64), 0) }
          if (ks == 17) WD_READ(6, spw + tdb, spw + (tdb ^ 64), 2048)
          if (ks == 18) LN_MAX(7, 11)
          if (ks == 20) asm volatile("s_waitcnt lgkmcnt(0)" ::: "memory");
          if (ks > 20 && (ks & 1) == 1) WD_DMA1(16 + ((ks - 21) >> 1), spw + ((ks - 21) >> 1) * 1024)
        }
        if (tt == 2) {
          if (ks < 6 && (ks & 1) == 0) LN_DMA(11 + (ks >> 1))
          if (ks >= 10 && ks < 18 && (ks & 1) == 0) WD_DMA1(22 + ((ks - 10) >> 1), L0 + 16384 + w * 4096 + ((ks - 10) >> 1) * 1024)
          if (ks == 22) { VMW(5); LN_MAX(11, 14) }
        }
        if (tt == 3) {
          if (ks == 0) { WD_DMA1(26, L0 + 57344 + w * 2048) WD_DMA1(27, L0 + 57344 + w * 2048 + 1024) }
          if (ks >= 1 && ks < 8) H_LOAD(0, ks - 1)
          if (ks == 8) VMW(10);
          if (ks == 20) VMW(8);
          if (ks >= 8 && ks < 22 && (ks & 1) == 0) {
            const int u = 7 + ((ks - 8) >> 1);
            const unsigned ub = u == 7 ? spw + 4096 : u < 10 ? spw + (u - 8) * 2048 : u == 10 ? L0 + w * 2048
                                : u < 13 ? L0 + 16384 + w * 4096 + (u - 11) * 2048 : L0 + 57344 + w * 2048;
            WD_READ(u, ub + tdb, ub + (tdb ^ 64), 0)
          }
          if (ks >= 21) H_LOAD(1, ks - 21)
        }
        SB();
      }
      accp = acc;
      if (tt == 0) LN_FIX(4, 7)
      if (tt == 1) LN_FIX(7, 11)
      if (tt == 2) LN_FIX(11, 14)
      if (tt < 3) BAR();
      if (tt == 0) STAMP(15)
    };
    dpass(IC<0>{}); dpass(IC<1>{}); dpass(IC<2>{}); dpass(IC<3>{});
    CNT(22, lspins)
    STAMP(16)
#pragma unroll
    for (int j = 0; j < 4; ++j) ho[j] = glu2(accp[2 * j], accp[2 * j + 1], accp[8 + 2 * j], accp[8 + 2 * j + 1]);
    st16(ho, rws, lane * 16, hso + 3 * 114688);
#pragma unroll
    for (int i = 0; i < 7; ++i) H_LOAD(2, i)
    const int ttf = w >> 1, c0 = 64 * nE + 32 * (w & 1) + 8 * q4;
    v2u hr0, hr1;
    unsigned hro0, hro1;
    const unsigned hrs = HR_OFF + (unsigned)((4 * mG + ttf) * 28672 + pE * 256);
    {
      const int seg = c0 / 112, hi = seg >> 1, mir = seg & 1, cc = c0 - 224 * hi;
      const int ksa = mir ? (220 - cc) >> 2 : cc >> 2;
      hro0 = (unsigned)((ksa * 64 + 32 * mir + r) * 16 + 8 * hi);
      hro1 = mir ? hro0 - 1024 : hro0 + 1024;
      hr0 = ld8(rws, hro0, hrs); hr1 = ld8(rws, hro1, hrs);
    }
    SB();
    STAMP(17)
    int espins = 0;
#pragma unroll
    for (int tt = 0; tt < 4; ++tt) {
      {
        mx = 0;
#pragma unroll
        for (int i = 0; i < 7; ++i) mx = max4(hb[tt][i], mx);
        if (__any(mx == SENT)) {
          int spins = 0;
          do {
            MEMB();
#pragma unroll
            for (int i = 0; i < 7; ++i) H_LOAD(tt, i)
            mx = 0;
#pragma unroll
            for (int i = 0; i < 7; ++i) mx = max4(hb[tt][i], mx);
            espins += 1 << (8 * tt);
          } while (__any(mx == SENT) && ++spins < SPIN_MAX);
        }
      }
      f32x4 ea[4];
#pragma unroll
      for (int nt = 0; nt < 4; ++nt) ea[nt] = f32x4{0, 0, 0, 0};
#pragma unroll
      for (int i = 0; i < 7; ++i)
#pragma unroll
        for (int nt = 0; nt < 4; ++nt) ea[nt] = mfma16b(wdf[i][nt], hb[tt][i], ea[nt]);
      SB();
      if (tt == 1) {
#pragma unroll
        for (int i = 0; i < 7; ++i) H_LOAD(3, i)
      }
      if (tt == 0) BAR();
#pragma unroll
      for (int nt = 0; nt < 4; ++nt) *(LAS f32x4*)(size_t)(la + w * 16384 + (tt * 4 + nt) * 1024) = ea[nt];
      SB();
    }
    CNT(23, espins)
    STAMP(18)
    if (__any(max(max(hr0.x, hr0.y), max(hr1.x, hr1.y)) == SENT)) {
      int spins = 0;
      do {
        MEMB();
        hr0 = ld8(rws, hro0, hrs); hr1 = ld8(rws, hro1, hrs);
      } while (__any(max(max(hr0.x, hr0.y), max(hr1.x, hr1.y)) == SENT) && ++spins < SPIN_MAX);
    }
    BAR();
    {
      f32x4 s0 = {0, 0, 0, 0}, s1 = {0, 0, 0, 0};
      unsigned ra = la + w * 2048, rb = ra + 65536;
      asm volatile("" : "+v"(rb));
#pragma unroll
      for (int ww = 0; ww < 8; ++ww) {
        const unsigned a = (ww < 4 ? ra : rb) + (ww & 3) * 16384;
        s0 += *(LAS const f32x4*)(size_t)a;
        s1 += *(LAS const f32x4*)(size_t)(a + 1024);
      }
      const unsigned f0 = pkb(s0[0], s0[1]), f1 = pkb(s0[2], s0[3]), f2 = pkb(s1[0], s1[1]), f3 = pkb(s1[2], s1[3]);
      v4u o;
      o[0] = pkb(blo(hr0.x) + blo(f0), bhi(hr0.x) + bhi(f0));
      o[1] = pkb(blo(hr0.y) + blo(f1), bhi(hr0.y) + bhi(f1));
      o[2] = pkb(blo(hr1.x) + blo(f2), bhi(hr1.x) + bhi(f2));
      o[3] = pkb(blo(hr1.y) + blo(f3), bhi(hr1.y) + bhi(f3));
      *(v4u*)(out + (size_t)(128 * r + 8 * mG + 2 * ttf + pE) * D_ + c0) = o;
    }
  }
}

extern "C" void kernel_launch(void* const* d_in, const int* in_sizes, int n_in, void* d_out, int out_size, void* d_ws,
                              size_t ws_size, hipStream_t stream) {
  fused<<<256, 512, 0, stream>>>((const uint16_t*)d_in[0], (const uint16_t*)d_in[1], (const uint16_t*)d_in[2],
                                 (const uint16_t*)d_in[4], (const uint16_t*)d_in[5], (const uint16_t*)d_in[6],
                                 (unsigned char*)d_ws, (uint16_t*)d_out);
}
```

```cpp
#include <hip/hip_runtime.h>
#include <hip/hip_bf16.h>
#include <stdint.h>

typedef unsigned v4u __attribute__((ext_vector_type(4)));
typedef unsigned v2u __attribute__((ext_vector_type(2)));
typedef float f32x2 __attribute__((ext_vector_type(2)));
typedef float f32x4 __attribute__((ext_vector_type(4)));
typedef float f32x16 __attribute__((ext_vector_type(16)));
typedef _Float16 h8 __attribute__((ext_vector_type(8)));
typedef _Float16 h2 __attribute__((ext_vector_type(2)));
typedef __bf16 b8 __attribute__((ext_vector_type(8)));
typedef __bf16 b2 __attribute__((ext_vector_type(2)));
typedef short s4v __attribute__((ext_vector_type(4)));
typedef __amdgpu_buffer_rsrc_t rsrc_t;
#define DI __device__ __forceinline__
template <int N> struct IC { static constexpr int value = N; };
#define LAS __attribute__((address_space(3)))

constexpr int S_ = 2048, D_ = 448, F_ = 1792, DF_ = 225;
constexpr unsigned MB = 1u << 20;
constexpr unsigned U_OFF = 0;
constexpr unsigned EP_OFF = 2 * MB;
constexpr unsigned FL_OFF = 2 * MB + 4096;
constexpr unsigned SS_OFF = 2 * MB + 65536;
constexpr unsigned LH_OFF = 3 * MB;
constexpr unsigned LH_STR = 5 * 57344;
constexpr unsigned FIX_BYTES = 16 * MB;
constexpr unsigned REG_BYTES = 4 * MB;
#define NROT 8
constexpr unsigned OOB = 0x80000000u;
constexpr unsigned SENT = 0xffffffffu;
constexpr int SPIN_MAX = 20000;
constexpr unsigned L_SP = 17 * 8192;
constexpr unsigned L_A = L_SP, L_BT = L_SP + 8192, L_BP = L_SP + 16384, L_F = L_BP, L_X = L_SP, L_C = L_SP, L_ST = L_SP + 23552;
constexpr int wslot(int n) { return n < 17 ? n : n - 17; }
constexpr int dslot(int j) {
  return j < 2 ? 11 + j : j < 8 ? j - 2 : j < 14 ? j + 6 : j < 18 ? j : j < 20 ? j - 18 : j < 22 ? j - 2 : j < 26 ? j - 20 : j - 12;
}

#define NAF 3
#define DLY_T 0
#define DLY_U 0
#define DLY_L 0

#define STAMP_TID 0
#define STAMP(i) {}
#define CNT(i, v)

DI unsigned pkh(float a, float b) { h2 v = {(_Float16)a, (_Float16)b}; return __builtin_bit_cast(unsigned, v); }
DI unsigned pkb(float a, float b) { b2 v = {(__bf16)a, (__bf16)b}; return __builtin_bit_cast(unsigned, v); }
DI float blo(unsigned u) { return __builtin_bit_cast(float, u << 16); }
DI float bhi(unsigned u) { return __builtin_bit_cast(float, u & 0xffff0000u); }
DI float cosr(float x) { return __builtin_amdgcn_cosf(x); }
DI float sinr(float x) { return __builtin_amdgcn_sinf(x); }
DI unsigned hadd(unsigned a, unsigned b) { return __builtin_bit_cast(unsigned, __builtin_bit_cast(h2, a) + __builtin_bit_cast(h2, b)); }
DI unsigned hsub(unsigned a, unsigned b) { return __builtin_bit_cast(unsigned, __builtin_bit_cast(h2, a) - __builtin_bit_cast(h2, b)); }
DI f32x4 mfma16h(v4u a, v4u b, f32x4 c) {
  return __builtin_amdgcn_mfma_f32_16x16x32_f16(__builtin_bit_cast(h8, a), __builtin_bit_cast(h8, b), c, 0, 0, 0);
}
DI f32x4 mfma16b(v4u a, v4u b, f32x4 c) {
  return __builtin_amdgcn_mfma_f32_16x16x32_bf16(__builtin_bit_cast(b8, a), __builtin_bit_cast(b8, b), c, 0, 0, 0);
}
DI f32x16 mfma32b(v4u a, v4u b, f32x16 c) {
  return __builtin_amdgcn_mfma_f32_32x32x16_bf16(__builtin_bit_cast(b8, a), __builtin_bit_cast(b8, b), c, 0, 0, 0);
}
template <int CTRL> DI float dpp_add(float v) {
  return v + __builtin_bit_cast(float, __builtin_amdgcn_update_dpp(0, __builtin_bit_cast(int, v), CTRL, 0xf, 0xf, true));
}
DI float xor16_add(float v) {
  float a = v, b = v;
  asm("s_nop 1\n\tv_permlane16_swap_b32 %0, %1" : "+v"(a), "+v"(b));
  return a + b;
}
DI float xor32_add(float v) {
  float a = v, b = v;
  asm("s_nop 1\n\tv_permlane32_swap_b32 %0, %1" : "+v"(a), "+v"(b));
  return a + b;
}
DI float sum32(float v) {
  v = dpp_add<0xB1>(v); v = dpp_add<0x4E>(v); v = dpp_add<0x141>(v); v = dpp_add<0x140>(v);
  return xor16_add(v);
}
DI rsrc_t mkrs(const void* p, unsigned bytes) { return __builtin_amdgcn_make_buffer_rsrc((void*)p, (short)0, (int)bytes, 0x00020000); }
DI void dma(rsrc_t rs, unsigned voff, unsigned soff, unsigned lds) {
  asm volatile("s_mov_b32 m0, %0\n\ts_nop 0\n\tbuffer_load_dwordx4 %1, %2, %3 offen lds" ::"s"(lds), "v"(voff), "s"(rs), "s"(soff) : "memory");
}
DI void dma1(rsrc_t rs, unsigned voff, unsigned soff, unsigned lds) {
  asm volatile("s_mov_b32 m0, %0\n\ts_nop 0\n\tbuffer_load_dwordx4 %1, %2, %3 offen sc1 lds" ::"s"(lds), "v"(voff), "s"(rs), "s"(soff) : "memory");
}
template <int OFF> DI void ald8(v2u& d, rsrc_t rs, unsigned voff, unsigned soff) {
  asm volatile("buffer_load_dwordx2 %0, %1, %2, %3 offen offset:%4 sc1" : "=&v"(d) : "v"(voff), "s"(rs), "s"(soff), "n"(OFF) : "memory");
}
template <int OFF> DI void ald16(v4u& d, rsrc_t rs, unsigned voff, unsigned soff) {
  asm volatile("buffer_load_dwordx4 %0, %1, %2, %3 offen offset:%4 sc1" : "=&v"(d) : "v"(voff), "s"(rs), "s"(soff), "n"(OFF) : "memory");
}
DI v4u ld16(rsrc_t rs, unsigned voff, unsigned soff) { return __builtin_amdgcn_raw_buffer_load_b128(rs, voff, soff, 16); }
DI v2u ld8(rsrc_t rs, unsigned voff, unsigned soff) { return __builtin_amdgcn_raw_buffer_load_b64(rs, voff, soff, 16); }
DI void st16(v4u v, rsrc_t rs, unsigned voff, unsigned soff) { __builtin_amdgcn_raw_buffer_store_b128(v, rs, voff, soff, 16); }
DI v4u lds16(unsigned a) { return *(LAS const v4u*)(size_t)a; }
DI v2u ldtr(unsigned a) { return __builtin_bit_cast(v2u, __builtin_amdgcn_ds_read_tr16_b64_v4i16((LAS s4v*)(size_t)a)); }
DI unsigned max4(v4u v, unsigned m) { return max(max(max(max(m, v[0]), v[1]), v[2]), v[3]); }
#define BAR() asm volatile("s_waitcnt lgkmcnt(0)\n\ts_barrier" ::: "memory")
#define VMW(n) asm volatile("s_waitcnt vmcnt(" #n ")" ::: "memory")
#define SB() __builtin_amdgcn_sched_barrier(0)
#define MEMB() asm volatile("" ::: "memory")
#define PLO() __builtin_amdgcn_s_setprio(0)
#define PHI() __builtin_amdgcn_s_setprio(2)

constexpr float SCALE_B = 1.0f / 16.0f;
constexpr float SCALE_Y = 512.0f / (917504.0f * SCALE_B);

DI unsigned glu2(float g0, float g1, float u0, float u1, float rs, float rl) {
  const float s0 = __builtin_amdgcn_rcpf(1.0f + __builtin_amdgcn_exp2f(g0 * rl));
  const float s1 = __builtin_amdgcn_rcpf(1.0f + __builtin_amdgcn_exp2f(g1 * rl));
  return pkb(s0 * (u0 * rs), s1 * (u1 * rs));
}
DI void ald4(unsigned& d, rsrc_t rs, unsigned voff, unsigned soff) {
  asm volatile("buffer_load_dword %0, %1, %2, %3 offen sc1" : "=&v"(d) : "v"(voff), "s"(rs), "s"(soff) : "memory");
}
DI void touch(unsigned& d, rsrc_t rs, unsigned voff, unsigned soff) {
  asm volatile("buffer_load_dword %0, %1, %2, %3 offen sc1" : "+v"(d) : "v"(voff), "s"(rs), "s"(soff) : "memory");
}
DI float sum2(unsigned u, float c) {
  const b2 one2 = {(__bf16)1.0f, (__bf16)1.0f};
  return __builtin_amdgcn_fdot2_f32_bf16(__builtin_bit_cast(b2, u), one2, c, false);
}
DI void ast16(v4u v, rsrc_t rs, unsigned voff, unsigned soff) {
  asm volatile("buffer_store_dwordx4 %0, %1, %2, %3 offen sc1" ::"v"(v), "v"(voff), "s"(rs), "s"(soff) : "memory");
}
DI float trunc16(float v) { return bhi(__builtin_bit_cast(unsigned, v)); }
DI unsigned hi16(float a, float b) { return __builtin_amdgcn_perm(__builtin_bit_cast(unsigned, b), __builtin_bit_cast(unsigned, a), 0x07060302u); }

template <bool SPARE>
DI void body(const uint16_t* __restrict__ x, const uint16_t* __restrict__ wr, const uint16_t* __restrict__ wi,
             const uint16_t* __restrict__ wg, const uint16_t* __restrict__ wu, const uint16_t* __restrict__ wd,
             unsigned char* __restrict__ ws, uint16_t* __restrict__ out, const unsigned L0) {
  STAMP(0)
  const int b = blockIdx.x, tid = threadIdx.x, lane = tid & 63;
  const int w = __builtin_amdgcn_readfirstlane(tid >> 6);
  const int r = lane & 15, q4 = lane >> 4;
  const int s2 = (b & 7) + 8 * (b >> 4), q = (b >> 3) & 1;
  const int j1b = b & 15, tileb = b >> 4;
  const int yb = b >> 3;
  const int mG = 4 * ((b >> 1) & 3) + (yb & 3), nD = 7 * (b & 1) + (yb >> 2);
  const int mE = 2 * (b & 7) + (yb & 1), pE = (yb >> 1) & 1, nE = yb >> 2;
  const int sbk = 4 * (b & 7) + (yb & 3);
  const rsrc_t rx = mkrs(x, S_ * D_ * 2), rfx = mkrs(ws, FIX_BYTES);
  PHI();

  v2u xa[4];
  unsigned ep;
  const int l32 = lane & 31;
  {
    const int lc = l32 < 28 ? l32 : 27;
    const unsigned rowo = (unsigned)(128 * (2 * w + (lane >> 5)) + s2) * 896u;
    const unsigned o1 = rowo + 8 * lc, o2 = rowo + 440 - 8 * lc;
    asm volatile("buffer_load_dwordx2 %0, %5, %7, 0 offen\n\t"
                 "buffer_load_dwordx2 %1, %6, %7, 0 offen\n\t"
                 "buffer_load_dwordx2 %2, %5, %7, 0 offen offset:448\n\t"
                 "buffer_load_dwordx2 %3, %6, %7, 0 offen offset:448\n\t"
                 "s_load_dword %4, %8, 0"
                 : "=&v"(xa[0]), "=&v"(xa[1]), "=&v"(xa[2]), "=&v"(xa[3]), "=&s"(ep)
                 : "v"(o1), "v"(o2), "s"(rx), "s"(ws + EP_OFF)
                 : "memory");
  }
  PLO();
  const v4u sent4 = {SENT, SENT, SENT, SENT};
  unsigned junk;
  {
    const int i5 = (lane * 52) >> 8, l5 = lane - 5 * i5;
    const unsigned tf = lane < 40 ? (unsigned)((((b & 7) + 8 * (8 * yb + i5)) * 450 + (l5 < 4 ? 128 * l5 : 446)) & ~3) : OOB;
    const unsigned td = lane < 49 ? (unsigned)(49 * b + lane) * 128 : OOB;
    ald4(junk, mkrs(w == 0 ? wr : w == 1 ? wi : wd, w < 2 ? S_ * DF_ * 2 : F_ * D_ * 2), w < 2 ? tf : w == 2 ? td : OOB, 0);
    ast16(sent4, rfx, tid * 16, U_OFF + (2 * s2 + q) * 8192);
    if (SPARE) ast16(sent4, rfx, lane < 8 ? (unsigned)(w * 8 + lane) * 16 : OOB, SS_OFF + sbk * 1024);
  }
  const int wmat = w & 1, wrange = w >> 1;
  const rsrc_t rw = mkrs(wmat ? wu : wg, D_ * F_ * 2);
  const unsigned wvoff = (unsigned)(((wrange >> 1) ? 3 - q4 : q4) * 3584 + ((r ^ (2 * (q4 + 4 * wmat))) * 16));
  const unsigned wsoff0 = (unsigned)((224 * (wrange & 1) + ((wrange >> 1) ? 220 : 0)) * 3584 + nD * 256);
  const int wstep = (wrange >> 1) ? -4 * 3584 : 4 * 3584;
  int rot = 64 + 7 * (yb & 3) + ((7 * ((b >> 1) & 3)) >> 2);
  asm volatile("" : "+s"(rot));
  const unsigned clr0 = LH_OFF + (unsigned)sbk * LH_STR;
#define KROT(j) ((j) + rot >= 92 ? (j) + rot - 92 : (j) + rot - 64)
#define W_DMA(n0, n1)                                                                                  \
  _Pragma("unroll") for (int n_ = (n0); n_ < (n1); ++n_) {                                             \
    if (SPARE) ast16(sent4, rfx, tid * 16, clr0 + n_ * 8192);                                          \
    else dma(rw, wvoff, wsoff0 + KROT(n_) * wstep, L0 + (wslot(n_) * 8 + w) * 1024);                   \
  }
#define FLAG(i) if (SPARE && tid == 0) __builtin_amdgcn_raw_buffer_store_b32(tag, rfx, sbk * 16, FL_OFF + (i) * 512, 16);
  const int par = w >> 2, w3 = w & 3;
  const int kb = 16 * tileb + r;
  const int fc0 = tileb < 14 ? 16 * tileb : 209;
  SB();
  v4u bc[4], bs[4], mre, mim;
  {
    const int k = 2 * (16 * w + 8 * ((r >> 1) & 1) + 2 * (r >> 2) + (r & 1)) + q;
    const float kf = (float)k * (1.0f / 896.0f);
    const float c2 = 2.0f * cosr(2.0f * kf);
    const f32x2 c22 = {c2, c2};
#pragma unroll
    for (int kc = 0; kc < 4; ++kc) {
      const float a0 = (float)(64 * kc + 16 * q4 + 1) * kf, a1 = a0 + 2.0f * kf;
      f32x2 cs[8];
      cs[0] = f32x2{cosr(a0), -sinr(a0)}; cs[1] = f32x2{cosr(a1), -sinr(a1)};
#pragma unroll
      for (int e = 2; e < 8; ++e) cs[e] = __builtin_elementwise_fma(c22, cs[e - 1], -cs[e - 2]);
#pragma unroll
      for (int j = 0; j < 4; ++j) { bc[kc][j] = pkh(cs[2 * j].x, cs[2 * j + 1].x); bs[kc][j] = pkh(cs[2 * j].y, cs[2 * j + 1].y); }
    }
#pragma unroll
    for (int i = 0; i < 4; ++i) {
      const float a = (float)(r * (128 * (4 * q4 + i) + s2)) * (1.0f / 2048.0f);
      const float c = cosr(a), s = sinr(a);
      mre[i] = pkh(c, s); mim[i] = pkh(-s, c);
    }
#pragma unroll
    for (int kc = 0; kc < 4; ++kc) asm volatile("" : "+v"(bc[kc]), "+v"(bs[kc]));
    asm volatile("" : "+v"(mre), "+v"(mim));
  }
  W_DMA(0, 4)
  PHI();
  STAMP(1)
  asm volatile("s_waitcnt vmcnt(6) lgkmcnt(0)" : "+v"(xa[0]), "+v"(xa[1]), "+v"(xa[2]), "+v"(xa[3]), "+s"(ep)::"memory");
  const unsigned tag = ep * 1664525u + 1013904223u;
  const rsrc_t rws = mkrs(ws + FIX_BYTES + (size_t)(tag & (NROT - 1)) * REG_BYTES, REG_BYTES);
  v4u fraw;
  {
    const unsigned c0 = __builtin_amdgcn_readlane(xa[0].x, 0), c1 = __builtin_amdgcn_readlane(xa[0].x, 32);
    const float cc = 2.0f * blo(lane < 32 ? c0 : c1);
    float s1[4], sb[4], d1[4], d2[4];
    float sum = 0.f, sq = 0.f;
#pragma unroll
    for (int i = 0; i < 4; ++i) {
      const unsigned ua = xa[0][i >> 1], ub = xa[1][1 - (i >> 1)], ue = xa[2][i >> 1], uf = xa[3][1 - (i >> 1)];
      const float a = (i & 1) ? bhi(ua) : blo(ua), e = (i & 1) ? bhi(ue) : blo(ue);
      const float bb = (i & 1) ? blo(ub) : bhi(ub), f = (i & 1) ? blo(uf) : bhi(uf);
      s1[i] = (a + f) - cc; sb[i] = (bb + e) - cc; d1[i] = a - f; d2[i] = e - bb;
      sum += s1[i] + sb[i];
      sq = __builtin_fmaf(s1[i], s1[i], sq); sq = __builtin_fmaf(sb[i], sb[i], sq);
      sq = __builtin_fmaf(d1[i], d1[i], sq); sq = __builtin_fmaf(d2[i], d2[i], sq);
    }
    if (l32 >= 28) { sum = 0.f; sq = 0.f; }
    asm volatile("" : "+v"(sum), "+v"(sq));
    W_DMA(4, 5)
    sum = sum32(sum); sq = sum32(sq);
    asm volatile("" : "+v"(sum), "+v"(sq));
    W_DMA(5, 6)
    const float mu = sum * (1.0f / 448.0f);
    const float var = __builtin_fmaxf(__builtin_fmaf(-mu, mu, sq * (0.5f / 448.0f)), 0.f);
    float rs = __builtin_amdgcn_rsqf(var + 1e-5f);
    if (l32 >= 28) rs = 0.f;
    const float sgn = q ? -1.0f : 1.0f;
    const float off = q ? 0.f : -4.0f * mu * rs;
    float P[4], Q[4];
#pragma unroll
    for (int i = 0; i < 4; ++i) {
      P[i] = __builtin_fmaf(__builtin_fmaf(sgn, sb[i], s1[i]), rs, off);
      Q[i] = __builtin_fmaf(sgn, d2[i], d1[i]) * rs;
    }
    const int c = l32 >> 1, row = 2 * w + (lane >> 5);
    const unsigned o = L0 + L_A + (c * 16 + (row ^ (c & 7))) * 16 + (l32 & 1) * 8;
    *(LAS v2u*)(size_t)o = v2u{pkh(P[0], P[1]), pkh(P[2], P[3])};
    *(LAS v2u*)(size_t)(o + 4096) = v2u{pkh(Q[0], Q[1]), pkh(Q[2], Q[3])};
  }
  W_DMA(6, 7)
  {
    const rsrc_t rf = mkrs((w & 1) ? wi : wr, S_ * DF_ * 2);
    const unsigned fo = (unsigned)(((j1b + 16 * (32 * (w >> 1) + (lane >> 1))) * DF_ + fc0 + 8 * (lane & 1)) * 2);
    asm volatile("buffer_load_dwordx4 %0, %1, %2, 0 offen" : "=&v"(fraw) : "v"(fo), "s"(rf) : "memory");
  }
  VMW(8);
  BAR();
  STAMP(3)
  {
    f32x4 zre = {0, 0, 0, 0}, zim = {0, 0, 0, 0};
#pragma unroll
    for (int kc = 0; kc < 4; ++kc) {
      const int c = 4 * kc + q4;
      const unsigned o = L0 + L_A + (c * 16 + (r ^ (c & 7))) * 16;
      zre = mfma16h(lds16(o), bc[kc], zre);
      zim = mfma16h(lds16(o + 4096), bs[kc], zim);
    }
    v4u zop;
#pragma unroll
    for (int i = 0; i < 4; ++i) zop[i] = pkh(zre[i], zim[i]);
    const f32x4 z4 = {0, 0, 0, 0};
    const f32x4 tre = mfma16h(zop, mre, z4), tim = mfma16h(zop, mim, z4);
    const unsigned to = (unsigned)(r * 16 * 128 * 128 + q4 * 16);
    const unsigned tso = (unsigned)((2 * w * 128 + s2) * 128 + q * 64);
    st16(v4u{pkh(tre[0], tim[0]), tag, pkh(tre[1], tim[1]), tag}, rws, to, tso);
    st16(v4u{pkh(tre[2], tim[2]), tag, pkh(tre[3], tim[3]), tag}, rws, w == 7 ? OOB : to, tso + 128 * 128);
  }
  STAMP(4)
  MEMB();
  PLO();
  const int arow = 16 * w3 + 4 * (r >> 2) + 2 * (r & 1) + 64 * ((r >> 1) & 1) + par;
  v4u fre[4], fim[4];
  {
    const float af = (float)arow * (1.0f / 128.0f);
#pragma unroll
    for (int g = 0; g < 4; ++g)
#pragma unroll
      for (int i = 0; i < 4; ++i) {
        const float a = (float)(16 * g + 4 * q4 + 2 * (i & 1) + (i >> 1)) * af;
        const float c = cosr(a), s = sinr(a);
        fre[g][i] = pkh(c, s); fim[g][i] = pkh(-s, c);
      }
#pragma unroll
    for (int g = 0; g < 4; ++g) asm volatile("" : "+v"(fre[g]), "+v"(fim[g]));
  }
  v4u wf[28];
  float cs0 = 0.f, cs1 = 0.f;
  const unsigned trb = L0 + (unsigned)((((lane >> 5) * 4 + ((lane >> 4) & 1)) * 1024) + ((lane >> 2) & 3) * 256 +
                                       (((2 * w + ((lane >> 1) & 1)) ^ (2 * (((lane >> 2) & 3) + 4 * ((lane >> 4) & 1)))) * 16) + (lane & 1) * 8);
#define W_READ(n0, n1)                                                                   \
  if (!SPARE) {                                                                          \
    unsigned t0 = trb, t1 = trb + 65536, t2 = trb + 131072;                              \
    asm volatile("" : "+v"(t0), "+v"(t1), "+v"(t2));                                     \
    _Pragma("unroll") for (int n_ = (n0); n_ < (n1); ++n_) {                             \
      const int sl = wslot(n_);                                                          \
      const unsigned a = (sl < 8 ? t0 : sl < 16 ? t1 : t2) + (sl & 7) * 8192;            \
      const v2u lo = ldtr(a), hi = ldtr(a + 2048);                                       \
      wf[n_] = v4u{lo.x, lo.y, hi.x, hi.y};                                              \
    }                                                                                    \
    _Pragma("unroll") for (int n_ = (n0); n_ < (n1); ++n_) asm volatile("" : "+v"(wf[n_])); \
  }
#define W_SUM(n0, n1)                                                                    \
  if (!SPARE) {                                                                          \
    _Pragma("unroll") for (int n_ = (n0); n_ < (n1); ++n_) {                             \
      cs0 = sum2(wf[n_][0], cs0); cs1 = sum2(wf[n_][1], cs1);                            \
      cs0 = sum2(wf[n_][2], cs0); cs1 = sum2(wf[n_][3], cs1);                            \
    }                                                                                    \
    asm volatile("" : "+v"(cs0), "+v"(cs1));                                             \
  }
  float fa_[4], fb_[4];
  asm volatile("s_waitcnt vmcnt(2)" : "+v"(fraw), "+v"(junk)::"memory");
  *(LAS v4u*)(size_t)(L0 + L_F + (w & 1) * 4096 + (w >> 1) * 1024 + lane * 16) = fraw;
  BAR();
  FLAG(0)
  W_READ(0, 7)
  {
    const unsigned fa = L0 + L_F + (unsigned)((16 * w3 + 4 * q4 + par) * 32 + ((kb <= 224 ? kb : 224) - fc0) * 2);
    float sc = SCALE_B * 0.125f;
    if (kb == 0 || kb == 224) sc = 0.5f * SCALE_B * 0.125f;
    if (kb > 224) sc = 0.f;
#pragma unroll
    for (int i = 0; i < 4; ++i) {
      fa_[i] = blo(*(LAS const unsigned short*)(size_t)(fa + (2 * (i & 1) + 64 * (i >> 1)) * 32)) * sc;
      fb_[i] = blo(*(LAS const unsigned short*)(size_t)(fa + (2 * (i & 1) + 64 * (i >> 1)) * 32 + 4096)) * sc;
    }
#pragma unroll
    for (int i = 0; i < 4; ++i) asm volatile("" : "+v"(fa_[i]), "+v"(fb_[i]));
  }
  BAR();
  STAMP(5)
  if (DLY_T) __builtin_amdgcn_s_sleep(DLY_T);
  const int dt = w < 7 ? w : 6;
  const int srow = 128 * r + s2;
  v4u ce[4], co[4], se[4], so[4];
  {
    const unsigned tso = (unsigned)((j1b * 16 + tileb) * 16384 + w * 1024);
    const unsigned tvo = tileb < 15 ? (unsigned)lane * 16 : OOB;
    v4u g0, g1;
    PHI();
    ald16<0>(g0, rws, tvo, tso); ald16<0>(g1, rws, tvo, tso + 8192);
    PLO();
    SB();
    {
#pragma unroll
      for (int i = 0; i < 4; ++i) {
        const float a = (float)((4 * q4 + i) * srow) * (1.0f / 2048.0f);
        const float c = 0.25f * cosr(a), s = 0.25f * sinr(a);
        mre[i] = pkh(c, -s); mim[i] = pkh(s, c);
      }
      const float df = (float)(2 * (16 * dt + r) + 1) * (1.0f / 896.0f);
      const float c2 = 2.0f * cosr(df);
      const f32x2 c22 = {c2, c2};
#pragma unroll
      for (int kc = 0; kc < 4; ++kc) {
        W_DMA(7 + kc, 8 + kc)
        const float a0 = (float)(64 * kc + 16 * q4) * df, a1 = a0 + df;
        f32x2 cs[16];
        cs[0] = f32x2{cosr(a0), sinr(a0)}; cs[1] = f32x2{cosr(a1), sinr(a1)};
#pragma unroll
        for (int e = 2; e < 16; ++e) cs[e] = __builtin_elementwise_fma(c22, cs[e - 1], -cs[e - 2]);
#pragma unroll
        for (int j = 0; j < 4; ++j) {
          ce[kc][j] = pkh(cs[4 * j].x, cs[4 * j + 2].x); co[kc][j] = pkh(cs[4 * j + 1].x, cs[4 * j + 3].x);
          se[kc][j] = pkh(cs[4 * j].y, cs[4 * j + 2].y); so[kc][j] = pkh(cs[4 * j + 1].y, cs[4 * j + 3].y);
        }
        asm volatile("" : "+v"(ce[kc]), "+v"(co[kc]), "+v"(se[kc]), "+v"(so[kc]));
        SB();
      }
      asm volatile("" : "+v"(mre), "+v"(mim));
    }
    STAMP(6)
    PHI();
    asm volatile("s_waitcnt vmcnt(4)" : "+v"(g0), "+v"(g1)::"memory");
    int spins = tileb < 15 ? 0 : SPIN_MAX;
    while (!__all(g0.y == tag && g0.w == tag && g1.y == tag && g1.w == tag) && ++spins <= SPIN_MAX) {
      ald16<0>(g0, rws, tvo, tso); ald16<0>(g1, rws, tvo, tso + 8192);
      asm volatile("s_waitcnt vmcnt(0)" : "+v"(g0), "+v"(g1)::"memory");
    }
    CNT(20, tileb < 15 ? spins : 0)
    const int m = 8 * w + (lane >> 3);
    const unsigned o = L0 + L_BT + (unsigned)(((m >> 4) * 64 + ((m >> 2) & 3) * 16 + 4 * (lane & 3) + ((lane >> 2) & 1)) * 16 + (((m >> 1) & 1) + 2 * (m & 1)) * 4);
    *(LAS unsigned*)(size_t)o = hadd(g0.x, g1.x);
    *(LAS unsigned*)(size_t)(o + 32) = hadd(g0.z, g1.z);
    *(LAS unsigned*)(size_t)(o + 4096) = hsub(g0.x, g1.x);
    *(LAS unsigned*)(size_t)(o + 4096 + 32) = hsub(g0.z, g1.z);
    W_DMA(11, 13)
    STAMP(7)
    BAR();
    f32x4 vre = {0, 0, 0, 0}, vim = {0, 0, 0, 0};
    {
      v4u tf[4];
#pragma unroll
      for (int g = 0; g < 4; ++g) tf[g] = lds16(L0 + L_BT + par * 4096 + g * 1024 + lane * 16);
      W_DMA(13, 14)
#pragma unroll
      for (int g = 0; g < 4; ++g) { vre = mfma16h(fre[g], tf[g], vre); vim = mfma16h(fim[g], tf[g], vim); }
    }
    W_DMA(14, 15)
    float pr[4], pi[4];
#pragma unroll
    for (int i = 0; i < 4; ++i) { pr[i] = vre[i] * fa_[i] - vim[i] * fb_[i]; pi[i] = -(vre[i] * fb_[i] + vim[i] * fa_[i]); }
    const unsigned po = L0 + L_BP + w3 * 1024 + lane * 16 + par * 8;
    *(LAS v2u*)(size_t)po = v2u{pkh(pr[0] + pr[2], pi[0] + pi[2]), pkh(pr[1] + pr[3], pi[1] + pi[3])};
    *(LAS v2u*)(size_t)(po + 4096) = v2u{pkh(pr[0] - pr[2], pi[0] - pi[2]), pkh(pr[1] - pr[3], pi[1] - pi[3])};
    W_DMA(15, 17)
    BAR();
    f32x4 ure = {0, 0, 0, 0}, uim = {0, 0, 0, 0};
    {
      v4u pf[4];
#pragma unroll
      for (int g = 0; g < 4; ++g) pf[g] = lds16(L0 + L_BP + par * 4096 + g * 1024 + lane * 16);
      W_DMA(17, 18)
#pragma unroll
      for (int g = 0; g < 4; ++g) { ure = mfma16h(pf[g], fre[g], ure); uim = mfma16h(pf[g], fim[g], uim); }
    }
    const unsigned uo = tileb < 15 ? (unsigned)(arow * 16384 + q4 * 16) : OOB, uso = U_OFF + (unsigned)((tileb * 16 + j1b) * 64);
    st16(v4u{pkh(ure[0] * 0.125f, uim[0] * -0.125f), pkh(ure[1] * 0.125f, uim[1] * -0.125f), pkh(ure[2] * 0.125f, uim[2] * -0.125f),
             pkh(ure[3] * 0.125f, uim[3] * -0.125f)}, rfx, uo, uso);
  }
  STAMP(8)
  MEMB();
  PLO();
  W_DMA(18, 23)
  v4u xv0, xv1;
  {
    const int lc = l32 < 28 ? l32 : 27, row = 2 * w + (lane >> 5);
    const unsigned o = L0 + L_X + (unsigned)((lc * 16 + (row ^ (lc & 15))) * 16);
    *(LAS v4u*)(size_t)o = v4u{xa[0].x, xa[0].y, xa[2].x, xa[2].y};
    *(LAS v4u*)(size_t)(o + 7168) = v4u{xa[1].x, xa[1].y, xa[3].x, xa[3].y};
    VMW(6);
    BAR();
    if (b == 0 && tid == 0) __builtin_amdgcn_raw_buffer_store_b32(tag, rfx, 0, EP_OFF, 16);
    W_READ(7, 18)
    const int lr = 4 * dt + q4;
    const unsigned o2 = L0 + L_X + (unsigned)((lr * 16 + (r ^ (lr & 15))) * 16);
    xv0 = lds16(o2); xv1 = lds16(o2 + 7168);
    asm volatile("" : "+v"(xv0), "+v"(xv1));
    BAR();
  }
  W_DMA(23, 28)
  const rsrc_t rd = mkrs(wd, F_ * D_ * 2);
  const unsigned dvoff = (unsigned)((lane >> 3) * 896 + (((lane & 7) ^ (4 * ((lane >> 4) & 1))) * 16));
  const unsigned dsoff = (unsigned)(224 * w * 896 + nE * 128);
  int erot = 64 + (nE + 2 * (yb & 3)) % 7;
  asm volatile("" : "+s"(erot));
#define ER(i) ((i) + erot >= 71 ? (i) + erot - 71 : (i) + erot - 64)
#define WD_DMA1(j)                                                                                     \
  {                                                                                                    \
    if (SPARE) { if ((j) < 7) ast16(sent4, rfx, tid * 16, clr0 + (28 + (j)) * 8192); }                 \
    else dma(rd, dvoff, dsoff + (32 * ER((j) >> 2) + 8 * (((j) >> 1) & 1) + 16 * ((j) & 1)) * 896, L0 + (dslot(j) * 8 + w) * 1024); \
  }
#define WD_DMA(j0, j1) _Pragma("unroll") for (int j_ = (j0); j_ < (j1); ++j_) WD_DMA1(j_)
  STAMP(9)
  if (DLY_U) __builtin_amdgcn_s_sleep(DLY_U);
  {
    v2u ug[4];
    unsigned fl;
    const unsigned uso = U_OFF + (unsigned)((s2 * 16 + 2 * w) * 1024);
    const unsigned uo = (w == 7 && r >= 8) ? OOB : (unsigned)((r >> 3) * 1024 + q4 * 256 + (r & 7) * 8);
#define U_LOAD()                                                                                       \
  ald4(fl, rfx, l32 * 16, FL_OFF);                                                                      \
  ald8<0>(ug[0], rfx, uo, uso); ald8<64>(ug[1], rfx, uo, uso); ald8<128>(ug[2], rfx, uo, uso); ald8<192>(ug[3], rfx, uo, uso);
    PHI();
    U_LOAD()
    PLO();
    WD_DMA(0, 2)
    W_SUM(0, 18)
    VMW(7);
    BAR();
    FLAG(1)
    W_READ(18, 28)
    STAMP(10)
    PHI();
    asm volatile("s_waitcnt vmcnt(2)" : "+v"(ug[0]), "+v"(ug[1]), "+v"(ug[2]), "+v"(ug[3]), "+v"(fl)::"memory");
    int spins = 0;
#define U_BAD (max4(v4u{ug[0].x, ug[0].y, ug[1].x, ug[1].y}, max4(v4u{ug[2].x, ug[2].y, ug[3].x, ug[3].y}, 0)) == SENT || fl != tag)
    while (__any(U_BAD) && ++spins <= SPIN_MAX) {
      U_LOAD()
      asm volatile("s_waitcnt vmcnt(0)" : "+v"(ug[0]), "+v"(ug[1]), "+v"(ug[2]), "+v"(ug[3]), "+v"(fl)::"memory");
    }
    CNT(21, spins)
    STAMP(11)
    const f32x4 z4 = {0, 0, 0, 0};
#pragma unroll
    for (int u = 0; u < 2; ++u) {
      const v4u uf = {ug[0][u], ug[1][u], ug[2][u], ug[3][u]};
      const f32x4 yre = mfma16h(uf, mre, z4), yim = mfma16h(uf, mim, z4);
      const int c = 2 * w + (q4 >> 1);
      const unsigned o = L0 + L_C + u * 8192 + (c * 16 + (r ^ (c & 7))) * 16 + (q4 & 1) * 8;
      *(LAS v2u*)(size_t)o = v2u{pkh(yre[0], yre[1]), pkh(yre[2], yre[3])};
      *(LAS v2u*)(size_t)(o + 4096) = v2u{pkh(yim[0], yim[1]), pkh(yim[2], yim[3])};
    }
    BAR();
    WD_DMA(2, 4)
    f32x4 Ce = z4, Co = z4, Se = z4, So = z4;
#pragma unroll
    for (int kc = 0; kc < 4; ++kc) {
      const int c = 4 * kc + q4;
      const unsigned o = L0 + L_C + (c * 16 + (r ^ (c & 7))) * 16;
      Ce = mfma16h(ce[kc], lds16(o), Ce);
      Se = mfma16h(se[kc], lds16(o + 4096), Se);
      Co = mfma16h(co[kc], lds16(o + 8192), Co);
      So = mfma16h(so[kc], lds16(o + 12288), So);
    }
    v4u hq[2];
#pragma unroll
    for (int g = 0; g < 2; ++g) {
      const int gg = g ^ q;
      const float sg = gg ? 1.0f : -1.0f;
      const v4u xs = gg ? v4u{__builtin_amdgcn_alignbit(xv1.y, xv1.y, 16), __builtin_amdgcn_alignbit(xv1.x, xv1.x, 16),
                              __builtin_amdgcn_alignbit(xv1.w, xv1.w, 16), __builtin_amdgcn_alignbit(xv1.z, xv1.z, 16)} : xv0;
      float lo[4], hi[4];
#pragma unroll
      for (int i = 0; i < 4; ++i) {
        const float a = __builtin_fmaf(sg, Se[i], Ce[i]), bb = __builtin_fmaf(sg, Co[i], So[i]);
        const float x0 = (i & 1) ? bhi(xs[i >> 1]) : blo(xs[i >> 1]), x1 = (i & 1) ? bhi(xs[2 + (i >> 1)]) : blo(xs[2 + (i >> 1)]);
        lo[i] = __builtin_fmaf(a - bb, SCALE_Y, x0); hi[i] = __builtin_fmaf(a + bb, SCALE_Y, x1);
      }
      hq[g] = v4u{pkb(lo[0], lo[1]), pkb(lo[2], lo[3]), pkb(hi[0], hi[1]), pkb(hi[2], hi[3])};
      if (g == 0) {
        const unsigned lo_ = w < 7 ? (unsigned)((q4 * 64 + r) * 16) : OOB;
        const unsigned lso = LH_OFF + (unsigned)((s2 >> 2) * LH_STR + ((((s2 >> 1) & 1) * 28 + 4 * dt) * 64 + 16 * (s2 & 1) + 32 * q) * 16);
        st16(hq[0], rfx, lo_, lso);
        MEMB();
        STAMP(12)
        SB();
      }
    }
    PLO();
    WD_DMA(4, 8)
    float sum = 0.f;
#pragma unroll
    for (int g = 0; g < 2; ++g)
#pragma unroll
      for (int i = 0; i < 4; ++i) sum += blo(hq[g][i]) + bhi(hq[g][i]);
    sum = xor32_add(xor16_add(sum));
    const float lm = sum * (1.0f / 64.0f);
    float m2 = 0.f;
#pragma unroll
    for (int g = 0; g < 2; ++g)
#pragma unroll
      for (int i = 0; i < 4; ++i) {
        const float d0_ = blo(hq[g][i]) - lm, d1_ = bhi(hq[g][i]) - lm;
        m2 = __builtin_fmaf(d0_, d0_, m2); m2 = __builtin_fmaf(d1_, d1_, m2);
      }
    m2 = xor32_add(xor16_add(m2));
    if (q4 == 0) *(LAS f32x2*)(size_t)(L0 + L_ST + (r * 8 + w) * 8) = f32x2{lm, m2};
    BAR();
    if (w == 7 && q == 0) {
      PHI();
      f32x4 sv[4];
#pragma unroll
      for (int j = 0; j < 4; ++j) sv[j] = *(LAS const f32x4*)(size_t)(L0 + L_ST + r * 64 + j * 16);
      const float mean = (((sv[0].x + sv[0].z) + (sv[1].x + sv[1].z)) + ((sv[2].x + sv[2].z) + sv[3].x)) * (1.0f / 7.0f);
      float M2 = 0.f;
#pragma unroll
      for (int j = 0; j < 7; ++j) {
        const float dlt = sv[j >> 1][2 * (j & 1)] - mean;
        M2 += __builtin_fmaf(64.0f * dlt, dlt, sv[j >> 1][2 * (j & 1) + 1]);
      }
      const float rs = __builtin_amdgcn_rsqf(M2 * (1.0f / 448.0f) + 1e-5f);
      const float m0 = -mean, m1 = m0 - trunc16(m0), m2_ = m1 - trunc16(m1);
      st16(v4u{hi16(m0, m0), hi16(m0, m1), hi16(m1, m2_), hi16(rs, rs - trunc16(rs))}, rfx, q4 == 0 ? (unsigned)r * 16 : OOB, SS_OFF + s2 * 256);
      MEMB();
      PLO();
    }
  }
  if (SPARE) {
    VMW(0);
    BAR();
    FLAG(2)
  } else {
    WD_DMA(8, 12)
    v4u wdf[7][4];
    const unsigned tdb = L0 + (unsigned)(w * 1024 + (q4 >> 1) * 8192 + (4 * (q4 & 1) + ((lane >> 2) & 3)) * 128 + ((lane >> 3) & 1) * 64 + (lane & 3) * 16);
    unsigned td0 = tdb, td1 = tdb ^ 64, td4 = tdb + 114688, td5 = (tdb ^ 64) + 114688;
    asm volatile("" : "+v"(td0), "+v"(td1), "+v"(td4), "+v"(td5));
#define WD_READ(u)                                                                                     \
  _Pragma("unroll") for (int nt = 0; nt < 4; ++nt) {                                                   \
    constexpr int sl_ = dslot(2 * (u));                                                                \
    const unsigned b_ = sl_ < 14 ? ((nt >> 1) ? td1 : td0) + sl_ * 8192 : ((nt >> 1) ? td5 : td4) + (sl_ - 14) * 8192; \
    const v2u t_ = ldtr(b_ + (nt & 1) * 8);                                                            \
    wdf[(u) >> 1][nt][2 * ((u) & 1)] = t_.x; wdf[(u) >> 1][nt][2 * ((u) & 1) + 1] = t_.y;              \
  }
    v4u csf;
    W_SUM(18, 28)
    {
      const float c0_ = xor32_add(cs0 + cs1), c1_ = c0_ - trunc16(c0_), c2_ = c1_ - trunc16(c1_);
      csf = v4u{hi16(c0_, c1_), hi16(c2_, c0_), hi16(c1_, c0_), 0u};
      if (lane >= 32) csf = v4u{0u, 0u, 0u, 0u};
      asm volatile("" : "+v"(csf));
    }
    VMW(4);
    WD_READ(0) WD_READ(1) WD_READ(2) WD_READ(3)
    BAR();
    STAMP(13)
    if (DLY_L) __builtin_amdgcn_s_sleep(DLY_L);
    const unsigned lnso = LH_OFF + (unsigned)(2 * mG) * LH_STR;
    const unsigned la = L0 + lane * 16;
#define LN_DMA(i)                                                                                      \
  {                                                                                                    \
    const int pi_ = 8 * (i) + w, tt_ = pi_ / 28, j_ = pi_ - 28 * tt_;                                  \
    dma1(rfx, lane * 16, lnso + ((i) >= 7 ? LH_STR - 57344 : 0) + (28 * tt_ + KROT(j_)) * 1024, L0 + pi_ * 1024); \
  }
#define LN_MAX(i0, i1) \
  _Pragma("unroll") for (int i_ = (i0); i_ < (i1); ++i_) mx = max4(lds16((i_ < 7 ? la : la + 57344) + w * 1024 + (i_ < 7 ? i_ : i_ - 7) * 8192), mx);
#define LN_FIX(i0, i1)                                                                                 \
  if (__any(mx == SENT)) {                                                                             \
    int spins = 0;                                                                                     \
    do {                                                                                               \
      _Pragma("unroll") for (int i_ = (i0); i_ < (i1); ++i_) LN_DMA(i_)                                \
      VMW(0);                                                                                          \
      mx = 0;                                                                                          \
      LN_MAX(i0, i1)                                                                                   \
      lspins += 1 << (8 * ((i0) / 3));                                                                 \
    } while (__any(mx == SENT) && ++spins < SPIN_MAX);                                                 \
  }
    unsigned mx = 0;
    int lspins = 0;
    PHI();
#pragma unroll
    for (int i = 0; i < 4; ++i) LN_DMA(i)
    PLO();
    {
      const unsigned pb = (unsigned)(28672 + (96 * nE + 12 * w + lane) * 128);
      ald4(junk, rfx, lane < 12 ? (pb >= 57344 ? pb + (LH_STR - 57344) : pb) : OOB, lnso);
    }
    VMW(1);
    LN_MAX(0, 4)
    LN_FIX(0, 4)
    BAR();
    STAMP(14)
    f32x16 acc, accp;
    v4u af[NAF], ho, stf;
    v4u hb[4][7];
    unsigned fl;
    float rsv = 0.f, rlv = 0.f;
    const unsigned hso = LH_OFF + (unsigned)((2 * mG + (b & 1)) * LH_STR + 57344 + (8 * nE + w) * 1024);
    const unsigned hvo = (unsigned)(q4 * 512 + pE * 256 + r * 16);
    const unsigned hlo = LH_OFF + (unsigned)((2 * mE + (w >> 2)) * LH_STR + 57344 + 14 * (w & 3) * 1024);
    const unsigned sso = SS_OFF + (unsigned)(mG * 2048);
#define H_LOAD(tt, i) hb[tt][i] = ld16(rfx, hvo, hlo + (tt) * 57344 + ER(i) * 2048);
#define H_TOUCH(tt) touch(junk, rfx, hvo, hlo + (tt) * 57344 + ER(0) * 2048);
#define LGK3() asm volatile("s_waitcnt lgkmcnt(3)" ::: "memory")
#define VMWS(n) asm volatile("s_waitcnt vmcnt(" #n ")" : "+v"(stf), "+v"(fl), "+v"(junk)::"memory")
#define FL_FIX(i)                                                                                      \
  if (__any(fl != tag)) {                                                                              \
    int spins = 0;                                                                                     \
    do {                                                                                               \
      ald4(fl, rfx, l32 * 16, FL_OFF + (i) * 512);                                                      \
      VMWS(0);                                                                                         \
      lspins += 1 << 24;                                                                               \
    } while (__any(fl != tag) && ++spins < SPIN_MAX);                                                  \
  }
    auto dpass = [&](auto TT) __attribute__((always_inline)) {
      constexpr int tt = decltype(TT)::value;
      unsigned lbt = la + tt * 28672;
      asm volatile("" : "+v"(lbt));
      __builtin_amdgcn_s_setprio(3);
#pragma unroll
      for (int i = 0; i < NAF - 1; ++i) af[i] = lds16(lbt + i * 1024);
#pragma unroll
      for (int i = 0; i < 16; ++i) acc[i] = 0.f;
      mx = 0;
      SB();
#pragma unroll
      for (int ks = 0; ks < 28; ++ks) {
        if (ks + NAF - 1 < 28) af[(ks + NAF - 1) % NAF] = lds16(lbt + (ks + NAF - 1) * 1024);
        acc = mfma32b(wf[ks], af[ks % NAF], acc);
        if (ks == (tt ? 9 : 0)) ald16<tt * 512>(stf, rfx, l32 * 16, sso);
        if (tt > 0) {
          if (ks < 8 && (ks & 1) == 1) {
            const int j = ks >> 1;
            ho[j] = glu2(accp[2 * j], accp[2 * j + 1], accp[8 + 2 * j], accp[8 + 2 * j + 1], rsv, rlv);
          }
          if (ks == 8) { st16(ho, rfx, lane * 16, hso + (tt - 1) * 57344); MEMB(); }
        }
        if (ks == 9) __builtin_amdgcn_s_setprio(2);
        if (ks == 19 && tt < 3) __builtin_amdgcn_s_setprio(1);
        if (tt == 0) {
          if (ks == 1) ald4(fl, rfx, l32 * 16, FL_OFF + 512);
          if (ks >= 2 && ks < 22 && (ks & 1) == 0) LN_DMA(3 + (ks >> 1))
          if (ks == 21) WD_DMA1(12)
          if (ks == 22) WD_DMA1(13)
          if (ks == 4) WD_READ(4)
          if (ks == 7) WD_READ(5)
          if (ks == 24) { VMWS(9); LN_MAX(4, 7) }
        }
        if (tt == 1) {
          if (ks < 8 && (ks & 1) == 1) WD_DMA1(14 + (ks >> 1))
          if (ks == 10 || ks == 12) WD_DMA1(18 + ((ks - 10) >> 1))
          if (ks == 14) { VMW(8); WD_READ(6) }
          if (ks == 16) LN_MAX(7, 10)
          if (ks == 18) LN_MAX(10, 14)
          if (ks == 22) { LGK3(); WD_DMA1(20) }
          if (ks == 24) WD_DMA1(21)
          if (ks == 26) VMWS(4);
        }
        if (tt == 2) {
          if (ks < 8 && (ks & 1) == 0) WD_DMA1(22 + (ks >> 1))
          if (ks == 9) WD_READ(7)
          if (ks == 13) { LGK3(); WD_DMA1(26) }
          if (ks == 14) WD_DMA1(27)
          if (ks == 15) H_TOUCH(0)
          if (ks == 26) VMWS(3);
        }
        if (tt == 3) {
          if (ks == 0) H_TOUCH(1)
          if (ks >= 2 && ks < 9) H_LOAD(0, ks - 2)
          if (ks == 9) ald4(fl, rfx, l32 * 16, FL_OFF + 1024);
          if (ks == 10) WD_READ(8)
          if (ks == 12) WD_READ(9)
          if (ks == 14) WD_READ(10)
          if (ks == 16) WD_READ(11)
          if (ks == 18) WD_READ(12)
          if (ks == 20) { VMW(12); WD_READ(13) }
          if (ks >= 21) H_LOAD(1, ks - 21)
          if (ks == 27) VMWS(7);
        }
        SB();
      }
      if (__any(max4(stf, 0) == SENT)) {
        int spins = 0;
        do {
          ald16<tt * 512>(stf, rfx, l32 * 16, sso);
          VMWS(0);
          lspins += 1 << 16;
        } while (__any(max4(stf, 0) == SENT) && ++spins < SPIN_MAX);
      }
      accp = mfma32b(csf, stf, acc);
      rsv = blo(stf.w) + bhi(stf.w);
      rlv = rsv * -1.44269504f;
      if (tt == 0) { LN_FIX(4, 7) FL_FIX(1) }
      if (tt == 1) LN_FIX(7, 14)
      if (tt == 3) FL_FIX(2)
      if (tt < 3) BAR();
      if (tt == 0) STAMP(15)
      if (tt == 2) STAMP(2)
    };
    dpass(IC<0>{}); dpass(IC<1>{}); dpass(IC<2>{}); dpass(IC<3>{});
    CNT(22, lspins)
    STAMP(16)
#pragma unroll
    for (int j = 0; j < 4; ++j) ho[j] = glu2(accp[2 * j], accp[2 * j + 1], accp[8 + 2 * j], accp[8 + 2 * j + 1], rsv, rlv);
    st16(ho, rfx, lane * 16, hso + 3 * 57344);
    __builtin_amdgcn_s_setprio(1);
#pragma unroll
    for (int i = 0; i < 7; ++i) H_LOAD(2, i)
    const int ttf = w >> 1, c0 = 64 * nE + 32 * (w & 1) + 8 * q4;
    v2u hr0, hr1;
    unsigned hro0, hro1, sel0, sel1;
    const unsigned hrs = LH_OFF + (unsigned)((2 * mE + (ttf >> 1)) * LH_STR + (ttf & 1) * 28672 + pE * 256);
    {
      const int seg = c0 / 112, hi = seg >> 1, mir = seg & 1, cc = c0 - 224 * hi;
      const int ksa = mir ? (220 - cc) >> 2 : cc >> 2;
      hro0 = (unsigned)((ksa * 64 + 32 * mir + r) * 16 + 8 * hi);
      hro1 = mir ? hro0 - 1024 : hro0 + 1024;
      sel0 = mir ? 0x05040706u : 0x03020100u; sel1 = mir ? 0x01000302u : 0x07060504u;
      hr0 = ld8(rfx, hro0, hrs); hr1 = ld8(rfx, hro1, hrs);
    }
    SB();
    STAMP(17)
    int espins = 0;
#pragma unroll
    for (int tt = 0; tt < 4; ++tt) {
      {
        mx = 0;
#pragma unroll
        for (int i = 0; i < 7; ++i) mx = max4(hb[tt][i], mx);
        if (__any(mx == SENT)) {
          int spins = 0;
          do {
            MEMB();
#pragma unroll
            for (int i = 0; i < 7; ++i) H_LOAD(tt, i)
            mx = 0;
#pragma unroll
            for (int i = 0; i < 7; ++i) mx = max4(hb[tt][i], mx);
            espins += 1 << (8 * tt);
          } while (__any(mx == SENT) && ++spins < SPIN_MAX);
        }
      }
      f32x4 ea[4];
#pragma unroll
      for (int nt = 0; nt < 4; ++nt) ea[nt] = f32x4{0, 0, 0, 0};
#pragma unroll
      for (int i = 0; i < 7; ++i)
#pragma unroll
        for (int nt = 0; nt < 4; ++nt) ea[nt] = mfma16b(wdf[i][nt], hb[tt][i], ea[nt]);
      SB();
      if (tt == 1) {
#pragma unroll
        for (int i = 0; i < 7; ++i) H_LOAD(3, i)
      }
      if (tt == 0) { BAR(); PLO(); }
#pragma unroll
      for (int nt = 0; nt < 4; ++nt) *(LAS f32x4*)(size_t)(la + w * 16384 + (tt * 4 + nt) * 1024) = ea[nt];
      SB();
    }
    CNT(23, espins)
    STAMP(18)
    if (__any(max(max(hr0.x, hr0.y), max(hr1.x, hr1.y)) == SENT)) {
      int spins = 0;
      do {
        MEMB();
        hr0 = ld8(rfx, hro0, hrs); hr1 = ld8(rfx, hro1, hrs);
      } while (__any(max(max(hr0.x, hr0.y), max(hr1.x, hr1.y)) == SENT) && ++spins < SPIN_MAX);
    }
    BAR();
    {
      f32x4 s0 = {0, 0, 0, 0}, s1 = {0, 0, 0, 0};
      unsigned ra = la + w * 2048, rb = ra + 65536;
      asm volatile("" : "+v"(rb));
#pragma unroll
      for (int ww = 0; ww < 8; ++ww) {
        const unsigned a = (ww < 4 ? ra : rb) + (ww & 3) * 16384;
        s0 += *(LAS const f32x4*)(size_t)a;
        s1 += *(LAS const f32x4*)(size_t)(a + 1024);
      }
      const unsigned f0 = pkb(s0[0], s0[1]), f1 = pkb(s0[2], s0[3]), f2 = pkb(s1[0], s1[1]), f3 = pkb(s1[2], s1[3]);
      hr0 = v2u{__builtin_amdgcn_perm(hr0.y, hr0.x, sel0), __builtin_amdgcn_perm(hr0.y, hr0.x, sel1)};
      hr1 = v2u{__builtin_amdgcn_perm(hr1.y, hr1.x, sel0), __builtin_amdgcn_perm(hr1.y, hr1.x, sel1)};
      v4u o;
      o[0] = pkb(blo(hr0.x) + blo(f0), bhi(hr0.x) + bhi(f0));
      o[1] = pkb(blo(hr0.y) + blo(f1), bhi(hr0.y) + bhi(f1));
      o[2] = pkb(blo(hr1.x) + blo(f2), bhi(hr1.x) + bhi(f2));
      o[3] = pkb(blo(hr1.y) + blo(f3), bhi(hr1.y) + bhi(f3));
      st16(o, mkrs(out, S_ * D_ * 2), (unsigned)(((128 * r + 8 * mE + 2 * ttf + pE) * D_ + c0) * 2), 0);
    }
  }
}

__global__ __launch_bounds__(512, 2) void fused(const uint16_t* __restrict__ x, const uint16_t* __restrict__ wr,
                                                const uint16_t* __restrict__ wi, const uint16_t* __restrict__ wg,
                                                const uint16_t* __restrict__ wu, const uint16_t* __restrict__ wd,
                                                unsigned char* __restrict__ ws, uint16_t* __restrict__ out) {
  __shared__ __attribute__((aligned(16))) unsigned char smem[163840];
  const unsigned L0 = (unsigned)(size_t)smem;
  if (blockIdx.x < 224) body<false>(x, wr, wi, wg, wu, wd, ws, out, L0);
  else body<true>(x, wr, wi, wg, wu, wd, ws, out, L0);
}

extern "C" void kernel_launch(void* const* d_in, const int* in_sizes, int n_in, void* d_out, int out_size, void* d_ws,
                              size_t ws_size, hipStream_t stream) {
  fused<<<256, 512, 0, stream>>>((const uint16_t*)d_in[0], (const uint16_t*)d_in[1], (const uint16_t*)d_in[2],
                                 (const uint16_t*)d_in[4], (const uint16_t*)d_in[5], (const uint16_t*)d_in[6],
                                 (unsigned char*)d_ws, (uint16_t*)d_out);
}
```

```cpp
#include <hip/hip_runtime.h>
#include <hip/hip_bf16.h>
#include <stdint.h>

typedef unsigned v4u __attribute__((ext_vector_type(4)));
typedef unsigned v2u __attribute__((ext_vector_type(2)));
typedef float f32x2 __attribute__((ext_vector_type(2)));
typedef float f32x4 __attribute__((ext_vector_type(4)));
typedef float f32x16 __attribute__((ext_vector_type(16)));
typedef _Float16 h8 __attribute__((ext_vector_type(8)));
typedef _Float16 h2 __attribute__((ext_vector_type(2)));
typedef __bf16 b8 __attribute__((ext_vector_type(8)));
typedef __bf16 b2 __attribute__((ext_vector_type(2)));
typedef short s4v __attribute__((ext_vector_type(4)));
typedef __amdgpu_buffer_rsrc_t rsrc_t;
#define DI __device__ __forceinline__
template <int N> struct IC { static constexpr int value = N; };
#define LAS __attribute__((address_space(3)))

constexpr int S_ = 2048, D_ = 448, F_ = 1792, DF_ = 225;
constexpr unsigned MB = 1u << 20;
constexpr unsigned U_OFF = 0;
constexpr unsigned EP_OFF = 2 * MB;
constexpr unsigned FL_OFF = 2 * MB + 4096;
constexpr unsigned SS_OFF = 2 * MB + 65536;
constexpr unsigned LH_OFF = 3 * MB;
constexpr unsigned LH_STR = 5 * 57344;
constexpr unsigned FIX_BYTES = 16 * MB;
constexpr unsigned REG_BYTES = 4 * MB;
#define NROT 8
constexpr unsigned OOB = 0x80000000u;
constexpr unsigned SENT = 0xffffffffu;
constexpr int SPIN_MAX = 20000;
constexpr unsigned L_SP = 17 * 8192;
constexpr unsigned L_A = L_SP, L_BT = L_SP + 8192, L_BP = L_SP + 16384, L_F = L_BP, L_X = L_SP, L_C = L_SP, L_ST = L_SP + 23552;
constexpr int wslot(int n) { return n < 17 ? n : n - 17; }
constexpr int dslot(int j) {
  return j < 2 ? 11 + j : j < 8 ? j - 2 : j < 14 ? j + 6 : j < 18 ? j : j < 20 ? j - 18 : j < 22 ? j - 2 : j < 26 ? j - 20 : j - 12;
}
constexpr int pslot(int tt, int nt) { return tt == 0 ? 6 + nt : tt == 1 ? nt : tt == 2 ? 14 + nt : nt < 2 ? 11 + nt : 16 + nt; }

#define EV 0
#define H2N 4
#define H3_AT 7
#define H3_ST 3
#define SBM 1
#define NAF 3
#define DLY_T 0
#define DLY_U 0
#define DLY_L 0

#define STAMP_TID 0
#define STAMP(i) {}
#define CNT(i, v)

#define STAMPF(i) STAMP(i)
#define STAMPE(i) {}
DI unsigned pkh(float a, float b) { h2 v = {(_Float16)a, (_Float16)b}; return __builtin_bit_cast(unsigned, v); }
DI unsigned pkb(float a, float b) { b2 v = {(__bf16)a, (__bf16)b}; return __builtin_bit_cast(unsigned, v); }
DI float blo(unsigned u) { return __builtin_bit_cast(float, u << 16); }
DI float bhi(unsigned u) { return __builtin_bit_cast(float, u & 0xffff0000u); }
DI float cosr(float x) { return __builtin_amdgcn_cosf(x); }
DI float sinr(float x) { return __builtin_amdgcn_sinf(x); }
DI f32x2 cis(float x) { return f32x2{cosr(x), sinr(x)}; }
DI f32x2 crot(f32x2 a, f32x2 b) {
  return __builtin_elementwise_fma(f32x2{a.y, a.y}, f32x2{-b.y, b.x}, f32x2{a.x, a.x} * b);
}
DI f32x2 cheb(float c2, f32x2 a1, f32x2 a0) { return __builtin_elementwise_fma(f32x2{c2, c2}, a1, -a0); }
DI unsigned hadd(unsigned a, unsigned b) { return __builtin_bit_cast(unsigned, __builtin_bit_cast(h2, a) + __builtin_bit_cast(h2, b)); }
DI unsigned hsub(unsigned a, unsigned b) { return __builtin_bit_cast(unsigned, __builtin_bit_cast(h2, a) - __builtin_bit_cast(h2, b)); }
DI f32x4 mfma16h(v4u a, v4u b, f32x4 c) {
  return __builtin_amdgcn_mfma_f32_16x16x32_f16(__builtin_bit_cast(h8, a), __builtin_bit_cast(h8, b), c, 0, 0, 0);
}
DI f32x4 mfma16b(v4u a, v4u b, f32x4 c) {
  return __builtin_amdgcn_mfma_f32_16x16x32_bf16(__builtin_bit_cast(b8, a), __builtin_bit_cast(b8, b), c, 0, 0, 0);
}
DI f32x16 mfma32b(v4u a, v4u b, f32x16 c) {
  return __builtin_amdgcn_mfma_f32_32x32x16_bf16(__builtin_bit_cast(b8, a), __builtin_bit_cast(b8, b), c, 0, 0, 0);
}
template <int CTRL> DI float dpp_add(float v) {
  return v + __builtin_bit_cast(float, __builtin_amdgcn_update_dpp(0, __builtin_bit_cast(int, v), CTRL, 0xf, 0xf, true));
}
DI float xor16_add(float v) {
  float a = v, b = v;
  asm("s_nop 1\n\tv_permlane16_swap_b32 %0, %1" : "+v"(a), "+v"(b));
  return a + b;
}
DI float xor32_add(float v) {
  float a = v, b = v;
  asm("s_nop 1\n\tv_permlane32_swap_b32 %0, %1" : "+v"(a), "+v"(b));
  return a + b;
}
DI float sum32(float v) {
  v = dpp_add<0xB1>(v); v = dpp_add<0x4E>(v); v = dpp_add<0x141>(v); v = dpp_add<0x140>(v);
  return xor16_add(v);
}
DI rsrc_t mkrs(const void* p, unsigned bytes) { return __builtin_amdgcn_make_buffer_rsrc((void*)p, (short)0, (int)bytes, 0x00020000); }
DI void dma(rsrc_t rs, unsigned voff, unsigned soff, unsigned lds) {
  asm volatile("s_mov_b32 m0, %0\n\ts_nop 0\n\tbuffer_load_dwordx4 %1, %2, %3 offen lds" ::"s"(lds), "v"(voff), "s"(rs), "s"(soff) : "memory");
}
DI void dma1(rsrc_t rs, unsigned voff, unsigned soff, unsigned lds) {
  asm volatile("s_mov_b32 m0, %0\n\ts_nop 0\n\tbuffer_load_dwordx4 %1, %2, %3 offen sc1 lds" ::"s"(lds), "v"(voff), "s"(rs), "s"(soff) : "memory");
}
template <int OFF> DI void ald8(v2u& d, rsrc_t rs, unsigned voff, unsigned soff) {
  asm volatile("buffer_load_dwordx2 %0, %1, %2, %3 offen offset:%4 sc1" : "=&v"(d) : "v"(voff), "s"(rs), "s"(soff), "n"(OFF) : "memory");
}
template <int OFF> DI void ald16(v4u& d, rsrc_t rs, unsigned voff, unsigned soff) {
  asm volatile("buffer_load_dwordx4 %0, %1, %2, %3 offen offset:%4 sc1" : "=&v"(d) : "v"(voff), "s"(rs), "s"(soff), "n"(OFF) : "memory");
}
DI v4u ld16(rsrc_t rs, unsigned voff, unsigned soff) { return __builtin_amdgcn_raw_buffer_load_b128(rs, voff, soff, 16); }
DI v2u ld8(rsrc_t rs, unsigned voff, unsigned soff) { return __builtin_amdgcn_raw_buffer_load_b64(rs, voff, soff, 16); }
DI void st16(v4u v, rsrc_t rs, unsigned voff, unsigned soff) { __builtin_amdgcn_raw_buffer_store_b128(v, rs, voff, soff, 16); }
DI v4u lds16(unsigned a) { return *(LAS const v4u*)(size_t)a; }
DI v2u ldtr(unsigned a) { return __builtin_bit_cast(v2u, __builtin_amdgcn_ds_read_tr16_b64_v4i16((LAS s4v*)(size_t)a)); }
DI unsigned max4(v4u v, unsigned m) { return max(max(max(max(m, v[0]), v[1]), v[2]), v[3]); }
#define BAR() asm volatile("s_waitcnt lgkmcnt(0)\n\ts_barrier" ::: "memory")
#define VMW(n) asm volatile("s_waitcnt vmcnt(" #n ")" ::: "memory")
#define SB() __builtin_amdgcn_sched_barrier(0)
#define MEMB() asm volatile("" ::: "memory")
#define PLO() __builtin_amdgcn_s_setprio(0)
#define PHI() __builtin_amdgcn_s_setprio(2)

constexpr float SCALE_B = 1.0f / 16.0f;
constexpr float SCALE_Y = 512.0f / (917504.0f * SCALE_B);

DI unsigned glu2(float g0, float g1, float u0, float u1, float rs, float rl) {
  const float s0 = __builtin_amdgcn_rcpf(1.0f + __builtin_amdgcn_exp2f(g0 * rl));
  const float s1 = __builtin_amdgcn_rcpf(1.0f + __builtin_amdgcn_exp2f(g1 * rl));
  return pkb(s0 * (u0 * rs), s1 * (u1 * rs));
}
DI void ald4(unsigned& d, rsrc_t rs, unsigned voff, unsigned soff) {
  asm volatile("buffer_load_dword %0, %1, %2, %3 offen sc1" : "=&v"(d) : "v"(voff), "s"(rs), "s"(soff) : "memory");
}
DI void touch(unsigned& d, rsrc_t rs, unsigned voff, unsigned soff) {
  asm volatile("buffer_load_dword %0, %1, %2, %3 offen sc1" : "+v"(d) : "v"(voff), "s"(rs), "s"(soff) : "memory");
}
DI float sum2(unsigned u, float c) {
  const b2 one2 = {(__bf16)1.0f, (__bf16)1.0f};
  return __builtin_amdgcn_fdot2_f32_bf16(__builtin_bit_cast(b2, u), one2, c, false);
}
DI void ast16(v4u v, rsrc_t rs, unsigned voff, unsigned soff) {
  asm volatile("buffer_store_dwordx4 %0, %1, %2, %3 offen sc1" ::"v"(v), "v"(voff), "s"(rs), "s"(soff) : "memory");
}
DI float trunc16(float v) { return bhi(__builtin_bit_cast(unsigned, v)); }
DI unsigned hi16(float a, float b) { return __builtin_amdgcn_perm(__builtin_bit_cast(unsigned, b), __builtin_bit_cast(unsigned, a), 0x07060302u); }

template <bool SPARE>
DI void body(const uint16_t* __restrict__ x, const uint16_t* __restrict__ wr, const uint16_t* __restrict__ wi,
             const uint16_t* __restrict__ wg, const uint16_t* __restrict__ wu, const uint16_t* __restrict__ wd,
             unsigned char* __restrict__ ws, uint16_t* __restrict__ out, const unsigned L0) {
  STAMP(0)
  const int b = blockIdx.x, tid = threadIdx.x, lane = tid & 63;
  const int w = __builtin_amdgcn_readfirstlane(tid >> 6);
  const int r = lane & 15, q4 = lane >> 4;
  const int s2 = (b & 7) + 8 * (b >> 4), q = (b >> 3) & 1;
  const int j1b = b & 15, tileb = b >> 4;
  const int yb = b >> 3;
  const int mG = 4 * ((b >> 1) & 3) + (yb & 3), nD = 7 * (b & 1) + (yb >> 2);
  const int mE = 2 * (b & 7) + (yb & 1), pE = (yb >> 1) & 1, nE = yb >> 2;
  const int sbk = 4 * (b & 7) + (yb & 3);
  const rsrc_t rx = mkrs(x, S_ * D_ * 2), rfx = mkrs(ws, FIX_BYTES);
  PHI();

  v2u xa[4];
  unsigned ep;
  const int l32 = lane & 31;
  {
    const int lc = l32 < 28 ? l32 : 27;
    const unsigned rowo = (unsigned)(128 * (2 * w + (lane >> 5)) + s2) * 896u;
    const unsigned o1 = rowo + 8 * lc, o2 = rowo + 440 - 8 * lc;
    asm volatile("buffer_load_dwordx2 %0, %5, %7, 0 offen\n\t"
                 "buffer_load_dwordx2 %1, %6, %7, 0 offen\n\t"
                 "buffer_load_dwordx2 %2, %5, %7, 0 offen offset:448\n\t"
                 "buffer_load_dwordx2 %3, %6, %7, 0 offen offset:448\n\t"
                 "s_load_dword %4, %8, 0"
                 : "=&v"(xa[0]), "=&v"(xa[1]), "=&v"(xa[2]), "=&v"(xa[3]), "=&s"(ep)
                 : "v"(o1), "v"(o2), "s"(rx), "s"(ws + EP_OFF)
                 : "memory");
  }
  PLO();
  const v4u sent4 = {SENT, SENT, SENT, SENT};
  unsigned junk;
  {
    const int i5 = (lane * 52) >> 8, l5 = lane - 5 * i5;
    const unsigned tf = lane < 40 ? (unsigned)((((b & 7) + 8 * (8 * yb + i5)) * 450 + (l5 < 4 ? 128 * l5 : 446)) & ~3) : OOB;
    const unsigned td = lane < 49 ? (unsigned)(49 * b + lane) * 128 : OOB;
    ald4(junk, mkrs(w == 0 ? wr : w == 1 ? wi : wd, w < 2 ? S_ * DF_ * 2 : F_ * D_ * 2), w < 2 ? tf : w == 2 ? td : OOB, 0);
    ast16(sent4, rfx, tid * 16, U_OFF + (2 * s2 + q) * 8192);
    if (SPARE) ast16(sent4, rfx, lane < 8 ? (unsigned)(w * 8 + lane) * 16 : OOB, SS_OFF + sbk * 1024);
  }
  const int wmat = w & 1, wrange = w >> 1;
  const rsrc_t rw = mkrs(wmat ? wu : wg, D_ * F_ * 2);
  const unsigned wvoff = (unsigned)(((wrange >> 1) ? 3 - q4 : q4) * 3584 + ((r ^ (2 * (q4 + 4 * wmat))) * 16));
  const unsigned wsoff0 = (unsigned)((224 * (wrange & 1) + ((wrange >> 1) ? 220 : 0)) * 3584 + nD * 256);
  const int wstep = (wrange >> 1) ? -4 * 3584 : 4 * 3584;
  int rot = 64 + 7 * (yb & 3) + ((7 * ((b >> 1) & 3)) >> 2);
  asm volatile("" : "+s"(rot));
  const unsigned clr0 = LH_OFF + (unsigned)sbk * LH_STR;
#define KROT(j) ((j) + rot >= 92 ? (j) + rot - 92 : (j) + rot - 64)
#define W_DMA(n0, n1)                                                                                  \
  _Pragma("unroll") for (int n_ = (n0); n_ < (n1); ++n_) {                                             \
    if (SPARE) ast16(sent4, rfx, tid * 16, clr0 + n_ * 8192);                                          \
    else dma(rw, wvoff, wsoff0 + KROT(n_) * wstep, L0 + (wslot(n_) * 8 + w) * 1024);                   \
  }
#define FLAG(i) if (SPARE && tid == 0) __builtin_amdgcn_raw_buffer_store_b32(tag, rfx, sbk * 16, FL_OFF + (i) * 512, 16);
  const int par = w >> 2, w3 = w & 3;
  const int kb = 16 * tileb + r;
  const int fc0 = tileb < 14 ? 16 * tileb : 209;
  SB();
  v4u bc[4], bs[4], mre, mim;
  {
    const int k = 2 * (16 * w + 8 * ((r >> 1) & 1) + 2 * (r >> 2) + (r & 1)) + q;
    const float kf = (float)k * (1.0f / 896.0f);
    const f32x2 st = cis(-2.0f * kf), rk = cis(-64.0f * kf);
    const float c2 = 2.0f * st.x;
    f32x2 z0 = cis((float)(-16 * q4 - 1) * kf), z1 = crot(z0, st);
#pragma unroll
    for (int kc = 0; kc < 4; ++kc) {
      f32x2 cs[8];
      cs[0] = z0; cs[1] = z1;
      if (kc < 3) { z0 = crot(z0, rk); z1 = crot(z1, rk); }
#pragma unroll
      for (int e = 2; e < 8; ++e) cs[e] = cheb(c2, cs[e - 1], cs[e - 2]);
#pragma unroll
      for (int j = 0; j < 4; ++j) { bc[kc][j] = pkh(cs[2 * j].x, cs[2 * j + 1].x); bs[kc][j] = pkh(cs[2 * j].y, cs[2 * j + 1].y); }
    }
    f32x2 m = cis((float)(r * (512 * q4 + s2)) * (1.0f / 2048.0f));
    const f32x2 ms = cis((float)r * (1.0f / 16.0f));
#pragma unroll
    for (int i = 0; i < 4; ++i) {
      mre[i] = pkh(m.x, m.y); mim[i] = pkh(-m.y, m.x);
      if (i < 3) m = crot(m, ms);
    }
#pragma unroll
    for (int kc = 0; kc < 4; ++kc) asm volatile("" : "+v"(bc[kc]), "+v"(bs[kc]));
    asm volatile("" : "+v"(mre), "+v"(mim));
  }
  W_DMA(0, 4)
  PHI();
  STAMP(1)
  asm volatile("s_waitcnt vmcnt(6) lgkmcnt(0)" : "+v"(xa[0]), "+v"(xa[1]), "+v"(xa[2]), "+v"(xa[3]), "+s"(ep)::"memory");
  const unsigned tag = ep * 1664525u + 1013904223u;
  const rsrc_t rws = mkrs(ws + FIX_BYTES + (size_t)(tag & (NROT - 1)) * REG_BYTES, REG_BYTES);
  v4u fraw;
  {
    const unsigned c0 = __builtin_amdgcn_readlane(xa[0].x, 0), c1 = __builtin_amdgcn_readlane(xa[0].x, 32);
    const float cc = 2.0f * blo(lane < 32 ? c0 : c1);
    float s1[4], sb[4], d1[4], d2[4];
    float sum = 0.f, sq = 0.f;
#pragma unroll
    for (int i = 0; i < 4; ++i) {
      const unsigned ua = xa[0][i >> 1], ub = xa[1][1 - (i >> 1)], ue = xa[2][i >> 1], uf = xa[3][1 - (i >> 1)];
      const float a = (i & 1) ? bhi(ua) : blo(ua), e = (i & 1) ? bhi(ue) : blo(ue);
      const float bb = (i & 1) ? blo(ub) : bhi(ub), f = (i & 1) ? blo(uf) : bhi(uf);
      s1[i] = (a + f) - cc; sb[i] = (bb + e) - cc; d1[i] = a - f; d2[i] = e - bb;
      sum += s1[i] + sb[i];
      sq = __builtin_fmaf(s1[i], s1[i], sq); sq = __builtin_fmaf(sb[i], sb[i], sq);
      sq = __builtin_fmaf(d1[i], d1[i], sq); sq = __builtin_fmaf(d2[i], d2[i], sq);
    }
    if (l32 >= 28) { sum = 0.f; sq = 0.f; }
    asm volatile("" : "+v"(sum), "+v"(sq));
    W_DMA(4, 5)
    sum = sum32(sum); sq = sum32(sq);
    asm volatile("" : "+v"(sum), "+v"(sq));
    W_DMA(5, 6)
    const float mu = sum * (1.0f / 448.0f);
    const float var = __builtin_fmaxf(__builtin_fmaf(-mu, mu, sq * (0.5f / 448.0f)), 0.f);
    float rs = __builtin_amdgcn_rsqf(var + 1e-5f);
    if (l32 >= 28) rs = 0.f;
    const float sgn = q ? -1.0f : 1.0f;
    const float off = q ? 0.f : -4.0f * mu * rs;
    float P[4], Q[4];
#pragma unroll
    for (int i = 0; i < 4; ++i) {
      P[i] = __builtin_fmaf(__builtin_fmaf(sgn, sb[i], s1[i]), rs, off);
      Q[i] = __builtin_fmaf(sgn, d2[i], d1[i]) * rs;
    }
    const int c = l32 >> 1, row = 2 * w + (lane >> 5);
    const unsigned o = L0 + L_A + (c * 16 + (row ^ (c & 7))) * 16 + (l32 & 1) * 8;
    *(LAS v2u*)(size_t)o = v2u{pkh(P[0], P[1]), pkh(P[2], P[3])};
    *(LAS v2u*)(size_t)(o + 4096) = v2u{pkh(Q[0], Q[1]), pkh(Q[2], Q[3])};
  }
  W_DMA(6, 7)
  {
    const rsrc_t rf = mkrs((w & 1) ? wi : wr, S_ * DF_ * 2);
    const unsigned fo = (unsigned)(((j1b + 16 * (32 * (w >> 1) + (lane >> 1))) * DF_ + fc0 + 8 * (lane & 1)) * 2);
    asm volatile("buffer_load_dwordx4 %0, %1, %2, 0 offen" : "=&v"(fraw) : "v"(fo), "s"(rf) : "memory");
  }
  VMW(8);
  BAR();
  STAMPF(3)
  {
    f32x4 zre = {0, 0, 0, 0}, zim = {0, 0, 0, 0};
#pragma unroll
    for (int kc = 0; kc < 4; ++kc) {
      const int c = 4 * kc + q4;
      const unsigned o = L0 + L_A + (c * 16 + (r ^ (c & 7))) * 16;
      zre = mfma16h(lds16(o), bc[kc], zre);
      zim = mfma16h(lds16(o + 4096), bs[kc], zim);
    }
    v4u zop;
#pragma unroll
    for (int i = 0; i < 4; ++i) zop[i] = pkh(zre[i], zim[i]);
    const f32x4 z4 = {0, 0, 0, 0};
    const f32x4 tre = mfma16h(zop, mre, z4), tim = mfma16h(zop, mim, z4);
    const unsigned to = (unsigned)(r * 16 * 128 * 128 + q4 * 16);
    const unsigned tso = (unsigned)((2 * w * 128 + s2) * 128 + q * 64);
    st16(v4u{pkh(tre[0], tim[0]), tag, pkh(tre[1], tim[1]), tag}, rws, to, tso);
    st16(v4u{pkh(tre[2], tim[2]), tag, pkh(tre[3], tim[3]), tag}, rws, w == 7 ? OOB : to, tso + 128 * 128);
  }
  STAMPF(4)
  MEMB();
  PLO();
  const int arow = 16 * w3 + 4 * (r >> 2) + 2 * (r & 1) + 64 * ((r >> 1) & 1) + par;
  v4u fre[4], fim[4];
  {
    const float af = (float)arow * (1.0f / 128.0f);
    const f32x2 s1 = cis(af), s16 = cis(16.0f * af);
    f32x2 z[4][4];
    z[0][0] = cis((float)(4 * q4) * af);
    z[0][1] = crot(z[0][0], s1);
    z[0][2] = cheb(2.0f * s1.x, z[0][1], z[0][0]);
    z[0][3] = cheb(2.0f * s1.x, z[0][2], z[0][1]);
#pragma unroll
    for (int e = 0; e < 4; ++e) {
      z[1][e] = crot(z[0][e], s16);
      z[2][e] = cheb(2.0f * s16.x, z[1][e], z[0][e]);
      z[3][e] = cheb(2.0f * s16.x, z[2][e], z[1][e]);
    }
#pragma unroll
    for (int g = 0; g < 4; ++g)
#pragma unroll
      for (int i = 0; i < 4; ++i) {
        const f32x2 v = z[g][2 * (i & 1) + (i >> 1)];
        fre[g][i] = pkh(v.x, v.y); fim[g][i] = pkh(-v.y, v.x);
      }
#pragma unroll
    for (int g = 0; g < 4; ++g) asm volatile("" : "+v"(fre[g]), "+v"(fim[g]));
  }
  v4u wf[28];
  float cs0 = 0.f, cs1 = 0.f;
  const unsigned trb = L0 + (unsigned)((((lane >> 5) * 4 + ((lane >> 4) & 1)) * 1024) + ((lane >> 2) & 3) * 256 +
                                       (((2 * w + ((lane >> 1) & 1)) ^ (2 * (((lane >> 2) & 3) + 4 * ((lane >> 4) & 1)))) * 16) + (lane & 1) * 8);
#define W_READ(n0, n1)                                                                   \
  if (!SPARE) {                                                                          \
    unsigned t0 = trb, t1 = trb + 65536, t2 = trb + 131072;                              \
    asm volatile("" : "+v"(t0), "+v"(t1), "+v"(t2));                                     \
    _Pragma("unroll") for (int n_ = (n0); n_ < (n1); ++n_) {                             \
      const int sl = wslot(n_);                                                          \
      const unsigned a = (sl < 8 ? t0 : sl < 16 ? t1 : t2) + (sl & 7) * 8192;            \
      const v2u lo = ldtr(a), hi = ldtr(a + 2048);                                       \
      wf[n_] = v4u{lo.x, lo.y, hi.x, hi.y};                                              \
    }                                                                                    \
    _Pragma("unroll") for (int n_ = (n0); n_ < (n1); ++n_) asm volatile("" : "+v"(wf[n_])); \
  }
#define W_SUM(n0, n1)                                                                    \
  if (!SPARE) {                                                                          \
    _Pragma("unroll") for (int n_ = (n0); n_ < (n1); ++n_) {                             \
      cs0 = sum2(wf[n_][0], cs0); cs1 = sum2(wf[n_][1], cs1);                            \
      cs0 = sum2(wf[n_][2], cs0); cs1 = sum2(wf[n_][3], cs1);                            \
    }                                                                                    \
    asm volatile("" : "+v"(cs0), "+v"(cs1));                                             \
  }
  float fa_[4], fb_[4];
  asm volatile("s_waitcnt vmcnt(2)" : "+v"(fraw), "+v"(junk)::"memory");
  *(LAS v4u*)(size_t)(L0 + L_F + (w & 1) * 4096 + (w >> 1) * 1024 + lane * 16) = fraw;
  BAR();
  FLAG(0)
  W_READ(0, 7)
  {
    const unsigned fa = L0 + L_F + (unsigned)((16 * w3 + 4 * q4 + par) * 32 + ((kb <= 224 ? kb : 224) - fc0) * 2);
    float sc = SCALE_B * 0.125f;
    if (kb == 0 || kb == 224) sc = 0.5f * SCALE_B * 0.125f;
    if (kb > 224) sc = 0.f;
#pragma unroll
    for (int i = 0; i < 4; ++i) {
      fa_[i] = blo(*(LAS const unsigned short*)(size_t)(fa + (2 * (i & 1) + 64 * (i >> 1)) * 32)) * sc;
      fb_[i] = blo(*(LAS const unsigned short*)(size_t)(fa + (2 * (i & 1) + 64 * (i >> 1)) * 32 + 4096)) * sc;
    }
#pragma unroll
    for (int i = 0; i < 4; ++i) asm volatile("" : "+v"(fa_[i]), "+v"(fb_[i]));
  }
  BAR();
  STAMPF(5)
  if (DLY_T) __builtin_amdgcn_s_sleep(DLY_T);
  const int dt = w < 7 ? w : 6;
  const int srow = 128 * r + s2;
  v4u ce[4], co[4], se[4], so[4];
  {
    const unsigned tso = (unsigned)((j1b * 16 + tileb) * 16384 + w * 1024);
    const unsigned tvo = tileb < 15 ? (unsigned)lane * 16 : OOB;
    v4u g0, g1;
    PHI();
    ald16<0>(g0, rws, tvo, tso); ald16<0>(g1, rws, tvo, tso + 8192);
    PLO();
    SB();
    {
      f32x2 m = 0.25f * cis((float)(4 * q4 * srow) * (1.0f / 2048.0f));
      const f32x2 ms = cis((float)srow * (1.0f / 2048.0f));
#pragma unroll
      for (int i = 0; i < 4; ++i) {
        mre[i] = pkh(m.x, -m.y); mim[i] = pkh(m.y, m.x);
        if (i < 3) m = crot(m, ms);
      }
      const float df = (float)(2 * (16 * dt + r) + 1) * (1.0f / 896.0f);
      const f32x2 st = cis(df), rk = cis(64.0f * df);
      const float c2 = 2.0f * st.x;
      f32x2 z0 = cis((float)(16 * q4) * df), z1 = crot(z0, st);
#pragma unroll
      for (int kc = 0; kc < 4; ++kc) {
        W_DMA(7 + kc, 8 + kc)
        f32x2 cs[16];
        cs[0] = z0; cs[1] = z1;
        if (kc < 3) { z0 = crot(z0, rk); z1 = crot(z1, rk); }
#pragma unroll
        for (int e = 2; e < 16; ++e) cs[e] = cheb(c2, cs[e - 1], cs[e - 2]);
#pragma unroll
        for (int j = 0; j < 4; ++j) {
          ce[kc][j] = pkh(cs[4 * j].x, cs[4 * j + 2].x); co[kc][j] = pkh(cs[4 * j + 1].x, cs[4 * j + 3].x);
          se[kc][j] = pkh(cs[4 * j].y, cs[4 * j + 2].y); so[kc][j] = pkh(cs[4 * j + 1].y, cs[4 * j + 3].y);
        }
        asm volatile("" : "+v"(ce[kc]), "+v"(co[kc]), "+v"(se[kc]), "+v"(so[kc]));
        SB();
      }
      asm volatile("" : "+v"(mre), "+v"(mim));
    }
    STAMPF(6)
    PHI();
    asm volatile("s_waitcnt vmcnt(4)" : "+v"(g0), "+v"(g1)::"memory");
    int spins = tileb < 15 ? 0 : SPIN_MAX;
    while (!__all(g0.y == tag && g0.w == tag && g1.y == tag && g1.w == tag) && ++spins <= SPIN_MAX) {
      ald16<0>(g0, rws, tvo, tso); ald16<0>(g1, rws, tvo, tso + 8192);
      asm volatile("s_waitcnt vmcnt(0)" : "+v"(g0), "+v"(g1)::"memory");
    }
    CNT(20, tileb < 15 ? spins : 0)
    const int m = 8 * w + (lane >> 3);
    const unsigned o = L0 + L_BT + (unsigned)(((m >> 4) * 64 + ((m >> 2) & 3) * 16 + 4 * (lane & 3) + ((lane >> 2) & 1)) * 16 + (((m >> 1) & 1) + 2 * (m & 1)) * 4);
    *(LAS unsigned*)(size_t)o = hadd(g0.x, g1.x);
    *(LAS unsigned*)(size_t)(o + 32) = hadd(g0.z, g1.z);
    *(LAS unsigned*)(size_t)(o + 4096) = hsub(g0.x, g1.x);
    *(LAS unsigned*)(size_t)(o + 4096 + 32) = hsub(g0.z, g1.z);
    W_DMA(11, 13)
    STAMP(7)
    BAR();
    f32x4 vre = {0, 0, 0, 0}, vim = {0, 0, 0, 0};
    {
      v4u tf[4];
#pragma unroll
      for (int g = 0; g < 4; ++g) tf[g] = lds16(L0 + L_BT + par * 4096 + g * 1024 + lane * 16);
      W_DMA(13, 14)
#pragma unroll
      for (int g = 0; g < 4; ++g) { vre = mfma16h(fre[g], tf[g], vre); vim = mfma16h(fim[g], tf[g], vim); }
    }
    W_DMA(14, 15)
    float pr[4], pi[4];
#pragma unroll
    for (int i = 0; i < 4; ++i) { pr[i] = vre[i] * fa_[i] - vim[i] * fb_[i]; pi[i] = -(vre[i] * fb_[i] + vim[i] * fa_[i]); }
    const unsigned po = L0 + L_BP + w3 * 1024 + lane * 16 + par * 8;
    *(LAS v2u*)(size_t)po = v2u{pkh(pr[0] + pr[2], pi[0] + pi[2]), pkh(pr[1] + pr[3], pi[1] + pi[3])};
    *(LAS v2u*)(size_t)(po + 4096) = v2u{pkh(pr[0] - pr[2], pi[0] - pi[2]), pkh(pr[1] - pr[3], pi[1] - pi[3])};
    W_DMA(15, 17)
    BAR();
    f32x4 ure = {0, 0, 0, 0}, uim = {0, 0, 0, 0};
    {
      v4u pf[4];
#pragma unroll
      for (int g = 0; g < 4; ++g) pf[g] = lds16(L0 + L_BP + par * 4096 + g * 1024 + lane * 16);
      W_DMA(17, 18)
#pragma unroll
      for (int g = 0; g < 4; ++g) { ure = mfma16h(pf[g], fre[g], ure); uim = mfma16h(pf[g], fim[g], uim); }
    }
    const unsigned uo = tileb < 15 ? (unsigned)(arow * 16384 + q4 * 16) : OOB, uso = U_OFF + (unsigned)((tileb * 16 + j1b) * 64);
    st16(v4u{pkh(ure[0] * 0.125f, uim[0] * -0.125f), pkh(ure[1] * 0.125f, uim[1] * -0.125f), pkh(ure[2] * 0.125f, uim[2] * -0.125f),
             pkh(ure[3] * 0.125f, uim[3] * -0.125f)}, rfx, uo, uso);
  }
  STAMP(8)
  MEMB();
  PLO();
  W_DMA(18, 23)
  v4u xv0, xv1;
  {
    const int lc = l32 < 28 ? l32 : 27, row = 2 * w + (lane >> 5);
    const unsigned o = L0 + L_X + (unsigned)((lc * 16 + (row ^ (lc & 15))) * 16);
    *(LAS v4u*)(size_t)o = v4u{xa[0].x, xa[0].y, xa[2].x, xa[2].y};
    *(LAS v4u*)(size_t)(o + 7168) = v4u{xa[1].x, xa[1].y, xa[3].x, xa[3].y};
    VMW(6);
    BAR();
    if (b == 0 && tid == 0) __builtin_amdgcn_raw_buffer_store_b32(tag, rfx, 0, EP_OFF, 16);
    W_READ(7, 18)
    const int lr = 4 * dt + q4;
    const unsigned o2 = L0 + L_X + (unsigned)((lr * 16 + (r ^ (lr & 15))) * 16);
    xv0 = lds16(o2); xv1 = lds16(o2 + 7168);
    asm volatile("" : "+v"(xv0), "+v"(xv1));
    BAR();
  }
  W_DMA(23, 28)
  const rsrc_t rd = mkrs(wd, F_ * D_ * 2);
  const unsigned dvoff = (unsigned)((lane >> 3) * 896 + (((lane & 7) ^ (4 * ((lane >> 4) & 1))) * 16));
  const unsigned dsoff = (unsigned)(224 * w * 896 + nE * 128);
  int erot = 64 + (nE + 2 * (yb & 3)) % 7;
  asm volatile("" : "+s"(erot));
#define ER(i) ((i) + erot >= 71 ? (i) + erot - 71 : (i) + erot - 64)
#define WD_DMA1(j)                                                                                     \
  {                                                                                                    \
    if (SPARE) { if ((j) < 7) ast16(sent4, rfx, tid * 16, clr0 + (28 + (j)) * 8192); }                 \
    else dma(rd, dvoff, dsoff + (32 * ER((j) >> 2) + 8 * (((j) >> 1) & 1) + 16 * ((j) & 1)) * 896, L0 + (dslot(j) * 8 + w) * 1024); \
  }
#define WD_DMA(j0, j1) _Pragma("unroll") for (int j_ = (j0); j_ < (j1); ++j_) WD_DMA1(j_)
  STAMP(9)
  if (DLY_U) __builtin_amdgcn_s_sleep(DLY_U);
  {
    v2u ug[4];
    unsigned fl;
    const unsigned uso = U_OFF + (unsigned)((s2 * 16 + 2 * w) * 1024);
    const unsigned uo = (w == 7 && r >= 8) ? OOB : (unsigned)((r >> 3) * 1024 + q4 * 256 + (r & 7) * 8);
#define U_LOAD()                                                                                       \
  ald4(fl, rfx, l32 * 16, FL_OFF);                                                                      \
  ald8<0>(ug[0], rfx, uo, uso); ald8<64>(ug[1], rfx, uo, uso); ald8<128>(ug[2], rfx, uo, uso); ald8<192>(ug[3], rfx, uo, uso);
    PHI();
    U_LOAD()
    PLO();
    WD_DMA(0, 2)
    W_SUM(0, 18)
    VMW(7);
    BAR();
    FLAG(1)
    W_READ(18, 28)
    STAMP(10)
    PHI();
    asm volatile("s_waitcnt vmcnt(2)" : "+v"(ug[0]), "+v"(ug[1]), "+v"(ug[2]), "+v"(ug[3]), "+v"(fl)::"memory");
    int spins = 0;
#define U_BAD (max4(v4u{ug[0].x, ug[0].y, ug[1].x, ug[1].y}, max4(v4u{ug[2].x, ug[2].y, ug[3].x, ug[3].y}, 0)) == SENT || fl != tag)
    while (__any(U_BAD) && ++spins <= SPIN_MAX) {
      U_LOAD()
      asm volatile("s_waitcnt vmcnt(0)" : "+v"(ug[0]), "+v"(ug[1]), "+v"(ug[2]), "+v"(ug[3]), "+v"(fl)::"memory");
    }
    CNT(21, spins)
    STAMP(11)
    const f32x4 z4 = {0, 0, 0, 0};
#pragma unroll
    for (int u = 0; u < 2; ++u) {
      const v4u uf = {ug[0][u], ug[1][u], ug[2][u], ug[3][u]};
      const f32x4 yre = mfma16h(uf, mre, z4), yim = mfma16h(uf, mim, z4);
      const int c = 2 * w + (q4 >> 1);
      const unsigned o = L0 + L_C + u * 8192 + (c * 16 + (r ^ (c & 7))) * 16 + (q4 & 1) * 8;
      *(LAS v2u*)(size_t)o = v2u{pkh(yre[0], yre[1]), pkh(yre[2], yre[3])};
      *(LAS v2u*)(size_t)(o + 4096) = v2u{pkh(yim[0], yim[1]), pkh(yim[2], yim[3])};
    }
    BAR();
    WD_DMA(2, 4)
    f32x4 Ce = z4, Co = z4, Se = z4, So = z4;
#pragma unroll
    for (int kc = 0; kc < 4; ++kc) {
      const int c = 4 * kc + q4;
      const unsigned o = L0 + L_C + (c * 16 + (r ^ (c & 7))) * 16;
      Ce = mfma16h(ce[kc], lds16(o), Ce);
      Se = mfma16h(se[kc], lds16(o + 4096), Se);
      Co = mfma16h(co[kc], lds16(o + 8192), Co);
      So = mfma16h(so[kc], lds16(o + 12288), So);
    }
    v4u hq[2];
#pragma unroll
    for (int g = 0; g < 2; ++g) {
      const int gg = g ^ q;
      const float sg = gg ? 1.0f : -1.0f;
      const v4u xs = gg ? v4u{__builtin_amdgcn_alignbit(xv1.y, xv1.y, 16), __builtin_amdgcn_alignbit(xv1.x, xv1.x, 16),
                              __builtin_amdgcn_alignbit(xv1.w, xv1.w, 16), __builtin_amdgcn_alignbit(xv1.z, xv1.z, 16)} : xv0;
      float lo[4], hi[4];
#pragma unroll
      for (int i = 0; i < 4; ++i) {
        const float a = __builtin_fmaf(sg, Se[i], Ce[i]), bb = __builtin_fmaf(sg, Co[i], So[i]);
        const float x0 = (i & 1) ? bhi(xs[i >> 1]) : blo(xs[i >> 1]), x1 = (i & 1) ? bhi(xs[2 + (i >> 1)]) : blo(xs[2 + (i >> 1)]);
        lo[i] = __builtin_fmaf(a - bb, SCALE_Y, x0); hi[i] = __builtin_fmaf(a + bb, SCALE_Y, x1);
      }
      hq[g] = v4u{pkb(lo[0], lo[1]), pkb(lo[2], lo[3]), pkb(hi[0], hi[1]), pkb(hi[2], hi[3])};
      if (g == 0) {
        const unsigned lo_ = w < 7 ? (unsigned)((q4 * 64 + r) * 16) : OOB;
        const unsigned lso = LH_OFF + (unsigned)((s2 >> 2) * LH_STR + ((((s2 >> 1) & 1) * 28 + 4 * dt) * 64 + 16 * (s2 & 1) + 32 * q) * 16);
        st16(hq[0], rfx, lo_, lso);
        MEMB();
        STAMP(12)
        SB();
      }
    }
    PLO();
    WD_DMA(4, 8)
    float sum = 0.f;
#pragma unroll
    for (int g = 0; g < 2; ++g)
#pragma unroll
      for (int i = 0; i < 4; ++i) sum += blo(hq[g][i]) + bhi(hq[g][i]);
    sum = xor32_add(xor16_add(sum));
    const float lm = sum * (1.0f / 64.0f);
    float m2 = 0.f;
#pragma unroll
    for (int g = 0; g < 2; ++g)
#pragma unroll
      for (int i = 0; i < 4; ++i) {
        const float d0_ = blo(hq[g][i]) - lm, d1_ = bhi(hq[g][i]) - lm;
        m2 = __builtin_fmaf(d0_, d0_, m2); m2 = __builtin_fmaf(d1_, d1_, m2);
      }
    m2 = xor32_add(xor16_add(m2));
    if (q4 == 0) *(LAS f32x2*)(size_t)(L0 + L_ST + (r * 8 + w) * 8) = f32x2{lm, m2};
    BAR();
    if (w == 7 && q == 0) {
      PHI();
      f32x4 sv[4];
#pragma unroll
      for (int j = 0; j < 4; ++j) sv[j] = *(LAS const f32x4*)(size_t)(L0 + L_ST + r * 64 + j * 16);
      const float mean = (((sv[0].x + sv[0].z) + (sv[1].x + sv[1].z)) + ((sv[2].x + sv[2].z) + sv[3].x)) * (1.0f / 7.0f);
      float M2 = 0.f;
#pragma unroll
      for (int j = 0; j < 7; ++j) {
        const float dlt = sv[j >> 1][2 * (j & 1)] - mean;
        M2 += __builtin_fmaf(64.0f * dlt, dlt, sv[j >> 1][2 * (j & 1) + 1]);
      }
      const float rs = __builtin_amdgcn_rsqf(M2 * (1.0f / 448.0f) + 1e-5f);
      const float m0 = -mean, m1 = m0 - trunc16(m0), m2_ = m1 - trunc16(m1);
      st16(v4u{hi16(m0, m0), hi16(m0, m1), hi16(m1, m2_), hi16(rs, rs - trunc16(rs))}, rfx, q4 == 0 ? (unsigned)r * 16 : OOB, SS_OFF + s2 * 256);
      MEMB();
      PLO();
    }
  }
  if (SPARE) {
    VMW(0);
    BAR();
    FLAG(2)
  } else {
    WD_DMA(8, 12)
    v4u wdf[7][4];
    const unsigned tdb = L0 + (unsigned)(w * 1024 + (q4 >> 1) * 8192 + (4 * (q4 & 1) + ((lane >> 2) & 3)) * 128 + ((lane >> 3) & 1) * 64 + (lane & 3) * 16);
    unsigned td0 = tdb;
#define WD_READ(u)                                                                                     \
  {                                                                                                    \
    constexpr int sl_ = dslot(2 * (u));                                                                \
    asm volatile("" : "+v"(td0));                                                                      \
    const unsigned ta_ = td0 + (sl_ & ~7) * 8192, tb_ = ta_ ^ 64;                                      \
    _Pragma("unroll") for (int nt = 0; nt < 4; ++nt) {                                                 \
      const v2u t_ = ldtr(((nt >> 1) ? tb_ : ta_) + (sl_ & 7) * 8192 + (nt & 1) * 8);                  \
      wdf[(u) >> 1][nt][2 * ((u) & 1)] = t_.x; wdf[(u) >> 1][nt][2 * ((u) & 1) + 1] = t_.y;            \
    }                                                                                                  \
  }
    v4u csf;
    W_SUM(18, 28)
    {
      const float c0_ = xor32_add(cs0 + cs1), c1_ = c0_ - trunc16(c0_), c2_ = c1_ - trunc16(c1_);
      csf = v4u{hi16(c0_, c1_), hi16(c2_, c0_), hi16(c1_, c0_), 0u};
      if (lane >= 32) csf = v4u{0u, 0u, 0u, 0u};
      asm volatile("" : "+v"(csf));
    }
    VMW(4);
    WD_READ(0) WD_READ(1) WD_READ(2) WD_READ(3)
    BAR();
    STAMP(13)
    if (DLY_L) __builtin_amdgcn_s_sleep(DLY_L);
    const unsigned lnso = LH_OFF + (unsigned)(2 * mG) * LH_STR;
    const unsigned la = L0 + lane * 16;
#define LN_DMA(i)                                                                                      \
  {                                                                                                    \
    const int pi_ = 8 * (i) + w, tt_ = pi_ / 28, j_ = pi_ - 28 * tt_;                                  \
    dma1(rfx, lane * 16, lnso + ((i) >= 7 ? LH_STR - 57344 : 0) + (28 * tt_ + KROT(j_)) * 1024, L0 + pi_ * 1024); \
  }
#define LN_MAX(i0, i1) \
  _Pragma("unroll") for (int i_ = (i0); i_ < (i1); ++i_) mx = max4(lds16((i_ < 7 ? la : la + 57344) + w * 1024 + (i_ < 7 ? i_ : i_ - 7) * 8192), mx);
#define LN_FIX(i0, i1)                                                                                 \
  if (__any(mx == SENT)) {                                                                             \
    int spins = 0;                                                                                     \
    do {                                                                                               \
      _Pragma("unroll") for (int i_ = (i0); i_ < (i1); ++i_) LN_DMA(i_)                                \
      VMW(0);                                                                                          \
      mx = 0;                                                                                          \
      LN_MAX(i0, i1)                                                                                   \
      lspins += 1 << (8 * ((i0) / 3));                                                                 \
    } while (__any(mx == SENT) && ++spins < SPIN_MAX);                                                 \
  }
    unsigned mx = 0;
    int lspins = 0;
    PHI();
#pragma unroll
    for (int i = 0; i < 4; ++i) LN_DMA(i)
    PLO();
    {
      const unsigned pb = (unsigned)(28672 + (96 * nE + 12 * w + lane) * 128);
      ald4(junk, rfx, lane < 12 ? (pb >= 57344 ? pb + (LH_STR - 57344) : pb) : OOB, lnso);
    }
    VMW(1);
    LN_MAX(0, 4)
    LN_FIX(0, 4)
    BAR();
    STAMP(14)
    f32x16 acc, accp;
    v4u af[NAF], ho, stf;
    v4u hb[4][7];
    unsigned fl;
    float rsv = 0.f, rlv = 0.f;
    const unsigned hso = LH_OFF + (unsigned)((2 * mG + (b & 1)) * LH_STR + 57344 + (8 * nE + w) * 1024);
    unsigned hvo = (unsigned)(q4 * 512 + pE * 256 + r * 16);
    asm volatile("" : "+v"(hvo));
    const unsigned hlo = LH_OFF + (unsigned)((2 * mE + (w >> 2)) * LH_STR + 57344 + 14 * (w & 3) * 1024);
    const unsigned sso = SS_OFF + (unsigned)(mG * 2048);
#define H_LOAD(tt, i) hb[tt][i] = ld16(rfx, hvo, hlo + (tt) * 57344 + ER(i) * 2048);
#define H_TOUCH(tt) touch(junk, rfx, hvo, hlo + (tt) * 57344 + ER(0) * 2048);
#define LGK3() asm volatile("s_waitcnt lgkmcnt(3)" ::: "memory")
#define VMWS(n) asm volatile("s_waitcnt vmcnt(" #n ")" : "+v"(stf), "+v"(fl), "+v"(junk)::"memory")
#define FL_FIX(i)                                                                                      \
  if (__any(fl != tag)) {                                                                              \
    int spins = 0;                                                                                     \
    do {                                                                                               \
      ald4(fl, rfx, l32 * 16, FL_OFF + (i) * 512);                                                      \
      VMWS(0);                                                                                         \
      lspins += 1 << 24;                                                                               \
    } while (__any(fl != tag) && ++spins < SPIN_MAX);                                                  \
  }
    float ge = 0.f, gp = 0.f;
#define GLU_STEP(k)                                                                                    \
  {                                                                                                    \
    const int e_ = (k) >> 1;                                                                           \
    if (((k) & 1) == 0) {                                                                              \
      ge = __builtin_amdgcn_exp2f(accp[e_] * rlv);                                                     \
      asm volatile("" : "+v"(ge));                                                                     \
    } else {                                                                                           \
      const float p_ = __builtin_amdgcn_rcpf(1.0f + ge) * (accp[8 + e_] * rsv);                        \
      if (e_ & 1) { unsigned t_ = pkb(gp, p_); asm volatile("" : "+v"(t_)); ho[e_ >> 1] = t_; }        \
      else { gp = p_; asm volatile("" : "+v"(gp)); }                                                   \
    }                                                                                                  \
  }
    auto dpass = [&](auto TT) __attribute__((always_inline)) {
      constexpr int tt = decltype(TT)::value;
      unsigned lbt = la + tt * 28672;
      asm volatile("" : "+v"(lbt));
      __builtin_amdgcn_s_setprio(3);
#pragma unroll
      for (int i = 0; i < NAF - 1; ++i) af[i] = lds16(lbt + i * 1024);
#pragma unroll
      for (int i = 0; i < 16; ++i) acc[i] = 0.f;
      mx = 0;
      SB();
#pragma unroll
      for (int ks = 0; ks < 28; ++ks) {
        if (ks + NAF - 1 < 28) af[(ks + NAF - 1) % NAF] = lds16(lbt + (ks + NAF - 1) * 1024);
        acc = mfma32b(wf[ks], af[ks % NAF], acc);
        SB();
        if (ks == (tt ? 9 : 0)) ald16<tt * 512>(stf, rfx, l32 * 16, sso);
        if (tt > 0) {
          if (ks < 16) GLU_STEP(ks)
          if (ks == 16) { st16(ho, rfx, lane * 16, hso + (tt - 1) * 57344); MEMB(); }
        }
        if (ks == 9) __builtin_amdgcn_s_setprio(2);
        if (ks == 19 && tt < 3) __builtin_amdgcn_s_setprio(1);
        if (tt == 0) {
          if (ks == 1) ald4(fl, rfx, l32 * 16, FL_OFF + 512);
          if (ks >= 2 && ks < 22 && (ks & 1) == 0) LN_DMA(3 + (ks >> 1))
          if (ks == 21) WD_DMA1(12)
          if (ks == 22) WD_DMA1(13)
          if (ks == 4) WD_READ(4)
          if (ks == 7) WD_READ(5)
          if (ks == 24) { VMWS(9); LN_MAX(4, 7) }
        }
        if (tt == 1) {
          if (ks < 8 && (ks & 1) == 1) WD_DMA1(14 + (ks >> 1))
          if (ks == 10 || ks == 12) WD_DMA1(18 + ((ks - 10) >> 1))
          if (ks == 17) { VMW(8); WD_READ(6) }
          if (ks == 18) LN_MAX(7, 10)
          if (ks == 20) LN_MAX(10, 14)
          if (ks == 22) { LGK3(); WD_DMA1(20) }
          if (ks == 24) WD_DMA1(21)
          if (ks == 26) VMWS(5);
        }
        if (tt == 2) {
          if (ks < 8 && (ks & 1) == 0) WD_DMA1(22 + (ks >> 1))
          if (ks == 17) WD_READ(7)
          if (ks == 20) { LGK3(); WD_DMA1(26) }
          if (ks == 22) WD_DMA1(27)
          if (ks == 24) H_TOUCH(0)
          if (ks == 26) VMWS(4);
        }
        if (tt == 3) {
          if (ks == 1) H_TOUCH(1)
          if (ks >= 3 && ks <= 11 && (ks & 1) == 1) H_LOAD(0, (ks - 3) >> 1)
          if (ks == 15 || ks == 17) H_LOAD(0, (ks - 5) >> 1)
          if (ks >= 19 && ks <= 25 && (ks & 1) == 1) H_LOAD(1, (ks - 19) >> 1)
          if (ks == 9) ald4(fl, rfx, l32 * 16, FL_OFF + 1024);
          if (ks == 8) WD_READ(8)
          if (ks == 14) WD_READ(9)
          if (ks == 18) WD_READ(10)
          if (ks == 20) WD_READ(11)
          if (ks == 22) WD_READ(12)
          if (ks == 24) { VMWS(7); WD_READ(13) }
        }
        SB();
      }
      if (__any(max4(stf, 0) == SENT)) {
        int spins = 0;
        do {
          ald16<tt * 512>(stf, rfx, l32 * 16, sso);
          VMWS(0);
          lspins += 1 << 16;
        } while (__any(max4(stf, 0) == SENT) && ++spins < SPIN_MAX);
      }
      accp = mfma32b(csf, stf, acc);
      rsv = blo(stf.w) + bhi(stf.w);
      rlv = rsv * -1.44269504f;
      if (tt == 0) { LN_FIX(4, 7) FL_FIX(1) }
      if (tt == 1) LN_FIX(7, 14)
      if (tt == 3) FL_FIX(2)
      if (tt < 3) BAR();
      if (tt == 0) STAMP(15)
      if (tt == 2) STAMP(2)
    };
    dpass(IC<0>{}); dpass(IC<1>{}); dpass(IC<2>{}); dpass(IC<3>{});
    CNT(22, lspins)
    STAMP(16)
    int espins = 0;
    f32x4 ea[2][4];
    unsigned ln2 = la;
    asm volatile("" : "+v"(ln2));
    ln2 = (ln2 - L0) >> 4;
    unsigned hro, sel0, pa[2], ra, rb, ovo, hra;
    int hstep;
    v2u hr0, hr1;
    f32x4 rp[8];
    const unsigned hrs = LH_OFF + (unsigned)((2 * mE + (w >> 2)) * LH_STR + ((w >> 1) & 1) * 28672 + pE * 256);
    const rsrc_t ro = mkrs(out, S_ * D_ * 2);
#define HR_LOAD() { hr0 = ld8(rfx, hro, hrs); hr1 = ld8(rfx, hro + hstep, hrs); }
#define H_FIX(tt)                                                                                      \
  {                                                                                                    \
    int spins = 0;                                                                                     \
    do {                                                                                               \
      MEMB();                                                                                          \
      _Pragma("unroll") for (int i = 0; i < 7; ++i) H_LOAD(tt, i)                                      \
      mx = 0;                                                                                          \
      _Pragma("unroll") for (int i = 0; i < 7; ++i) mx = max4(hb[tt][i], mx);                          \
      espins += 1 << (8 * (tt));                                                                       \
    } while (__any(mx == SENT) && ++spins < SPIN_MAX);                                                 \
  }
#define E_ZERO(tt) _Pragma("unroll") for (int nt = 0; nt < 4; ++nt) ea[(tt) & 1][nt] = f32x4{0, 0, 0, 0};
#define E_MM(tt, k) ea[(tt) & 1][(k) & 3] = mfma16b(wdf[(k) >> 2][(k) & 3], hb[tt][(k) >> 2], ea[(tt) & 1][(k) & 3]);
#define E_STEP(tt, k) E_MM(tt, k)
#define E_END(tt)                                                                                      \
  if (__any(__builtin_isnan(ea[(tt) & 1][0][0]))) {                                                    \
    H_FIX(tt) E_ZERO(tt)                                                                               \
    _Pragma("unroll") for (int k = 0; k < 28; ++k) E_MM(tt, k)                                         \
  }
#define E_PUT(tt, nt)                                                                                  \
  {                                                                                                    \
    const int sl_ = pslot(tt, nt);                                                                     \
    *(LAS f32x4*)(size_t)((pa[(nt) & 1] + (sl_ & ~7) * 8192) + (sl_ & 7) * 8192) = ea[(tt) & 1][nt];         \
  }
#define R_READ(base, ww) rp[ww] = *(LAS const f32x4*)(size_t)((base) + (ww) * 1024);
#define R_FIN(h_, so)                                                                                  \
  {                                                                                                    \
    const f32x4 s_ = ((rp[0] + rp[1]) + (rp[2] + rp[3])) + ((rp[4] + rp[5]) + (rp[6] + rp[7]));        \
    const unsigned f0 = pkb(s_[0], s_[1]), f1 = pkb(s_[2], s_[3]);                                     \
    __builtin_amdgcn_raw_buffer_store_b64(v2u{pkb(blo(h_.x) + blo(f0), bhi(h_.x) + bhi(f0)), pkb(blo(h_.y) + blo(f1), bhi(h_.y) + bhi(f1))}, \
                                          ro, ovo, so, 16);                                            \
  }
    E_ZERO(0)
    SB();
#pragma unroll
    for (int k = 0; k < 28; ++k) {
      if (k < 16) GLU_STEP(k)
      if (k == 16) { st16(ho, rfx, lane * 16, hso + 3 * 57344); MEMB(); __builtin_amdgcn_s_setprio(1); }
      if (k < 6 && (k & 1) == 0) H_LOAD(1, 4 + (k >> 1))
      E_MM(0, k)
      if (k == 17) H_LOAD(2, 0)
      if (k == 24) {
#pragma unroll
        for (int nt = 0; nt < 2; ++nt) pa[nt] = L0 + w * 1024 + (ln2 >> 4) * 256 + ((ln2 + 2 * (ln2 >> 4) + 8 * nt) & 15) * 16;
        asm volatile("" : "+v"(pa[0]), "+v"(pa[1]));
      }
      if (k == 26) {
        const int c0 = 64 * nE + 32 * (w & 1) + 8 * (int)(ln2 >> 4);
        const int seg = ((c0 >> 4) * 37) >> 8, hi = seg >> 1, mir = seg & 1, cc = c0 - 224 * hi;
        const int ksa = mir ? (220 - cc) >> 2 : cc >> 2;
        hro = (unsigned)((ksa * 64 + 32 * mir + (ln2 & 15)) * 16 + 8 * hi);
        hstep = mir ? -1024 : 1024;
        sel0 = mir ? 0x05040706u : 0x03020100u;
        asm volatile("" : "+v"(hro), "+v"(hstep), "+v"(sel0));
      }
      SB();
    }
    E_END(0)
    STAMP(17)
    PLO();
    E_ZERO(1)
    SB();
#pragma unroll
    for (int k = 0; k < 28; ++k) {
      E_STEP(1, k)
      if (k < 24 && k % 6 == 2) E_PUT(0, (k / 6) & 3)
      if (k == 0) HR_LOAD()
      if (k >= 4 && k < 4 + 4 * H2N && (k & 3) == 0) H_LOAD(2, k >> 2)
      if (k == 23) {
        const int row = 8 * ((w >> 1) & 1) + (int)(ln2 >> 3), g = ln2 & 7, tl = w >> 2;
        const unsigned o = L0 + (unsigned)((g & 1) * 8192 + (g >> 1) * 256 + ((row + 2 * (g >> 1) + 8 * (g & 1)) & 15) * 16);
        ra = o + (unsigned)((tl ? pslot(1, 0) : pslot(0, 0)) + 2 * (w & 1)) * 8192;
        rb = o + (unsigned)(tl ? ((w & 1) ? pslot(3, 2) : pslot(3, 0)) : pslot(2, 0) + 2 * (w & 1)) * 8192;
        hra = L0 + 32768 + (unsigned)((2 * tl + (w & 1)) * 1024 + row * 64 + g * 8);
        ovo = (unsigned)(((128 * row + 8 * mE + 2 * tl + pE) * D_ + 64 * nE + 32 * (w & 1) + 4 * g) * 2);
        asm volatile("" : "+v"(ra), "+v"(rb), "+v"(hra), "+v"(ovo));
      }
      SB();
    }
    E_END(1)
    STAMPE(4)
    E_ZERO(2)
    SB();
#pragma unroll
    for (int k = 0; k < 28; ++k) {
      if (k < 2 * (6 - H2N) && (k & 1) == 0) H_LOAD(2, H2N + 1 + (k >> 1))
      if (k >= H3_AT && k < H3_AT + 7 * H3_ST && (k - H3_AT) % H3_ST == 0) H_LOAD(3, (k - H3_AT) / H3_ST)
      if (k < 24 && k % 6 == 2) E_PUT(1, (k / 6) & 3)
      if (k == 24) {
        if (__any(max(max(hr0.x, hr0.y), max(hr1.x, hr1.y)) == SENT)) {
          int spins = 0;
          do {
            MEMB();
            HR_LOAD()
          } while (__any(max(max(hr0.x, hr0.y), max(hr1.x, hr1.y)) == SENT) && ++spins < SPIN_MAX);
        }
        *(LAS v4u*)(size_t)(L0 + 32768 + w * 1024 + ((ln2 & 15) * 4 + (ln2 >> 4)) * 16) =
            v4u{__builtin_amdgcn_perm(hr0.y, hr0.x, sel0), __builtin_amdgcn_perm(hr0.y, hr0.x, sel0 ^ 0x04040404u),
                __builtin_amdgcn_perm(hr1.y, hr1.x, sel0), __builtin_amdgcn_perm(hr1.y, hr1.x, sel0 ^ 0x04040404u)};
      }
      E_STEP(2, k)
      SB();
    }
    E_END(2)
    STAMPE(5)
    BAR();
    STAMPE(6)
    E_ZERO(3)
    SB();
#pragma unroll
    for (int k = 0; k < 28; ++k) {
      if (k == 0) { hr0 = *(LAS const v2u*)(size_t)hra; hr1 = *(LAS const v2u*)(size_t)(hra + 4096); }
      if (k < 16 && (k & 1) == 0) R_READ(ra, k >> 1)
      if (k == 19) R_FIN(hr0, 0)
      if (k < 24 && k % 6 == 3) E_PUT(2, (k / 6) & 3)
      E_STEP(3, k)
      SB();
    }
    E_END(3)
    E_PUT(3, 0) E_PUT(3, 1) E_PUT(3, 2) E_PUT(3, 3)
    CNT(23, espins)
    STAMP(18)
    BAR();
#pragma unroll
    for (int ww = 0; ww < 8; ++ww) R_READ(rb, ww)
    R_FIN(hr1, 3584)
  }
}

__global__ __launch_bounds__(512, 2) void fused(const uint16_t* __restrict__ x, const uint16_t* __restrict__ wr,
                                                const uint16_t* __restrict__ wi, const uint16_t* __restrict__ wg,
                                                const uint16_t* __restrict__ wu, const uint16_t* __restrict__ wd,
                                                unsigned char* __restrict__ ws, uint16_t* __restrict__ out) {
  __shared__ __attribute__((aligned(16))) unsigned char smem[163840];
  const unsigned L0 = (unsigned)(size_t)smem;
  if (blockIdx.x < 224) body<false>(x, wr, wi, wg, wu, wd, ws, out, L0);
  else body<true>(x, wr, wi, wg, wu, wd, ws, out, L0);
}

extern "C" void kernel_launch(void* const* d_in, const int* in_sizes, int n_in, void* d_out, int out_size, void* d_ws,
                              size_t ws_size, hipStream_t stream) {
  fused<<<256, 512, 0, stream>>>((const uint16_t*)d_in[0], (const uint16_t*)d_in[1], (const uint16_t*)d_in[2],
                                 (const uint16_t*)d_in[4], (const uint16_t*)d_in[5], (const uint16_t*)d_in[6],
                                 (unsigned char*)d_ws, (uint16_t*)d_out);
}
```

```cpp
#include <hip/hip_runtime.h>
#include <hip/hip_bf16.h>
#include <stdint.h>

typedef unsigned v4u __attribute__((ext_vector_type(4)));
typedef unsigned v2u __attribute__((ext_vector_type(2)));
typedef float f32x2 __attribute__((ext_vector_type(2)));
typedef float f32x4 __attribute__((ext_vector_type(4)));
typedef float f32x16 __attribute__((ext_vector_type(16)));
typedef _Float16 h8 __attribute__((ext_vector_type(8)));
typedef _Float16 h2 __attribute__((ext_vector_type(2)));
typedef __bf16 b8 __attribute__((ext_vector_type(8)));
typedef __bf16 b2 __attribute__((ext_vector_type(2)));
typedef short s4v __attribute__((ext_vector_type(4)));
typedef __amdgpu_buffer_rsrc_t rsrc_t;
#define DI __device__ __forceinline__
template <int N> struct IC { static constexpr int value = N; };
#define LAS __attribute__((address_space(3)))

constexpr int S_ = 2048, D_ = 448, F_ = 1792, DF_ = 225;
constexpr unsigned MB = 1u << 20;
constexpr unsigned U_OFF = 0;
constexpr unsigned EP_OFF = 2 * MB;
constexpr unsigned FL_OFF = 2 * MB + 4096;
constexpr unsigned SS_OFF = 2 * MB + 65536;
constexpr unsigned LH_OFF = 3 * MB;
constexpr unsigned LH_STR = 5 * 57344;
constexpr unsigned FIX_BYTES = 16 * MB;
constexpr unsigned REG_BYTES = 4 * MB;
#define NROT 8
constexpr unsigned OOB = 0x80000000u;
constexpr unsigned SENT = 0xffffffffu;
constexpr int SPIN_MAX = 20000;
constexpr unsigned L_SP = 17 * 8192;
constexpr unsigned L_A = L_SP, L_BT = L_SP + 8192, L_BP = L_SP + 16384, L_F = L_BP, L_X = L_SP, L_C = L_SP, L_ST = L_SP + 23552;
constexpr int wslot(int n) { return n < 17 ? n : n - 17; }
constexpr int dslot(int j) {
  return j < 2 ? 11 + j : j < 8 ? j - 2 : j < 14 ? j + 6 : j < 18 ? j : j < 20 ? j - 18 : j < 22 ? j - 2 : j < 26 ? j - 20 : j - 12;
}
constexpr int pslot(int tt, int nt) { return tt == 0 ? 6 + nt : tt == 1 ? nt : tt == 2 ? 14 + nt : nt < 2 ? 4 + nt : 16 + nt; }

#define H2N 4
#define H3_AT 7
#define H3_ST 3
#define SBM 1
#define NAF 3
#define DLY_T 0
#define DLY_U 0
#define DLY_L 0

#define STAMP_TID 0
#define STAMP(i) {}
#define CNT(i, v)

#define STAMPF(i) STAMP(i)
#define STAMPE(i) {}
DI unsigned pkh(float a, float b) { h2 v = {(_Float16)a, (_Float16)b}; return __builtin_bit_cast(unsigned, v); }
DI unsigned pkb(float a, float b) { b2 v = {(__bf16)a, (__bf16)b}; return __builtin_bit_cast(unsigned, v); }
DI float blo(unsigned u) { return __builtin_bit_cast(float, u << 16); }
DI float bhi(unsigned u) { return __builtin_bit_cast(float, u & 0xffff0000u); }
DI float cosr(float x) { return __builtin_amdgcn_cosf(x); }
DI float sinr(float x) { return __builtin_amdgcn_sinf(x); }
DI f32x2 cis(float x) { return f32x2{cosr(x), sinr(x)}; }
DI f32x2 crot(f32x2 a, f32x2 b) {
  return __builtin_elementwise_fma(f32x2{a.y, a.y}, f32x2{-b.y, b.x}, f32x2{a.x, a.x} * b);
}
DI f32x2 cheb(float c2, f32x2 a1, f32x2 a0) { return __builtin_elementwise_fma(f32x2{c2, c2}, a1, -a0); }
DI unsigned hadd(unsigned a, unsigned b) { return __builtin_bit_cast(unsigned, __builtin_bit_cast(h2, a) + __builtin_bit_cast(h2, b)); }
DI unsigned hsub(unsigned a, unsigned b) { return __builtin_bit_cast(unsigned, __builtin_bit_cast(h2, a) - __builtin_bit_cast(h2, b)); }
DI f32x4 mfma16h(v4u a, v4u b, f32x4 c) {
  return __builtin_amdgcn_mfma_f32_16x16x32_f16(__builtin_bit_cast(h8, a), __builtin_bit_cast(h8, b), c, 0, 0, 0);
}
DI f32x4 mfma16b(v4u a, v4u b, f32x4 c) {
  return __builtin_amdgcn_mfma_f32_16x16x32_bf16(__builtin_bit_cast(b8, a), __builtin_bit_cast(b8, b), c, 0, 0, 0);
}
DI f32x16 mfma32b(v4u a, v4u b, f32x16 c) {
  return __builtin_amdgcn_mfma_f32_32x32x16_bf16(__builtin_bit_cast(b8, a), __builtin_bit_cast(b8, b), c, 0, 0, 0);
}
template <int CTRL> DI float dpp_add(float v) {
  return v + __builtin_bit_cast(float, __builtin_amdgcn_update_dpp(0, __builtin_bit_cast(int, v), CTRL, 0xf, 0xf, true));
}
DI float xor16_add(float v) {
  float a = v, b = v;
  asm("s_nop 1\n\tv_permlane16_swap_b32 %0, %1" : "+v"(a), "+v"(b));
  return a + b;
}
DI float xor32_add(float v) {
  float a = v, b = v;
  asm("s_nop 1\n\tv_permlane32_swap_b32 %0, %1" : "+v"(a), "+v"(b));
  return a + b;
}
DI float sum32(float v) {
  v = dpp_add<0xB1>(v); v = dpp_add<0x4E>(v); v = dpp_add<0x141>(v); v = dpp_add<0x140>(v);
  return xor16_add(v);
}
DI rsrc_t mkrs(const void* p, unsigned bytes) { return __builtin_amdgcn_make_buffer_rsrc((void*)p, (short)0, (int)bytes, 0x00020000); }
DI void dma(rsrc_t rs, unsigned voff, unsigned soff, unsigned lds) {
  asm volatile("s_mov_b32 m0, %0\n\ts_nop 0\n\tbuffer_load_dwordx4 %1, %2, %3 offen lds" ::"s"(lds), "v"(voff), "s"(rs), "s"(soff) : "memory");
}
DI void dma1(rsrc_t rs, unsigned voff, unsigned soff, unsigned lds) {
  asm volatile("s_mov_b32 m0, %0\n\ts_nop 0\n\tbuffer_load_dwordx4 %1, %2, %3 offen sc1 lds" ::"s"(lds), "v"(voff), "s"(rs), "s"(soff) : "memory");
}
template <int OFF> DI void ald8(v2u& d, rsrc_t rs, unsigned voff, unsigned soff) {
  asm volatile("buffer_load_dwordx2 %0, %1, %2, %3 offen offset:%4 sc1" : "=&v"(d) : "v"(voff), "s"(rs), "s"(soff), "n"(OFF) : "memory");
}
template <int OFF> DI void ald16(v4u& d, rsrc_t rs, unsigned voff, unsigned soff) {
  asm volatile("buffer_load_dwordx4 %0, %1, %2, %3 offen offset:%4 sc1" : "=&v"(d) : "v"(voff), "s"(rs), "s"(soff), "n"(OFF) : "memory");
}
DI v4u ld16(rsrc_t rs, unsigned voff, unsigned soff) { return __builtin_amdgcn_raw_buffer_load_b128(rs, voff, soff, 16); }
DI v2u ld8(rsrc_t rs, unsigned voff, unsigned soff) { return __builtin_amdgcn_raw_buffer_load_b64(rs, voff, soff, 16); }
DI void st16(v4u v, rsrc_t rs, unsigned voff, unsigned soff) { __builtin_amdgcn_raw_buffer_store_b128(v, rs, voff, soff, 16); }
DI v4u lds16(unsigned a) { return *(LAS const v4u*)(size_t)a; }
DI v2u ldtr(unsigned a) { return __builtin_bit_cast(v2u, __builtin_amdgcn_ds_read_tr16_b64_v4i16((LAS s4v*)(size_t)a)); }
DI unsigned max4(v4u v, unsigned m) { return max(max(max(max(m, v[0]), v[1]), v[2]), v[3]); }
#define BAR() asm volatile("s_waitcnt lgkmcnt(0)\n\ts_barrier" ::: "memory")
#define VMW(n) asm volatile("s_waitcnt vmcnt(" #n ")" ::: "memory")
#define SB() __builtin_amdgcn_sched_barrier(0)
#define MEMB() asm volatile("" ::: "memory")
#define PLO() __builtin_amdgcn_s_setprio(0)
#define PHI() __builtin_amdgcn_s_setprio(2)

constexpr float SCALE_B = 1.0f / 16.0f;
constexpr float SCALE_Y = 512.0f / (917504.0f * SCALE_B);

DI unsigned glu2(float g0, float g1, float u0, float u1, float rs, float rl) {
  const float s0 = __builtin_amdgcn_rcpf(1.0f + __builtin_amdgcn_exp2f(g0 * rl));
  const float s1 = __builtin_amdgcn_rcpf(1.0f + __builtin_amdgcn_exp2f(g1 * rl));
  return pkb(s0 * (u0 * rs), s1 * (u1 * rs));
}
DI void ald4(unsigned& d, rsrc_t rs, unsigned voff, unsigned soff) {
  asm volatile("buffer_load_dword %0, %1, %2, %3 offen sc1" : "=&v"(d) : "v"(voff), "s"(rs), "s"(soff) : "memory");
}
DI void touch(unsigned& d, rsrc_t rs, unsigned voff, unsigned soff) {
  asm volatile("buffer_load_dword %0, %1, %2, %3 offen sc1" : "+v"(d) : "v"(voff), "s"(rs), "s"(soff) : "memory");
}
DI float sum2(unsigned u, float c) {
  const b2 one2 = {(__bf16)1.0f, (__bf16)1.0f};
  return __builtin_amdgcn_fdot2_f32_bf16(__builtin_bit_cast(b2, u), one2, c, false);
}
DI void ast16(v4u v, rsrc_t rs, unsigned voff, unsigned soff) {
  asm volatile("buffer_store_dwordx4 %0, %1, %2, %3 offen sc1" ::"v"(v), "v"(voff), "s"(rs), "s"(soff) : "memory");
}
DI float trunc16(float v) { return bhi(__builtin_bit_cast(unsigned, v)); }
DI unsigned hi16(float a, float b) { return __builtin_amdgcn_perm(__builtin_bit_cast(unsigned, b), __builtin_bit_cast(unsigned, a), 0x07060302u); }

template <bool SPARE>
DI void body(const uint16_t* __restrict__ x, const uint16_t* __restrict__ wr, const uint16_t* __restrict__ wi,
             const uint16_t* __restrict__ wg, const uint16_t* __restrict__ wu, const uint16_t* __restrict__ wd,
             unsigned char* __restrict__ ws, uint16_t* __restrict__ out, const unsigned L0) {
  STAMP(0)
  const int b = blockIdx.x, tid = threadIdx.x, lane = tid & 63;
  const int w = __builtin_amdgcn_readfirstlane(tid >> 6);
  const int r = lane & 15, q4 = lane >> 4;
  const int s2 = (b & 7) + 8 * (b >> 4), q = (b >> 3) & 1;
  const int j1b = b & 15, tileb = b >> 4;
  const int yb = b >> 3;
  const int mG = 4 * ((b >> 1) & 3) + (yb & 3), nD = 7 * (b & 1) + (yb >> 2);
  const int mE = 2 * (b & 7) + (yb & 1), pE = (yb >> 1) & 1, nE = yb >> 2;
  const int sbk = 4 * (b & 7) + (yb & 3);
  const rsrc_t rx = mkrs(x, S_ * D_ * 2), rfx = mkrs(ws, FIX_BYTES);
  PHI();

  v2u xa[4];
  unsigned ep;
  const int l32 = lane & 31;
  {
    const int lc = l32 < 28 ? l32 : 27;
    const unsigned rowo = (unsigned)(128 * (2 * w + (lane >> 5)) + s2) * 896u;
    const unsigned o1 = rowo + 8 * lc, o2 = rowo + 440 - 8 * lc;
    asm volatile("buffer_load_dwordx2 %0, %5, %7, 0 offen\n\t"
                 "buffer_load_dwordx2 %1, %6, %7, 0 offen\n\t"
                 "buffer_load_dwordx2 %2, %5, %7, 0 offen offset:448\n\t"
                 "buffer_load_dwordx2 %3, %6, %7, 0 offen offset:448\n\t"
                 "s_load_dword %4, %8, 0"
                 : "=&v"(xa[0]), "=&v"(xa[1]), "=&v"(xa[2]), "=&v"(xa[3]), "=&s"(ep)
                 : "v"(o1), "v"(o2), "s"(rx), "s"(ws + EP_OFF)
                 : "memory");
  }
  PLO();
  const v4u sent4 = {SENT, SENT, SENT, SENT};
  unsigned junk;
  {
    const int i5 = (lane * 52) >> 8, l5 = lane - 5 * i5;
    const unsigned tf = lane < 40 ? (unsigned)((((b & 7) + 8 * (8 * yb + i5)) * 450 + (l5 < 4 ? 128 * l5 : 446)) & ~3) : OOB;
    const unsigned td = lane < 49 ? (unsigned)(49 * b + lane) * 128 : OOB;
    ald4(junk, mkrs(w == 0 ? wr : w == 1 ? wi : wd, w < 2 ? S_ * DF_ * 2 : F_ * D_ * 2), w < 2 ? tf : w == 2 ? td : OOB, 0);
    ast16(sent4, rfx, tid * 16, U_OFF + (2 * s2 + q) * 8192);
    if (SPARE) ast16(sent4, rfx, lane < 8 ? (unsigned)(w * 8 + lane) * 16 : OOB, SS_OFF + sbk * 1024);
  }
  const int wmat = w & 1, wrange = w >> 1;
  const rsrc_t rw = mkrs(wmat ? wu : wg, D_ * F_ * 2);
  const unsigned wvoff = (unsigned)(((wrange >> 1) ? 3 - q4 : q4) * 3584 + ((r ^ (2 * (q4 + 4 * wmat))) * 16));
  const unsigned wsoff0 = (unsigned)((224 * (wrange & 1) + ((wrange >> 1) ? 220 : 0)) * 3584 + nD * 256);
  const int wstep = (wrange >> 1) ? -4 * 3584 : 4 * 3584;
  int rot = 64 + 7 * (yb & 3) + ((7 * ((b >> 1) & 3)) >> 2);
  asm volatile("" : "+s"(rot));
  const unsigned clr0 = LH_OFF + (unsigned)sbk * LH_STR;
#define KROT(j) ((j) + rot >= 92 ? (j) + rot - 92 : (j) + rot - 64)
#define W_DMA(n0, n1)                                                                                  \
  _Pragma("unroll") for (int n_ = (n0); n_ < (n1); ++n_) {                                             \
    if (SPARE) ast16(sent4, rfx, tid * 16, clr0 + n_ * 8192);                                          \
    else dma(rw, wvoff, wsoff0 + KROT(n_) * wstep, L0 + (wslot(n_) * 8 + w) * 1024);                   \
  }
#define FLAG(i) if (SPARE && tid == 0) __builtin_amdgcn_raw_buffer_store_b32(tag, rfx, sbk * 16, FL_OFF + (i) * 512, 16);
  const int par = w >> 2, w3 = w & 3;
  const int kb = 16 * tileb + r;
  const int fc0 = tileb < 14 ? 16 * tileb : 209;
  SB();
  v4u bc[4], bs[4], mre, mim;
  {
    const int k = 2 * (16 * w + 8 * ((r >> 1) & 1) + 2 * (r >> 2) + (r & 1)) + q;
    const float kf = (float)k * (1.0f / 896.0f);
    const f32x2 st = cis(-2.0f * kf), rk = cis(-64.0f * kf);
    const float c2 = 2.0f * st.x;
    f32x2 z0 = cis((float)(-16 * q4 - 1) * kf), z1 = crot(z0, st);
#pragma unroll
    for (int kc = 0; kc < 4; ++kc) {
      f32x2 cs[8];
      cs[0] = z0; cs[1] = z1;
      if (kc < 3) { z0 = crot(z0, rk); z1 = crot(z1, rk); }
#pragma unroll
      for (int e = 2; e < 8; ++e) cs[e] = cheb(c2, cs[e - 1], cs[e - 2]);
#pragma unroll
      for (int j = 0; j < 4; ++j) { bc[kc][j] = pkh(cs[2 * j].x, cs[2 * j + 1].x); bs[kc][j] = pkh(cs[2 * j].y, cs[2 * j + 1].y); }
    }
    f32x2 m = cis((float)(r * (512 * q4 + s2)) * (1.0f / 2048.0f));
    const f32x2 ms = cis((float)r * (1.0f / 16.0f));
#pragma unroll
    for (int i = 0; i < 4; ++i) {
      mre[i] = pkh(m.x, m.y); mim[i] = pkh(-m.y, m.x);
      if (i < 3) m = crot(m, ms);
    }
#pragma unroll
    for (int kc = 0; kc < 4; ++kc) asm volatile("" : "+v"(bc[kc]), "+v"(bs[kc]));
    asm volatile("" : "+v"(mre), "+v"(mim));
  }
  W_DMA(0, 4)
  PHI();
  STAMP(1)
  asm volatile("s_waitcnt vmcnt(6) lgkmcnt(0)" : "+v"(xa[0]), "+v"(xa[1]), "+v"(xa[2]), "+v"(xa[3]), "+s"(ep)::"memory");
  const unsigned tag = ep * 1664525u + 1013904223u;
  const rsrc_t rws = mkrs(ws + FIX_BYTES + (size_t)(tag & (NROT - 1)) * REG_BYTES, REG_BYTES);
  v4u fraw;
  {
    const unsigned c0 = __builtin_amdgcn_readlane(xa[0].x, 0), c1 = __builtin_amdgcn_readlane(xa[0].x, 32);
    const float cc = 2.0f * blo(lane < 32 ? c0 : c1);
    float s1[4], sb[4], d1[4], d2[4];
    float sum = 0.f, sq = 0.f;
#pragma unroll
    for (int i = 0; i < 4; ++i) {
      const unsigned ua = xa[0][i >> 1], ub = xa[1][1 - (i >> 1)], ue = xa[2][i >> 1], uf = xa[3][1 - (i >> 1)];
      const float a = (i & 1) ? bhi(ua) : blo(ua), e = (i & 1) ? bhi(ue) : blo(ue);
      const float bb = (i & 1) ? blo(ub) : bhi(ub), f = (i & 1) ? blo(uf) : bhi(uf);
      s1[i] = (a + f) - cc; sb[i] = (bb + e) - cc; d1[i] = a - f; d2[i] = e - bb;
      sum += s1[i] + sb[i];
      sq = __builtin_fmaf(s1[i], s1[i], sq); sq = __builtin_fmaf(sb[i], sb[i], sq);
      sq = __builtin_fmaf(d1[i], d1[i], sq); sq = __builtin_fmaf(d2[i], d2[i], sq);
    }
    if (l32 >= 28) { sum = 0.f; sq = 0.f; }
    asm volatile("" : "+v"(sum), "+v"(sq));
    W_DMA(4, 5)
    sum = sum32(sum); sq = sum32(sq);
    asm volatile("" : "+v"(sum), "+v"(sq));
    W_DMA(5, 6)
    const float mu = sum * (1.0f / 448.0f);
    const float var = __builtin_fmaxf(__builtin_fmaf(-mu, mu, sq * (0.5f / 448.0f)), 0.f);
    float rs = __builtin_amdgcn_rsqf(var + 1e-5f);
    if (l32 >= 28) rs = 0.f;
    const float sgn = q ? -1.0f : 1.0f;
    const float off = q ? 0.f : -4.0f * mu * rs;
    float P[4], Q[4];
#pragma unroll
    for (int i = 0; i < 4; ++i) {
      P[i] = __builtin_fmaf(__builtin_fmaf(sgn, sb[i], s1[i]), rs, off);
      Q[i] = __builtin_fmaf(sgn, d2[i], d1[i]) * rs;
    }
    const int c = l32 >> 1, row = 2 * w + (lane >> 5);
    const unsigned o = L0 + L_A + (c * 16 + (row ^ (c & 7))) * 16 + (l32 & 1) * 8;
    *(LAS v2u*)(size_t)o = v2u{pkh(P[0], P[1]), pkh(P[2], P[3])};
    *(LAS v2u*)(size_t)(o + 4096) = v2u{pkh(Q[0], Q[1]), pkh(Q[2], Q[3])};
  }
  W_DMA(6, 7)
  {
    const rsrc_t rf = mkrs((w & 1) ? wi : wr, S_ * DF_ * 2);
    const unsigned fo = (unsigned)(((j1b + 16 * (32 * (w >> 1) + (lane >> 1))) * DF_ + fc0 + 8 * (lane & 1)) * 2);
    asm volatile("buffer_load_dwordx4 %0, %1, %2, 0 offen" : "=&v"(fraw) : "v"(fo), "s"(rf) : "memory");
  }
  VMW(8);
  BAR();
  STAMPF(3)
  {
    f32x4 zre = {0, 0, 0, 0}, zim = {0, 0, 0, 0};
#pragma unroll
    for (int kc = 0; kc < 4; ++kc) {
      const int c = 4 * kc + q4;
      const unsigned o = L0 + L_A + (c * 16 + (r ^ (c & 7))) * 16;
      zre = mfma16h(lds16(o), bc[kc], zre);
      zim = mfma16h(lds16(o + 4096), bs[kc], zim);
    }
    v4u zop;
#pragma unroll
    for (int i = 0; i < 4; ++i) zop[i] = pkh(zre[i], zim[i]);
    const f32x4 z4 = {0, 0, 0, 0};
    const f32x4 tre = mfma16h(zop, mre, z4), tim = mfma16h(zop, mim, z4);
    const unsigned to = (unsigned)(r * 16 * 128 * 128 + q4 * 16);
    const unsigned tso = (unsigned)((2 * w * 128 + s2) * 128 + q * 64);
    st16(v4u{pkh(tre[0], tim[0]), tag, pkh(tre[1], tim[1]), tag}, rws, to, tso);
    st16(v4u{pkh(tre[2], tim[2]), tag, pkh(tre[3], tim[3]), tag}, rws, w == 7 ? OOB : to, tso + 128 * 128);
  }
  STAMPF(4)
  MEMB();
  PLO();
  const int arow = 16 * w3 + 4 * (r >> 2) + 2 * (r & 1) + 64 * ((r >> 1) & 1) + par;
  v4u fre[4], fim[4];
  {
    const float af = (float)arow * (1.0f / 128.0f);
    const f32x2 s1 = cis(af), s16 = cis(16.0f * af);
    f32x2 z[4][4];
    z[0][0] = cis((float)(4 * q4) * af);
    z[0][1] = crot(z[0][0], s1);
    z[0][2] = cheb(2.0f * s1.x, z[0][1], z[0][0]);
    z[0][3] = cheb(2.0f * s1.x, z[0][2], z[0][1]);
#pragma unroll
    for (int e = 0; e < 4; ++e) {
      z[1][e] = crot(z[0][e], s16);
      z[2][e] = cheb(2.0f * s16.x, z[1][e], z[0][e]);
      z[3][e] = cheb(2.0f * s16.x, z[2][e], z[1][e]);
    }
#pragma unroll
    for (int g = 0; g < 4; ++g)
#pragma unroll
      for (int i = 0; i < 4; ++i) {
        const f32x2 v = z[g][2 * (i & 1) + (i >> 1)];
        fre[g][i] = pkh(v.x, v.y); fim[g][i] = pkh(-v.y, v.x);
      }
#pragma unroll
    for (int g = 0; g < 4; ++g) asm volatile("" : "+v"(fre[g]), "+v"(fim[g]));
  }
  v4u wf[28];
  float cs0 = 0.f, cs1 = 0.f;
  const unsigned trb = L0 + (unsigned)((((lane >> 5) * 4 + ((lane >> 4) & 1)) * 1024) + ((lane >> 2) & 3) * 256 +
                                       (((2 * w + ((lane >> 1) & 1)) ^ (2 * (((lane >> 2) & 3) + 4 * ((lane >> 4) & 1)))) * 16) + (lane & 1) * 8);
#define W_READ(n0, n1)                                                                   \
  if (!SPARE) {                                                                          \
    unsigned t0 = trb, t1 = trb + 65536, t2 = trb + 131072;                              \
    asm volatile("" : "+v"(t0), "+v"(t1), "+v"(t2));                                     \
    _Pragma("unroll") for (int n_ = (n0); n_ < (n1); ++n_) {                             \
      const int sl = wslot(n_);                                                          \
      const unsigned a = (sl < 8 ? t0 : sl < 16 ? t1 : t2) + (sl & 7) * 8192;            \
      const v2u lo = ldtr(a), hi = ldtr(a + 2048);                                       \
      wf[n_] = v4u{lo.x, lo.y, hi.x, hi.y};                                              \
    }                                                                                    \
    _Pragma("unroll") for (int n_ = (n0); n_ < (n1); ++n_) asm volatile("" : "+v"(wf[n_])); \
  }
#define W_SUM(n0, n1)                                                                    \
  if (!SPARE) {                                                                          \
    _Pragma("unroll") for (int n_ = (n0); n_ < (n1); ++n_) {                             \
      cs0 = sum2(wf[n_][0], cs0); cs1 = sum2(wf[n_][1], cs1);                            \
      cs0 = sum2(wf[n_][2], cs0); cs1 = sum2(wf[n_][3], cs1);                            \
    }                                                                                    \
    asm volatile("" : "+v"(cs0), "+v"(cs1));                                             \
  }
  float fa_[4], fb_[4];
  asm volatile("s_waitcnt vmcnt(2)" : "+v"(fraw), "+v"(junk)::"memory");
  *(LAS v4u*)(size_t)(L0 + L_F + (w & 1) * 4096 + (w >> 1) * 1024 + lane * 16) = fraw;
  BAR();
  FLAG(0)
  W_READ(0, 7)
  {
    const unsigned fa = L0 + L_F + (unsigned)((16 * w3 + 4 * q4 + par) * 32 + ((kb <= 224 ? kb : 224) - fc0) * 2);
    float sc = SCALE_B * 0.125f;
    if (kb == 0 || kb == 224) sc = 0.5f * SCALE_B * 0.125f;
    if (kb > 224) sc = 0.f;
#pragma unroll
    for (int i = 0; i < 4; ++i) {
      fa_[i] = blo(*(LAS const unsigned short*)(size_t)(fa + (2 * (i & 1) + 64 * (i >> 1)) * 32)) * sc;
      fb_[i] = blo(*(LAS const unsigned short*)(size_t)(fa + (2 * (i & 1) + 64 * (i >> 1)) * 32 + 4096)) * sc;
    }
#pragma unroll
    for (int i = 0; i < 4; ++i) asm volatile("" : "+v"(fa_[i]), "+v"(fb_[i]));
  }
  BAR();
  STAMPF(5)
  if (DLY_T) __builtin_amdgcn_s_sleep(DLY_T);
  const int dt = w < 7 ? w : 6;
  const int srow = 128 * r + s2;
  v4u ce[4], co[4], se[4], so[4];
  {
    const unsigned tso = (unsigned)((j1b * 16 + tileb) * 16384 + w * 1024);
    const unsigned tvo = tileb < 15 ? (unsigned)lane * 16 : OOB;
    v4u g0, g1;
    PHI();
    ald16<0>(g0, rws, tvo, tso); ald16<0>(g1, rws, tvo, tso + 8192);
    PLO();
    SB();
    {
      f32x2 m = 0.25f * cis((float)(4 * q4 * srow) * (1.0f / 2048.0f));
      const f32x2 ms = cis((float)srow * (1.0f / 2048.0f));
#pragma unroll
      for (int i = 0; i < 4; ++i) {
        mre[i] = pkh(m.x, -m.y); mim[i] = pkh(m.y, m.x);
        if (i < 3) m = crot(m, ms);
      }
      const float df = (float)(2 * (16 * dt + r) + 1) * (1.0f / 896.0f);
      const f32x2 st = cis(df), rk = cis(64.0f * df);
      const float c2 = 2.0f * st.x;
      f32x2 z0 = cis((float)(16 * q4) * df), z1 = crot(z0, st);
#pragma unroll
      for (int kc = 0; kc < 4; ++kc) {
        W_DMA(7 + kc, 8 + kc)
        f32x2 cs[16];
        cs[0] = z0; cs[1] = z1;
        if (kc < 3) { z0 = crot(z0, rk); z1 = crot(z1, rk); }
#pragma unroll
        for (int e = 2; e < 16; ++e) cs[e] = cheb(c2, cs[e - 1], cs[e - 2]);
#pragma unroll
        for (int j = 0; j < 4; ++j) {
          ce[kc][j] = pkh(cs[4 * j].x, cs[4 * j + 2].x); co[kc][j] = pkh(cs[4 * j + 1].x, cs[4 * j + 3].x);
          se[kc][j] = pkh(cs[4 * j].y, cs[4 * j + 2].y); so[kc][j] = pkh(cs[4 * j + 1].y, cs[4 * j + 3].y);
        }
        asm volatile("" : "+v"(ce[kc]), "+v"(co[kc]), "+v"(se[kc]), "+v"(so[kc]));
        SB();
      }
      asm volatile("" : "+v"(mre), "+v"(mim));
    }
    STAMPF(6)
    PHI();
    asm volatile("s_waitcnt vmcnt(4)" : "+v"(g0), "+v"(g1)::"memory");
    int spins = tileb < 15 ? 0 : SPIN_MAX;
    while (!__all(g0.y == tag && g0.w == tag && g1.y == tag && g1.w == tag) && ++spins <= SPIN_MAX) {
      ald16<0>(g0, rws, tvo, tso); ald16<0>(g1, rws, tvo, tso + 8192);
      asm volatile("s_waitcnt vmcnt(0)" : "+v"(g0), "+v"(g1)::"memory");
    }
    CNT(20, tileb < 15 ? spins : 0)
    const int m = 8 * w + (lane >> 3);
    const unsigned o = L0 + L_BT + (unsigned)(((m >> 4) * 64 + ((m >> 2) & 3) * 16 + 4 * (lane & 3) + ((lane >> 2) & 1)) * 16 + (((m >> 1) & 1) + 2 * (m & 1)) * 4);
    *(LAS unsigned*)(size_t)o = hadd(g0.x, g1.x);
    *(LAS unsigned*)(size_t)(o + 32) = hadd(g0.z, g1.z);
    *(LAS unsigned*)(size_t)(o + 4096) = hsub(g0.x, g1.x);
    *(LAS unsigned*)(size_t)(o + 4096 + 32) = hsub(g0.z, g1.z);
    W_DMA(11, 13)
    STAMP(7)
    BAR();
    f32x4 vre = {0, 0, 0, 0}, vim = {0, 0, 0, 0};
    {
      v4u tf[4];
#pragma unroll
      for (int g = 0; g < 4; ++g) tf[g] = lds16(L0 + L_BT + par * 4096 + g * 1024 + lane * 16);
      W_DMA(13, 14)
#pragma unroll
      for (int g = 0; g < 4; ++g) { vre = mfma16h(fre[g], tf[g], vre); vim = mfma16h(fim[g], tf[g], vim); }
    }
    W_DMA(14, 15)
    float pr[4], pi[4];
#pragma unroll
    for (int i = 0; i < 4; ++i) { pr[i] = vre[i] * fa_[i] - vim[i] * fb_[i]; pi[i] = -(vre[i] * fb_[i] + vim[i] * fa_[i]); }
    const unsigned po = L0 + L_BP + w3 * 1024 + lane * 16 + par * 8;
    *(LAS v2u*)(size_t)po = v2u{pkh(pr[0] + pr[2], pi[0] + pi[2]), pkh(pr[1] + pr[3], pi[1] + pi[3])};
    *(LAS v2u*)(size_t)(po + 4096) = v2u{pkh(pr[0] - pr[2], pi[0] - pi[2]), pkh(pr[1] - pr[3], pi[1] - pi[3])};
    W_DMA(15, 17)
    BAR();
    f32x4 ure = {0, 0, 0, 0}, uim = {0, 0, 0, 0};
    {
      v4u pf[4];
#pragma unroll
      for (int g = 0; g < 4; ++g) pf[g] = lds16(L0 + L_BP + par * 4096 + g * 1024 + lane * 16);
      W_DMA(17, 18)
#pragma unroll
      for (int g = 0; g < 4; ++g) { ure = mfma16h(pf[g], fre[g], ure); uim = mfma16h(pf[g], fim[g], uim); }
    }
    const unsigned uo = tileb < 15 ? (unsigned)(arow * 16384 + q4 * 16) : OOB, uso = U_OFF + (unsigned)((tileb * 16 + j1b) * 64);
    st16(v4u{pkh(ure[0] * 0.125f, uim[0] * -0.125f), pkh(ure[1] * 0.125f, uim[1] * -0.125f), pkh(ure[2] * 0.125f, uim[2] * -0.125f),
             pkh(ure[3] * 0.125f, uim[3] * -0.125f)}, rfx, uo, uso);
  }
  STAMP(8)
  MEMB();
  PLO();
  W_DMA(18, 23)
  v4u xv0, xv1;
  {
    const int lc = l32 < 28 ? l32 : 27, row = 2 * w + (lane >> 5);
    const unsigned o = L0 + L_X + (unsigned)((lc * 16 + (row ^ (lc & 15))) * 16);
    *(LAS v4u*)(size_t)o = v4u{xa[0].x, xa[0].y, xa[2].x, xa[2].y};
    *(LAS v4u*)(size_t)(o + 7168) = v4u{xa[1].x, xa[1].y, xa[3].x, xa[3].y};
    VMW(6);
    BAR();
    if (b == 0 && tid == 0) __builtin_amdgcn_raw_buffer_store_b32(tag, rfx, 0, EP_OFF, 16);
    W_READ(7, 18)
    const int lr = 4 * dt + q4;
    const unsigned o2 = L0 + L_X + (unsigned)((lr * 16 + (r ^ (lr & 15))) * 16);
    xv0 = lds16(o2); xv1 = lds16(o2 + 7168);
    asm volatile("" : "+v"(xv0), "+v"(xv1));
    BAR();
  }
  W_DMA(23, 28)
  const rsrc_t rd = mkrs(wd, F_ * D_ * 2);
  const unsigned dvoff = (unsigned)((lane >> 3) * 896 + (((lane & 7) ^ (4 * ((lane >> 4) & 1))) * 16));
  const unsigned dsoff = (unsigned)(224 * w * 896 + nE * 128);
  int erot = 64 + (nE + 2 * (yb & 3)) % 7;
  asm volatile("" : "+s"(erot));
#define ER(i) ((i) + erot >= 71 ? (i) + erot - 71 : (i) + erot - 64)
#define WD_DMA1(j)                                                                                     \
  {                                                                                                    \
    if (SPARE) { if ((j) < 7) ast16(sent4, rfx, tid * 16, clr0 + (28 + (j)) * 8192); }                 \
    else dma(rd, dvoff, dsoff + (32 * ER((j) >> 2) + 8 * (((j) >> 1) & 1) + 16 * ((j) & 1)) * 896, L0 + (dslot(j) * 8 + w) * 1024); \
  }
#define WD_DMA(j0, j1) _Pragma("unroll") for (int j_ = (j0); j_ < (j1); ++j_) WD_DMA1(j_)
  STAMP(9)
  if (DLY_U) __builtin_amdgcn_s_sleep(DLY_U);
  {
    v2u ug[4];
    unsigned fl;
    const unsigned uso = U_OFF + (unsigned)((s2 * 16 + 2 * w) * 1024);
    const unsigned uo = (w == 7 && r >= 8) ? OOB : (unsigned)((r >> 3) * 1024 + q4 * 256 + (r & 7) * 8);
#define U_LOAD()                                                                                       \
  ald4(fl, rfx, l32 * 16, FL_OFF);                                                                      \
  ald8<0>(ug[0], rfx, uo, uso); ald8<64>(ug[1], rfx, uo, uso); ald8<128>(ug[2], rfx, uo, uso); ald8<192>(ug[3], rfx, uo, uso);
    PHI();
    U_LOAD()
    PLO();
    WD_DMA(0, 2)
    W_SUM(0, 18)
    VMW(7);
    BAR();
    FLAG(1)
    W_READ(18, 28)
    STAMP(10)
    PHI();
    asm volatile("s_waitcnt vmcnt(2)" : "+v"(ug[0]), "+v"(ug[1]), "+v"(ug[2]), "+v"(ug[3]), "+v"(fl)::"memory");
    int spins = 0;
#define U_BAD (max4(v4u{ug[0].x, ug[0].y, ug[1].x, ug[1].y}, max4(v4u{ug[2].x, ug[2].y, ug[3].x, ug[3].y}, 0)) == SENT || fl != tag)
    while (__any(U_BAD) && ++spins <= SPIN_MAX) {
      U_LOAD()
      asm volatile("s_waitcnt vmcnt(0)" : "+v"(ug[0]), "+v"(ug[1]), "+v"(ug[2]), "+v"(ug[3]), "+v"(fl)::"memory");
    }
    CNT(21, spins)
    STAMP(11)
    const f32x4 z4 = {0, 0, 0, 0};
#pragma unroll
    for (int u = 0; u < 2; ++u) {
      const v4u uf = {ug[0][u], ug[1][u], ug[2][u], ug[3][u]};
      const f32x4 yre = mfma16h(uf, mre, z4), yim = mfma16h(uf, mim, z4);
      const int c = 2 * w + (q4 >> 1);
      const unsigned o = L0 + L_C + u * 8192 + (c * 16 + (r ^ (c & 7))) * 16 + (q4 & 1) * 8;
      *(LAS v2u*)(size_t)o = v2u{pkh(yre[0], yre[1]), pkh(yre[2], yre[3])};
      *(LAS v2u*)(size_t)(o + 4096) = v2u{pkh(yim[0], yim[1]), pkh(yim[2], yim[3])};
    }
    BAR();
    WD_DMA(2, 4)
    f32x4 Ce = z4, Co = z4, Se = z4, So = z4;
#pragma unroll
    for (int kc = 0; kc < 4; ++kc) {
      const int c = 4 * kc + q4;
      const unsigned o = L0 + L_C + (c * 16 + (r ^ (c & 7))) * 16;
      Ce = mfma16h(ce[kc], lds16(o), Ce);
      Se = mfma16h(se[kc], lds16(o + 4096), Se);
      Co = mfma16h(co[kc], lds16(o + 8192), Co);
      So = mfma16h(so[kc], lds16(o + 12288), So);
    }
    v4u hq[2];
#pragma unroll
    for (int g = 0; g < 2; ++g) {
      const int gg = g ^ q;
      const float sg = gg ? 1.0f : -1.0f;
      const v4u xs = gg ? v4u{__builtin_amdgcn_alignbit(xv1.y, xv1.y, 16), __builtin_amdgcn_alignbit(xv1.x, xv1.x, 16),
                              __builtin_amdgcn_alignbit(xv1.w, xv1.w, 16), __builtin_amdgcn_alignbit(xv1.z, xv1.z, 16)} : xv0;
      float lo[4], hi[4];
#pragma unroll
      for (int i = 0; i < 4; ++i) {
        const float a = __builtin_fmaf(sg, Se[i], Ce[i]), bb = __builtin_fmaf(sg, Co[i], So[i]);
        const float x0 = (i & 1) ? bhi(xs[i >> 1]) : blo(xs[i >> 1]), x1 = (i & 1) ? bhi(xs[2 + (i >> 1)]) : blo(xs[2 + (i >> 1)]);
        lo[i] = __builtin_fmaf(a - bb, SCALE_Y, x0); hi[i] = __builtin_fmaf(a + bb, SCALE_Y, x1);
      }
      hq[g] = v4u{pkb(lo[0], lo[1]), pkb(lo[2], lo[3]), pkb(hi[0], hi[1]), pkb(hi[2], hi[3])};
      if (g == 0) {
        const unsigned lo_ = w < 7 ? (unsigned)((q4 * 64 + r) * 16) : OOB;
        const unsigned lso = LH_OFF + (unsigned)((s2 >> 2) * LH_STR + ((((s2 >> 1) & 1) * 28 + 4 * dt) * 64 + 16 * (s2 & 1) + 32 * q) * 16);
        st16(hq[0], rfx, lo_, lso);
        MEMB();
        STAMP(12)
        SB();
      }
    }
    PLO();
    WD_DMA(4, 8)
    float sum = 0.f;
#pragma unroll
    for (int g = 0; g < 2; ++g)
#pragma unroll
      for (int i = 0; i < 4; ++i) sum += blo(hq[g][i]) + bhi(hq[g][i]);
    sum = xor32_add(xor16_add(sum));
    const float lm = sum * (1.0f / 64.0f);
    float m2 = 0.f;
#pragma unroll
    for (int g = 0; g < 2; ++g)
#pragma unroll
      for (int i = 0; i < 4; ++i) {
        const float d0_ = blo(hq[g][i]) - lm, d1_ = bhi(hq[g][i]) - lm;
        m2 = __builtin_fmaf(d0_, d0_, m2); m2 = __builtin_fmaf(d1_, d1_, m2);
      }
    m2 = xor32_add(xor16_add(m2));
    if (q4 == 0) *(LAS f32x2*)(size_t)(L0 + L_ST + (r * 8 + w) * 8) = f32x2{lm, m2};
    BAR();
    if (w == 7 && q == 0) {
      PHI();
      f32x4 sv[4];
#pragma unroll
      for (int j = 0; j < 4; ++j) sv[j] = *(LAS const f32x4*)(size_t)(L0 + L_ST + r * 64 + j * 16);
      const float mean = (((sv[0].x + sv[0].z) + (sv[1].x + sv[1].z)) + ((sv[2].x + sv[2].z) + sv[3].x)) * (1.0f / 7.0f);
      float M2 = 0.f;
#pragma unroll
      for (int j = 0; j < 7; ++j) {
        const float dlt = sv[j >> 1][2 * (j & 1)] - mean;
        M2 += __builtin_fmaf(64.0f * dlt, dlt, sv[j >> 1][2 * (j & 1) + 1]);
      }
      const float rs = __builtin_amdgcn_rsqf(M2 * (1.0f / 448.0f) + 1e-5f);
      const float m0 = -mean, m1 = m0 - trunc16(m0), m2_ = m1 - trunc16(m1);
      st16(v4u{hi16(m0, m0), hi16(m0, m1), hi16(m1, m2_), hi16(rs, rs - trunc16(rs))}, rfx, q4 == 0 ? (unsigned)r * 16 : OOB, SS_OFF + s2 * 256);
      MEMB();
      PLO();
    }
  }
  if (SPARE) {
    VMW(0);
    BAR();
    FLAG(2)
  } else {
    WD_DMA(8, 12)
    v4u wdf[7][4];
    const unsigned tdb = L0 + (unsigned)(w * 1024 + (q4 >> 1) * 8192 + (4 * (q4 & 1) + ((lane >> 2) & 3)) * 128 + ((lane >> 3) & 1) * 64 + (lane & 3) * 16);
    unsigned td0 = tdb;
#define WD_READ(u)                                                                                     \
  {                                                                                                    \
    constexpr int sl_ = dslot(2 * (u));                                                                \
    asm volatile("" : "+v"(td0));                                                                      \
    const unsigned ta_ = td0 + (sl_ & ~7) * 8192, tb_ = ta_ ^ 64;                                      \
    _Pragma("unroll") for (int nt = 0; nt < 4; ++nt) {                                                 \
      const v2u t_ = ldtr(((nt >> 1) ? tb_ : ta_) + (sl_ & 7) * 8192 + (nt & 1) * 8);                  \
      wdf[(u) >> 1][nt][2 * ((u) & 1)] = t_.x; wdf[(u) >> 1][nt][2 * ((u) & 1) + 1] = t_.y;            \
    }                                                                                                  \
  }
    v4u csf;
    W_SUM(18, 28)
    {
      const float c0_ = xor32_add(cs0 + cs1), c1_ = c0_ - trunc16(c0_), c2_ = c1_ - trunc16(c1_);
      csf = v4u{hi16(c0_, c1_), hi16(c2_, c0_), hi16(c1_, c0_), 0u};
      if (lane >= 32) csf = v4u{0u, 0u, 0u, 0u};
      asm volatile("" : "+v"(csf));
    }
    VMW(4);
    WD_READ(0) WD_READ(1) WD_READ(2) WD_READ(3)
    BAR();
    STAMP(13)
    if (DLY_L) __builtin_amdgcn_s_sleep(DLY_L);
    const unsigned lnso = LH_OFF + (unsigned)(2 * mG) * LH_STR;
    const unsigned la = L0 + lane * 16;
#define LN_DMA(i)                                                                                      \
  {                                                                                                    \
    const int pi_ = 8 * (i) + w, tt_ = pi_ / 28, j_ = pi_ - 28 * tt_;                                  \
    dma1(rfx, lane * 16, lnso + ((i) >= 7 ? LH_STR - 57344 : 0) + (28 * tt_ + KROT(j_)) * 1024, L0 + pi_ * 1024); \
  }
#define LN_MAX(i0, i1) \
  _Pragma("unroll") for (int i_ = (i0); i_ < (i1); ++i_) mx = max4(lds16((i_ < 7 ? la : la + 57344) + w * 1024 + (i_ < 7 ? i_ : i_ - 7) * 8192), mx);
#define LN_FIX(i0, i1)                                                                                 \
  if (__any(mx == SENT)) {                                                                             \
    int spins = 0;                                                                                     \
    do {                                                                                               \
      _Pragma("unroll") for (int i_ = (i0); i_ < (i1); ++i_) LN_DMA(i_)                                \
      VMW(0);                                                                                          \
      mx = 0;                                                                                          \
      LN_MAX(i0, i1)                                                                                   \
      lspins += 1 << (8 * ((i0) / 3));                                                                 \
    } while (__any(mx == SENT) && ++spins < SPIN_MAX);                                                 \
  }
    unsigned mx = 0;
    int lspins = 0;
    PHI();
#pragma unroll
    for (int i = 0; i < 4; ++i) LN_DMA(i)
    PLO();
    {
      const unsigned pb = (unsigned)(28672 + (96 * nE + 12 * w + lane) * 128);
      ald4(junk, rfx, lane < 12 ? (pb >= 57344 ? pb + (LH_STR - 57344) : pb) : OOB, lnso);
    }
    VMW(1);
    LN_MAX(0, 4)
    LN_FIX(0, 4)
    BAR();
    STAMP(14)
    f32x16 acc, accp;
    v4u af[NAF], ho, stf;
    v4u hb[4][7];
    unsigned fl;
    float rsv = 0.f, rlv = 0.f;
    const unsigned hso = LH_OFF + (unsigned)((2 * mG + (b & 1)) * LH_STR + 57344 + (8 * nE + w) * 1024);
    unsigned hvo = (unsigned)(q4 * 512 + pE * 256 + r * 16);
    asm volatile("" : "+v"(hvo));
    const unsigned hlo = LH_OFF + (unsigned)((2 * mE + (w >> 2)) * LH_STR + 57344 + 14 * (w & 3) * 1024);
    const unsigned sso = SS_OFF + (unsigned)(mG * 2048);
#define H_LOAD(tt, i) hb[tt][i] = ld16(rfx, hvo, hlo + (tt) * 57344 + ER(i) * 2048);
#define H_TOUCH(tt) touch(junk, rfx, hvo, hlo + (tt) * 57344 + ER(0) * 2048);
#define LGK3() asm volatile("s_waitcnt lgkmcnt(3)" ::: "memory")
#define VMWS(n) asm volatile("s_waitcnt vmcnt(" #n ")" : "+v"(stf), "+v"(fl), "+v"(junk)::"memory")
#define FL_FIX(i)                                                                                      \
  if (__any(fl != tag)) {                                                                              \
    int spins = 0;                                                                                     \
    do {                                                                                               \
      ald4(fl, rfx, l32 * 16, FL_OFF + (i) * 512);                                                      \
      VMWS(0);                                                                                         \
      lspins += 1 << 24;                                                                               \
    } while (__any(fl != tag) && ++spins < SPIN_MAX);                                                  \
  }
    float ge = 0.f, gp = 0.f;
#define GLU_STEP(k)                                                                                    \
  {                                                                                                    \
    const int e_ = (k) >> 1;                                                                           \
    if (((k) & 1) == 0) {                                                                              \
      ge = __builtin_amdgcn_exp2f(accp[e_] * rlv);                                                     \
      asm volatile("" : "+v"(ge));                                                                     \
    } else {                                                                                           \
      const float p_ = __builtin_amdgcn_rcpf(1.0f + ge) * (accp[8 + e_] * rsv);                        \
      if (e_ & 1) { unsigned t_ = pkb(gp, p_); asm volatile("" : "+v"(t_)); ho[e_ >> 1] = t_; }        \
      else { gp = p_; asm volatile("" : "+v"(gp)); }                                                   \
    }                                                                                                  \
  }
    auto dpass = [&](auto TT) __attribute__((always_inline)) {
      constexpr int tt = decltype(TT)::value;
      unsigned lbt = la + tt * 28672;
      asm volatile("" : "+v"(lbt));
      __builtin_amdgcn_s_setprio(3);
#pragma unroll
      for (int i = 0; i < NAF - 1; ++i) af[i] = lds16(lbt + i * 1024);
#pragma unroll
      for (int i = 0; i < 16; ++i) acc[i] = 0.f;
      mx = 0;
      SB();
#pragma unroll
      for (int ks = 0; ks < 28; ++ks) {
        if (ks + NAF - 1 < 28) af[(ks + NAF - 1) % NAF] = lds16(lbt + (ks + NAF - 1) * 1024);
        acc = mfma32b(wf[ks], af[ks % NAF], acc);
        SB();
        if (ks == (tt ? 9 : 0)) ald16<tt * 512>(stf, rfx, l32 * 16, sso);
        if (tt > 0) {
          if (ks < 16) GLU_STEP(ks)
          if (ks == 16) { st16(ho, rfx, lane * 16, hso + (tt - 1) * 57344); MEMB(); }
        }
        if (ks == 9) __builtin_amdgcn_s_setprio(2);
        if (ks == 19 && tt < 3) __builtin_amdgcn_s_setprio(1);
        if (tt == 0) {
          if (ks == 1) ald4(fl, rfx, l32 * 16, FL_OFF + 512);
          if (ks >= 2 && ks < 22 && (ks & 1) == 0) LN_DMA(3 + (ks >> 1))
          if (ks == 21) WD_DMA1(12)
          if (ks == 22) WD_DMA1(13)
          if (ks == 4) WD_READ(4)
          if (ks == 7) WD_READ(5)
          if (ks == 24) { VMWS(9); LN_MAX(4, 7) }
        }
        if (tt == 1) {
          if (ks < 8 && (ks & 1) == 1) WD_DMA1(14 + (ks >> 1))
          if (ks == 10 || ks == 12) WD_DMA1(18 + ((ks - 10) >> 1))
          if (ks == 17) { VMW(8); WD_READ(6) }
          if (ks == 18) LN_MAX(7, 10)
          if (ks == 20) LN_MAX(10, 14)
          if (ks == 22) { LGK3(); WD_DMA1(20) }
          if (ks == 24) WD_DMA1(21)
          if (ks == 26) VMWS(5);
        }
        if (tt == 2) {
          if (ks < 8 && (ks & 1) == 0) WD_DMA1(22 + (ks >> 1))
          if (ks == 17) WD_READ(7)
          if (ks == 20) { LGK3(); WD_DMA1(26) }
          if (ks == 22) WD_DMA1(27)
          if (ks == 24) H_TOUCH(0)
          if (ks == 26) VMWS(4);
        }
        if (tt == 3) {
          if (ks == 1) H_TOUCH(1)
          if (ks >= 3 && ks <= 11 && (ks & 1) == 1) H_LOAD(0, (ks - 3) >> 1)
          if (ks == 15 || ks == 17) H_LOAD(0, (ks - 5) >> 1)
          if (ks >= 19 && ks <= 25 && (ks & 1) == 1) H_LOAD(1, (ks - 19) >> 1)
          if (ks == 9) ald4(fl, rfx, l32 * 16, FL_OFF + 1024);
          if (ks == 8) WD_READ(8)
          if (ks == 14) WD_READ(9)
          if (ks == 18) WD_READ(10)
          if (ks == 20) WD_READ(11)
          if (ks == 22) WD_READ(12)
          if (ks == 24) { VMWS(7); WD_READ(13) }
        }
        SB();
      }
      if (__any(max4(stf, 0) == SENT)) {
        int spins = 0;
        do {
          ald16<tt * 512>(stf, rfx, l32 * 16, sso);
          VMWS(0);
          lspins += 1 << 16;
        } while (__any(max4(stf, 0) == SENT) && ++spins < SPIN_MAX);
      }
      accp = mfma32b(csf, stf, acc);
      rsv = blo(stf.w) + bhi(stf.w);
      rlv = rsv * -1.44269504f;
      if (tt == 0) { LN_FIX(4, 7) FL_FIX(1) }
      if (tt == 1) LN_FIX(7, 14)
      if (tt == 3) FL_FIX(2)
      if (tt < 3) BAR();
      if (tt == 0) STAMP(15)
      if (tt == 2) STAMP(2)
    };
    dpass(IC<0>{}); dpass(IC<1>{}); dpass(IC<2>{}); dpass(IC<3>{});
    CNT(22, lspins)
    STAMP(16)
    int espins = 0;
    f32x4 ea[2][4];
    unsigned ln2 = la;
    asm volatile("" : "+v"(ln2));
    ln2 = (ln2 - L0) >> 4;
    unsigned hr[4], hro, sel0, pa[4], ra, ra3, ovo, hra;
    int hstep;
    v2u hr0, hr1;
    f32x2 rp[2][8];
    const unsigned hrs = LH_OFF + (unsigned)((2 * mE + (w >> 2)) * LH_STR + ((w >> 1) & 1) * 28672 + pE * 256);
    const rsrc_t ro = mkrs(out, S_ * D_ * 2);
#define HR_LOAD() { hr0 = ld8(rfx, hro, hrs); hr1 = ld8(rfx, hro + hstep, hrs); }
#define H_FIX(tt)                                                                                      \
  {                                                                                                    \
    int spins = 0;                                                                                     \
    do {                                                                                               \
      MEMB();                                                                                          \
      _Pragma("unroll") for (int i = 0; i < 7; ++i) H_LOAD(tt, i)                                      \
      mx = 0;                                                                                          \
      _Pragma("unroll") for (int i = 0; i < 7; ++i) mx = max4(hb[tt][i], mx);                          \
      espins += 1 << (8 * (tt));                                                                       \
    } while (__any(mx == SENT) && ++spins < SPIN_MAX);                                                 \
  }
#define E_ZERO(tt) _Pragma("unroll") for (int nt = 0; nt < 4; ++nt) ea[(tt) & 1][nt] = f32x4{0, 0, 0, 0};
#define E_MM(tt, k) ea[(tt) & 1][(k) & 3] = mfma16b(wdf[(k) >> 2][(k) & 3], hb[tt][(k) >> 2], ea[(tt) & 1][(k) & 3]);
#define E_STEP(tt, k)                                                                                  \
  E_MM(tt, k)                                                                                          \
  if (((k) & 3) == 1) { mx = max4(hb[tt][(k) >> 2], mx); asm volatile("" : "+v"(mx)); }
#define E_END(tt)                                                                                      \
  if (__any(mx == SENT)) {                                                                             \
    H_FIX(tt) E_ZERO(tt)                                                                               \
    _Pragma("unroll") for (int k = 0; k < 28; ++k) E_MM(tt, k)                                         \
  }                                                                                                    \
  mx = 0;
#define E_PUT(tt, nt)                                                                                  \
  {                                                                                                    \
    const int sl_ = pslot(tt, nt);                                                                     \
    *(LAS f32x4*)(size_t)((pa[nt] + (sl_ & ~7) * 8192) + (sl_ & 7) * 8192) = ea[(tt) & 1][nt];         \
  }
#define R_READ(tt, w0, w1)                                                                             \
  {                                                                                                    \
    asm volatile("" : "+v"(ra));                                                                       \
    const unsigned rb_ = (tt) == 3 ? ra3 : ra + pslot(tt, 0) * 8192;                                   \
    _Pragma("unroll") for (int ww = (w0); ww < (w1); ++ww) rp[(tt) & 1][ww] = *(LAS const f32x2*)(size_t)(rb_ + ww * 1024); \
  }
#define R_FIN(tt)                                                                                      \
  {                                                                                                    \
    const f32x2* p_ = rp[(tt) & 1];                                                                    \
    const f32x2 s_ = ((p_[0] + p_[1]) + (p_[2] + p_[3])) + ((p_[4] + p_[5]) + (p_[6] + p_[7]));        \
    const unsigned f_ = pkb(s_.x, s_.y), h_ = hr[tt];                                                  \
    __builtin_amdgcn_raw_buffer_store_b32(pkb(blo(h_) + blo(f_), bhi(h_) + bhi(f_)), ro, ovo, (tt) * 1792, 16); \
  }
    mx = 0;
    E_ZERO(0)
    SB();
#pragma unroll
    for (int k = 0; k < 28; ++k) {
      if (k < 16) GLU_STEP(k)
      if (k == 16) { st16(ho, rfx, lane * 16, hso + 3 * 57344); MEMB(); __builtin_amdgcn_s_setprio(1); }
      if (k < 6 && (k & 1) == 0) H_LOAD(1, 4 + (k >> 1))
      E_MM(0, k)
      if (k >= 17 && k < 24) { mx = max4(hb[0][k - 17], mx); asm volatile("" : "+v"(mx)); }
      if (k == 17) H_LOAD(2, 0)
      if (k == 24) {
#pragma unroll
        for (int nt = 0; nt < 4; ++nt) pa[nt] = L0 + w * 1024 + (ln2 >> 4) * 256 + ((ln2 + (ln2 >> 4) + 4 * nt) & 15) * 16;
        asm volatile("" : "+v"(pa[0]), "+v"(pa[1]), "+v"(pa[2]), "+v"(pa[3]));
      }
      if (k == 26) {
        const int c0 = 64 * nE + 32 * (w & 1) + 8 * (int)(ln2 >> 4);
        const int seg = ((c0 >> 4) * 37) >> 8, hi = seg >> 1, mir = seg & 1, cc = c0 - 224 * hi;
        const int ksa = mir ? (220 - cc) >> 2 : cc >> 2;
        hro = (unsigned)((ksa * 64 + 32 * mir + (ln2 & 15)) * 16 + 8 * hi);
        hstep = mir ? -1024 : 1024;
        sel0 = mir ? 0x05040706u : 0x03020100u;
        asm volatile("" : "+v"(hro), "+v"(hstep), "+v"(sel0));
      }
      SB();
    }
    E_END(0)
    STAMP(17)
    PLO();
    E_ZERO(1)
    SB();
#pragma unroll
    for (int k = 0; k < 28; ++k) {
      E_STEP(1, k)
      if (k < 24 && k % 6 == 2) E_PUT(0, (k / 6) & 3)
      if (k == 0) HR_LOAD()
      if (k >= 4 && k < 4 + 4 * H2N && (k & 3) == 0) H_LOAD(2, k >> 2)
      if (k == 23) {
        const int row = 2 * w + (ln2 >> 5), cp = ln2 & 31, nt = 2 * (cp >> 4) + ((cp >> 1) & 1), qq = (cp >> 2) & 3;
        ra = L0 + (unsigned)(nt * 8192 + qq * 256 + ((row + 4 * nt + qq) & 15) * 16 + (cp & 1) * 8);
        ra3 = ra + (unsigned)(4 + 12 * (cp >> 4)) * 8192;
        hra = L0 + 32768 + (unsigned)((cp >> 4) * 1024 + row * 64 + (cp & 15) * 4);
        ovo = (unsigned)(((128 * row + 8 * mE + pE) * D_ + 64 * nE + 2 * cp) * 2);
        asm volatile("" : "+v"(ra), "+v"(ra3), "+v"(hra), "+v"(ovo));
      }
      SB();
    }
    E_END(1)
    STAMPE(4)
    E_ZERO(2)
    SB();
#pragma unroll
    for (int k = 0; k < 28; ++k) {
      if (k < 2 * (6 - H2N) && (k & 1) == 0) H_LOAD(2, H2N + 1 + (k >> 1))
      if (k >= H3_AT && k < H3_AT + 7 * H3_ST && (k - H3_AT) % H3_ST == 0) H_LOAD(3, (k - H3_AT) / H3_ST)
      if (k < 8 && (k & 1) == 0) E_PUT(1, k >> 1)
      if (k == 8) {
        if (__any(max(max(hr0.x, hr0.y), max(hr1.x, hr1.y)) == SENT)) {
          int spins = 0;
          do {
            MEMB();
            HR_LOAD()
          } while (__any(max(max(hr0.x, hr0.y), max(hr1.x, hr1.y)) == SENT) && ++spins < SPIN_MAX);
        }
        *(LAS v4u*)(size_t)(L0 + 32768 + w * 1024 + ((ln2 & 15) * 4 + (ln2 >> 4)) * 16) =
            v4u{__builtin_amdgcn_perm(hr0.y, hr0.x, sel0), __builtin_amdgcn_perm(hr0.y, hr0.x, sel0 ^ 0x04040404u),
                __builtin_amdgcn_perm(hr1.y, hr1.x, sel0), __builtin_amdgcn_perm(hr1.y, hr1.x, sel0 ^ 0x04040404u)};
      }
      if (k == 11) BAR();
      if (k == 11) { _Pragma("unroll") for (int t = 0; t < 4; ++t) hr[t] = *(LAS const unsigned*)(size_t)(hra + t * 2048); }
      if (k >= 12 && k < 16) R_READ(0, 2 * (k - 12), 2 * (k - 12) + 2)
      if (k >= 17 && k < 21) R_READ(1, 2 * (k - 17), 2 * (k - 17) + 2)
      if (k == 22) R_FIN(0)
      if (k == 26) R_FIN(1)
      E_STEP(2, k)
      SB();
    }
    E_END(2)
    STAMPE(5)
    E_ZERO(3)
    SB();
#pragma unroll
    for (int k = 0; k < 28; ++k) {
      if (k < 8 && (k & 1) == 0) E_PUT(2, k >> 1)
      if (k == 10) BAR();
      if (k >= 10 && k < 14) R_READ(2, 2 * (k - 10), 2 * (k - 10) + 2)
      if (k == 19) R_FIN(2)
      E_STEP(3, k)
      SB();
    }
    E_END(3)
    E_PUT(3, 0) E_PUT(3, 1) E_PUT(3, 2) E_PUT(3, 3)
    CNT(23, espins)
    STAMP(18)
    BAR();
    R_READ(3, 0, 8)
    R_FIN(3)
  }
}

__global__ __launch_bounds__(512, 2) void fused(const uint16_t* __restrict__ x, const uint16_t* __restrict__ wr,
                                                const uint16_t* __restrict__ wi, const uint16_t* __restrict__ wg,
                                                const uint16_t* __restrict__ wu, const uint16_t* __restrict__ wd,
                                                unsigned char* __restrict__ ws, uint16_t* __restrict__ out) {
  __shared__ __attribute__((aligned(16))) unsigned char smem[163840];
  const unsigned L0 = (unsigned)(size_t)smem;
  if (blockIdx.x < 224) body<false>(x, wr, wi, wg, wu, wd, ws, out, L0);
  else body<true>(x, wr, wi, wg, wu, wd, ws, out, L0);
}

extern "C" void kernel_launch(void* const* d_in, const int* in_sizes, int n_in, void* d_out, int out_size, void* d_ws,
                              size_t ws_size, hipStream_t stream) {
  fused<<<256, 512, 0, stream>>>((const uint16_t*)d_in[0], (const uint16_t*)d_in[1], (const uint16_t*)d_in[2],
                                 (const uint16_t*)d_in[4], (const uint16_t*)d_in[5], (const uint16_t*)d_in[6],
                                 (unsigned char*)d_ws, (uint16_t*)d_out);
}
```

```cpp
#include <hip/hip_runtime.h>
#include <hip/hip_bf16.h>
#include <stdint.h>

typedef unsigned v4u __attribute__((ext_vector_type(4)));
typedef unsigned v2u __attribute__((ext_vector_type(2)));
typedef float f32x2 __attribute__((ext_vector_type(2)));
typedef float f32x4 __attribute__((ext_vector_type(4)));
typedef float f32x16 __attribute__((ext_vector_type(16)));
typedef _Float16 h8 __attribute__((ext_vector_type(8)));
typedef _Float16 h2 __attribute__((ext_vector_type(2)));
typedef __bf16 b8 __attribute__((ext_vector_type(8)));
typedef __bf16 b2 __attribute__((ext_vector_type(2)));
typedef short s4v __attribute__((ext_vector_type(4)));
typedef __amdgpu_buffer_rsrc_t rsrc_t;
#define DI __device__ __forceinline__
template <int N> struct IC { static constexpr int value = N; };
#define LAS __attribute__((address_space(3)))

constexpr int S_ = 2048, D_ = 448, F_ = 1792, DF_ = 225;
constexpr unsigned MB = 1u << 20;
constexpr unsigned U_OFF = 0;
constexpr unsigned EP_OFF = 2 * MB;
constexpr unsigned FL_OFF = 2 * MB + 4096;
constexpr unsigned SS_OFF = 2 * MB + 65536;
constexpr unsigned LH_OFF = 3 * MB;
constexpr unsigned LH_STR = 5 * 57344;
constexpr unsigned FIX_BYTES = 16 * MB;
constexpr unsigned REG_BYTES = 4 * MB;
#define NROT 8
constexpr unsigned OOB = 0x80000000u;
constexpr unsigned SENT = 0xffffffffu;
constexpr int SPIN_MAX = 20000;
constexpr unsigned L_SP = 17 * 8192;
constexpr unsigned L_A = L_SP, L_BT = L_SP + 8192, L_BP = L_SP + 16384, L_F = L_BP, L_X = L_SP, L_C = L_SP, L_ST = L_SP + 23552;
constexpr int wslot(int n) { return n < 17 ? n : n - 17; }
constexpr int dslot(int j) {
  return j < 2 ? 11 + j : j < 8 ? j - 2 : j < 14 ? j + 6 : j < 18 ? j : j < 20 ? j - 18 : j < 22 ? j - 2 : j < 26 ? j - 20 : j - 12;
}
constexpr int pslot(int tt, int nt) { return tt == 0 ? 6 + nt : tt == 1 ? nt : tt == 2 ? 14 + nt : nt < 2 ? 11 + nt : 16 + nt; }

#define ROTX 2
#define TMASK 1
#define TSLP 0
#define EV 0
#define H2N 4
#define H3_AT 7
#define H3_ST 3
#define SBM 1
#define NAF 3
#define DLY_T 0
#define DLY_U 0
#define DLY_L 0

#define STAMP_TID 0
#define STAMP(i) {}
#define CNT(i, v)

#define STAMPF(i) STAMP(i)
#define STAMPE(i) {}
DI unsigned pkh(float a, float b) { h2 v = {(_Float16)a, (_Float16)b}; return __builtin_bit_cast(unsigned, v); }
DI unsigned pkb(float a, float b) { b2 v = {(__bf16)a, (__bf16)b}; return __builtin_bit_cast(unsigned, v); }
DI float blo(unsigned u) { return __builtin_bit_cast(float, u << 16); }
DI float bhi(unsigned u) { return __builtin_bit_cast(float, u & 0xffff0000u); }
DI float cosr(float x) { return __builtin_amdgcn_cosf(x); }
DI float sinr(float x) { return __builtin_amdgcn_sinf(x); }
DI f32x2 cis(float x) { return f32x2{cosr(x), sinr(x)}; }
DI f32x2 crot(f32x2 a, f32x2 b) {
  return __builtin_elementwise_fma(f32x2{a.y, a.y}, f32x2{-b.y, b.x}, f32x2{a.x, a.x} * b);
}
DI f32x2 cheb(float c2, f32x2 a1, f32x2 a0) { return __builtin_elementwise_fma(f32x2{c2, c2}, a1, -a0); }
DI unsigned hadd(unsigned a, unsigned b) { return __builtin_bit_cast(unsigned, __builtin_bit_cast(h2, a) + __builtin_bit_cast(h2, b)); }
DI unsigned hsub(unsigned a, unsigned b) { return __builtin_bit_cast(unsigned, __builtin_bit_cast(h2, a) - __builtin_bit_cast(h2, b)); }
DI f32x4 mfma16h(v4u a, v4u b, f32x4 c) {
  return __builtin_amdgcn_mfma_f32_16x16x32_f16(__builtin_bit_cast(h8, a), __builtin_bit_cast(h8, b), c, 0, 0, 0);
}
DI f32x4 mfma16b(v4u a, v4u b, f32x4 c) {
  return __builtin_amdgcn_mfma_f32_16x16x32_bf16(__builtin_bit_cast(b8, a), __builtin_bit_cast(b8, b), c, 0, 0, 0);
}
DI f32x16 mfma32b(v4u a, v4u b, f32x16 c) {
  return __builtin_amdgcn_mfma_f32_32x32x16_bf16(__builtin_bit_cast(b8, a), __builtin_bit_cast(b8, b), c, 0, 0, 0);
}
template <int CTRL> DI float dpp_add(float v) {
  return v + __builtin_bit_cast(float, __builtin_amdgcn_update_dpp(0, __builtin_bit_cast(int, v), CTRL, 0xf, 0xf, true));
}
DI float xor16_add(float v) {
  float a = v, b = v;
  asm("s_nop 1\n\tv_permlane16_swap_b32 %0, %1" : "+v"(a), "+v"(b));
  return a + b;
}
DI float xor32_add(float v) {
  float a = v, b = v;
  asm("s_nop 1\n\tv_permlane32_swap_b32 %0, %1" : "+v"(a), "+v"(b));
  return a + b;
}
DI float sum32(float v) {
  v = dpp_add<0xB1>(v); v = dpp_add<0x4E>(v); v = dpp_add<0x141>(v); v = dpp_add<0x140>(v);
  return xor16_add(v);
}
DI rsrc_t mkrs(const void* p, unsigned bytes) { return __builtin_amdgcn_make_buffer_rsrc((void*)p, (short)0, (int)bytes, 0x00020000); }
DI void dma(rsrc_t rs, unsigned voff, unsigned soff, unsigned lds) {
  asm volatile("s_mov_b32 m0, %0\n\ts_nop 0\n\tbuffer_load_dwordx4 %1, %2, %3 offen lds" ::"s"(lds), "v"(voff), "s"(rs), "s"(soff) : "memory");
}
DI void dma1(rsrc_t rs, unsigned voff, unsigned soff, unsigned lds) {
  asm volatile("s_mov_b32 m0, %0\n\ts_nop 0\n\tbuffer_load_dwordx4 %1, %2, %3 offen sc1 lds" ::"s"(lds), "v"(voff), "s"(rs), "s"(soff) : "memory");
}
template <int OFF> DI void ald8(v2u& d, rsrc_t rs, unsigned voff, unsigned soff) {
  asm volatile("buffer_load_dwordx2 %0, %1, %2, %3 offen offset:%4 sc1" : "=&v"(d) : "v"(voff), "s"(rs), "s"(soff), "n"(OFF) : "memory");
}
template <int OFF> DI void ald16(v4u& d, rsrc_t rs, unsigned voff, unsigned soff) {
  asm volatile("buffer_load_dwordx4 %0, %1, %2, %3 offen offset:%4 sc1" : "=&v"(d) : "v"(voff), "s"(rs), "s"(soff), "n"(OFF) : "memory");
}
DI v4u ld16(rsrc_t rs, unsigned voff, unsigned soff) { return __builtin_amdgcn_raw_buffer_load_b128(rs, voff, soff, 16); }
DI v2u ld8(rsrc_t rs, unsigned voff, unsigned soff) { return __builtin_amdgcn_raw_buffer_load_b64(rs, voff, soff, 16); }
DI void st16(v4u v, rsrc_t rs, unsigned voff, unsigned soff) { __builtin_amdgcn_raw_buffer_store_b128(v, rs, voff, soff, 16); }
DI v4u lds16(unsigned a) { return *(LAS const v4u*)(size_t)a; }
DI v2u ldtr(unsigned a) { return __builtin_bit_cast(v2u, __builtin_amdgcn_ds_read_tr16_b64_v4i16((LAS s4v*)(size_t)a)); }
DI unsigned max4(v4u v, unsigned m) { return max(max(max(max(m, v[0]), v[1]), v[2]), v[3]); }
#define BAR() asm volatile("s_waitcnt lgkmcnt(0)\n\ts_barrier" ::: "memory")
#define VMW(n) asm volatile("s_waitcnt vmcnt(" #n ")" ::: "memory")
#define SB() __builtin_amdgcn_sched_barrier(0)
#define MEMB() asm volatile("" ::: "memory")
#define PLO() __builtin_amdgcn_s_setprio(0)
#define PHI() __builtin_amdgcn_s_setprio(2)

constexpr float SCALE_B = 1.0f / 16.0f;
constexpr float SCALE_Y = 512.0f / (917504.0f * SCALE_B);

DI unsigned glu2(float g0, float g1, float u0, float u1, float rs, float rl) {
  const float s0 = __builtin_amdgcn_rcpf(1.0f + __builtin_amdgcn_exp2f(g0 * rl));
  const float s1 = __builtin_amdgcn_rcpf(1.0f + __builtin_amdgcn_exp2f(g1 * rl));
  return pkb(s0 * (u0 * rs), s1 * (u1 * rs));
}
DI void ald4(unsigned& d, rsrc_t rs, unsigned voff, unsigned soff) {
  asm volatile("buffer_load_dword %0, %1, %2, %3 offen sc1" : "=&v"(d) : "v"(voff), "s"(rs), "s"(soff) : "memory");
}
DI void touch(unsigned& d, rsrc_t rs, unsigned voff, unsigned soff) {
  asm volatile("buffer_load_dword %0, %1, %2, %3 offen sc1" : "+v"(d) : "v"(voff), "s"(rs), "s"(soff) : "memory");
}
DI float sum2(unsigned u, float c) {
  const b2 one2 = {(__bf16)1.0f, (__bf16)1.0f};
  return __builtin_amdgcn_fdot2_f32_bf16(__builtin_bit_cast(b2, u), one2, c, false);
}
DI void ast16(v4u v, rsrc_t rs, unsigned voff, unsigned soff) {
  asm volatile("buffer_store_dwordx4 %0, %1, %2, %3 offen sc1" ::"v"(v), "v"(voff), "s"(rs), "s"(soff) : "memory");
}
DI float trunc16(float v) { return bhi(__builtin_bit_cast(unsigned, v)); }
DI unsigned hi16(float a, float b) { return __builtin_amdgcn_perm(__builtin_bit_cast(unsigned, b), __builtin_bit_cast(unsigned, a), 0x07060302u); }

template <bool SPARE>
DI void body(const uint16_t* __restrict__ x, const uint16_t* __restrict__ wr, const uint16_t* __restrict__ wi,
             const uint16_t* __restrict__ wg, const uint16_t* __restrict__ wu, const uint16_t* __restrict__ wd,
             unsigned char* __restrict__ ws, uint16_t* __restrict__ out, const unsigned L0) {
  STAMP(0)
  const int b = blockIdx.x, tid = threadIdx.x, lane = tid & 63;
  const int w = __builtin_amdgcn_readfirstlane(tid >> 6);
  const int r = lane & 15, q4 = lane >> 4;
  const int s2 = (b & 7) + 8 * (b >> 4), q = (b >> 3) & 1;
  const int j1b = b & 15, tileb = b >> 4;
  const int yb = b >> 3;
  const int cel = 28 * (b & 3) + yb, mG = 8 * ((b >> 2) & 1) + (cel & 7), nD = cel >> 3;
  const int mE = 2 * (b & 7) + (yb & 1), pE = (yb >> 1) & 1, nE = yb >> 2;
  const int sbk = 4 * (b & 7) + (yb & 3);
  const rsrc_t rx = mkrs(x, S_ * D_ * 2), rfx = mkrs(ws, FIX_BYTES);
  PHI();

  v2u xa[4];
  unsigned ep;
  const int l32 = lane & 31;
  {
    const int lc = l32 < 28 ? l32 : 27;
    const unsigned rowo = (unsigned)(128 * (2 * w + (lane >> 5)) + s2) * 896u;
    const unsigned o1 = rowo + 8 * lc, o2 = rowo + 440 - 8 * lc;
    asm volatile("buffer_load_dwordx2 %0, %5, %7, 0 offen\n\t"
                 "buffer_load_dwordx2 %1, %6, %7, 0 offen\n\t"
                 "buffer_load_dwordx2 %2, %5, %7, 0 offen offset:448\n\t"
                 "buffer_load_dwordx2 %3, %6, %7, 0 offen offset:448\n\t"
                 "s_load_dword %4, %8, 0"
                 : "=&v"(xa[0]), "=&v"(xa[1]), "=&v"(xa[2]), "=&v"(xa[3]), "=&s"(ep)
                 : "v"(o1), "v"(o2), "s"(rx), "s"(ws + EP_OFF)
                 : "memory");
  }
  PLO();
  const v4u sent4 = {SENT, SENT, SENT, SENT};
  unsigned junk;
  {
    const int i5 = (lane * 52) >> 8, l5 = lane - 5 * i5;
    const unsigned tf = lane < 40 ? (unsigned)((((b & 7) + 8 * (8 * yb + i5)) * 450 + (l5 < 4 ? 128 * l5 : 446)) & ~3) : OOB;
    const unsigned td = lane < 49 ? (unsigned)(49 * b + lane) * 128 : OOB;
    ald4(junk, mkrs(w == 0 ? wr : w == 1 ? wi : wd, w < 2 ? S_ * DF_ * 2 : F_ * D_ * 2), w < 2 ? tf : w == 2 ? td : OOB, 0);
    ast16(sent4, rfx, tid * 16, U_OFF + (2 * s2 + q) * 8192);
    if (SPARE) ast16(sent4, rfx, lane < 8 ? (unsigned)(w * 8 + lane) * 16 : OOB, SS_OFF + sbk * 1024);
  }
  const int wmat = w & 1, wrange = w >> 1;
  const rsrc_t rw = mkrs(wmat ? wu : wg, D_ * F_ * 2);
  const unsigned wvoff = (unsigned)(((wrange >> 1) ? 3 - q4 : q4) * 3584 + ((r ^ (2 * (q4 + 4 * wmat))) * 16));
  const unsigned wsoff0 = (unsigned)((224 * (wrange & 1) + ((wrange >> 1) ? 220 : 0)) * 3584 + nD * 256);
  const int wstep = (wrange >> 1) ? -4 * 3584 : 4 * 3584;
  int rot = 64 + ((nD == 3 || nD == 10) ? 7 * (cel & 3) + 3 * ((cel >> 2) & 1) : (7 * (cel & 7)) >> 1) + ROTX * ((b >> 2) & 1);
  asm volatile("" : "+s"(rot));
  const unsigned clr0 = LH_OFF + (unsigned)sbk * LH_STR;
#define KROT(j) ((j) + rot >= 92 ? (j) + rot - 92 : (j) + rot - 64)
#define W_DMA(n0, n1)                                                                                  \
  _Pragma("unroll") for (int n_ = (n0); n_ < (n1); ++n_) {                                             \
    if (SPARE) ast16(sent4, rfx, tid * 16, clr0 + n_ * 8192);                                          \
    else dma(rw, wvoff, wsoff0 + KROT(n_) * wstep, L0 + (wslot(n_) * 8 + w) * 1024);                   \
  }
#define FLAG(i) if (SPARE && tid == 0) __builtin_amdgcn_raw_buffer_store_b32(tag, rfx, sbk * 16, FL_OFF + (i) * 512, 16);
  const int par = w >> 2, w3 = w & 3;
  const int kb = 16 * tileb + r;
  const int fc0 = tileb < 14 ? 16 * tileb : 209;
  SB();
  v4u bc[4], bs[4], mre, mim;
  {
    const int k = 2 * (16 * w + 8 * ((r >> 1) & 1) + 2 * (r >> 2) + (r & 1)) + q;
    const float kf = (float)k * (1.0f / 896.0f);
    const f32x2 st = cis(-2.0f * kf), rk = cis(-64.0f * kf);
    const float c2 = 2.0f * st.x;
    f32x2 z0 = cis((float)(-16 * q4 - 1) * kf), z1 = crot(z0, st);
#pragma unroll
    for (int kc = 0; kc < 4; ++kc) {
      f32x2 cs[8];
      cs[0] = z0; cs[1] = z1;
      if (kc < 3) { z0 = crot(z0, rk); z1 = crot(z1, rk); }
#pragma unroll
      for (int e = 2; e < 8; ++e) cs[e] = cheb(c2, cs[e - 1], cs[e - 2]);
#pragma unroll
      for (int j = 0; j < 4; ++j) { bc[kc][j] = pkh(cs[2 * j].x, cs[2 * j + 1].x); bs[kc][j] = pkh(cs[2 * j].y, cs[2 * j + 1].y); }
    }
    f32x2 m = cis((float)(r * (512 * q4 + s2)) * (1.0f / 2048.0f));
    const f32x2 ms = cis((float)r * (1.0f / 16.0f));
#pragma unroll
    for (int i = 0; i < 4; ++i) {
      mre[i] = pkh(m.x, m.y); mim[i] = pkh(-m.y, m.x);
      if (i < 3) m = crot(m, ms);
    }
#pragma unroll
    for (int kc = 0; kc < 4; ++kc) asm volatile("" : "+v"(bc[kc]), "+v"(bs[kc]));
    asm volatile("" : "+v"(mre), "+v"(mim));
  }
  W_DMA(0, 4)
  PHI();
  STAMP(1)
  asm volatile("s_waitcnt vmcnt(6) lgkmcnt(0)" : "+v"(xa[0]), "+v"(xa[1]), "+v"(xa[2]), "+v"(xa[3]), "+s"(ep)::"memory");
  const unsigned tag = ep * 1664525u + 1013904223u;
  const rsrc_t rws = mkrs(ws + FIX_BYTES + (size_t)(tag & (NROT - 1)) * REG_BYTES, REG_BYTES);
  v4u fraw;
  {
    const unsigned c0 = __builtin_amdgcn_readlane(xa[0].x, 0), c1 = __builtin_amdgcn_readlane(xa[0].x, 32);
    const float cc = 2.0f * blo(lane < 32 ? c0 : c1);
    float s1[4], sb[4], d1[4], d2[4];
    float sum = 0.f, sq = 0.f;
#pragma unroll
    for (int i = 0; i < 4; ++i) {
      const unsigned ua = xa[0][i >> 1], ub = xa[1][1 - (i >> 1)], ue = xa[2][i >> 1], uf = xa[3][1 - (i >> 1)];
      const float a = (i & 1) ? bhi(ua) : blo(ua), e = (i & 1) ? bhi(ue) : blo(ue);
      const float bb = (i & 1) ? blo(ub) : bhi(ub), f = (i & 1) ? blo(uf) : bhi(uf);
      s1[i] = (a + f) - cc; sb[i] = (bb + e) - cc; d1[i] = a - f; d2[i] = e - bb;
      sum += s1[i] + sb[i];
      sq = __builtin_fmaf(s1[i], s1[i], sq); sq = __builtin_fmaf(sb[i], sb[i], sq);
      sq = __builtin_fmaf(d1[i], d1[i], sq); sq = __builtin_fmaf(d2[i], d2[i], sq);
    }
    if (l32 >= 28) { sum = 0.f; sq = 0.f; }
    asm volatile("" : "+v"(sum), "+v"(sq));
    W_DMA(4, 5)
    sum = sum32(sum); sq = sum32(sq);
    asm volatile("" : "+v"(sum), "+v"(sq));
    W_DMA(5, 6)
    const float mu = sum * (1.0f / 448.0f);
    const float var = __builtin_fmaxf(__builtin_fmaf(-mu, mu, sq * (0.5f / 448.0f)), 0.f);
    float rs = __builtin_amdgcn_rsqf(var + 1e-5f);
    if (l32 >= 28) rs = 0.f;
    const float sgn = q ? -1.0f : 1.0f;
    const float off = q ? 0.f : -4.0f * mu * rs;
    float P[4], Q[4];
#pragma unroll
    for (int i = 0; i < 4; ++i) {
      P[i] = __builtin_fmaf(__builtin_fmaf(sgn, sb[i], s1[i]), rs, off);
      Q[i] = __builtin_fmaf(sgn, d2[i], d1[i]) * rs;
    }
    const int c = l32 >> 1, row = 2 * w + (lane >> 5);
    const unsigned o = L0 + L_A + (c * 16 + (row ^ (c & 7))) * 16 + (l32 & 1) * 8;
    *(LAS v2u*)(size_t)o = v2u{pkh(P[0], P[1]), pkh(P[2], P[3])};
    *(LAS v2u*)(size_t)(o + 4096) = v2u{pkh(Q[0], Q[1]), pkh(Q[2], Q[3])};
  }
  W_DMA(6, 7)
  {
    const rsrc_t rf = mkrs((w & 1) ? wi : wr, S_ * DF_ * 2);
    const unsigned fo = (unsigned)(((j1b + 16 * (32 * (w >> 1) + (lane >> 1))) * DF_ + fc0 + 8 * (lane & 1)) * 2);
    asm volatile("buffer_load_dwordx4 %0, %1, %2, 0 offen" : "=&v"(fraw) : "v"(fo), "s"(rf) : "memory");
  }
  VMW(8);
  BAR();
  STAMPF(3)
  {
    f32x4 zre = {0, 0, 0, 0}, zim = {0, 0, 0, 0};
#pragma unroll
    for (int kc = 0; kc < 4; ++kc) {
      const int c = 4 * kc + q4;
      const unsigned o = L0 + L_A + (c * 16 + (r ^ (c & 7))) * 16;
      zre = mfma16h(lds16(o), bc[kc], zre);
      zim = mfma16h(lds16(o + 4096), bs[kc], zim);
    }
    v4u zop;
#pragma unroll
    for (int i = 0; i < 4; ++i) zop[i] = pkh(zre[i], zim[i]);
    const f32x4 z4 = {0, 0, 0, 0};
    const f32x4 tre = mfma16h(zop, mre, z4), tim = mfma16h(zop, mim, z4);
    const unsigned to = (unsigned)(r * 16 * 128 * 128 + q4 * 16);
    const unsigned tso = (unsigned)((2 * w * 128 + s2) * 128 + q * 64);
    st16(v4u{pkh(tre[0], tim[0]), tag, pkh(tre[1], tim[1]), tag}, rws, to, tso);
    st16(v4u{pkh(tre[2], tim[2]), tag, pkh(tre[3], tim[3]), tag}, rws, w == 7 ? OOB : to, tso + 128 * 128);
  }
  STAMPF(4)
  MEMB();
  PLO();
  const int arow = 16 * w3 + 4 * (r >> 2) + 2 * (r & 1) + 64 * ((r >> 1) & 1) + par;
  v4u fre[4], fim[4];
  {
    const float af = (float)arow * (1.0f / 128.0f);
    const f32x2 s1 = cis(af), s16 = cis(16.0f * af);
    f32x2 z[4][4];
    z[0][0] = cis((float)(4 * q4) * af);
    z[0][1] = crot(z[0][0], s1);
    z[0][2] = cheb(2.0f * s1.x, z[0][1], z[0][0]);
    z[0][3] = cheb(2.0f * s1.x, z[0][2], z[0][1]);
#pragma unroll
    for (int e = 0; e < 4; ++e) {
      z[1][e] = crot(z[0][e], s16);
      z[2][e] = cheb(2.0f * s16.x, z[1][e], z[0][e]);
      z[3][e] = cheb(2.0f * s16.x, z[2][e], z[1][e]);
    }
#pragma unroll
    for (int g = 0; g < 4; ++g)
#pragma unroll
      for (int i = 0; i < 4; ++i) {
        const f32x2 v = z[g][2 * (i & 1) + (i >> 1)];
        fre[g][i] = pkh(v.x, v.y); fim[g][i] = pkh(-v.y, v.x);
      }
#pragma unroll
    for (int g = 0; g < 4; ++g) asm volatile("" : "+v"(fre[g]), "+v"(fim[g]));
  }
  v4u wf[28];
  float cs0 = 0.f, cs1 = 0.f;
  const unsigned trb = L0 + (unsigned)((((lane >> 5) * 4 + ((lane >> 4) & 1)) * 1024) + ((lane >> 2) & 3) * 256 +
                                       (((2 * w + ((lane >> 1) & 1)) ^ (2 * (((lane >> 2) & 3) + 4 * ((lane >> 4) & 1)))) * 16) + (lane & 1) * 8);
#define W_READ(n0, n1)                                                                   \
  if (!SPARE) {                                                                          \
    unsigned t0 = trb, t1 = trb + 65536, t2 = trb + 131072;                              \
    asm volatile("" : "+v"(t0), "+v"(t1), "+v"(t2));                                     \
    _Pragma("unroll") for (int n_ = (n0); n_ < (n1); ++n_) {                             \
      const int sl = wslot(n_);                                                          \
      const unsigned a = (sl < 8 ? t0 : sl < 16 ? t1 : t2) + (sl & 7) * 8192;            \
      const v2u lo = ldtr(a), hi = ldtr(a + 2048);                                       \
      wf[n_] = v4u{lo.x, lo.y, hi.x, hi.y};                                              \
    }                                                                                    \
    _Pragma("unroll") for (int n_ = (n0); n_ < (n1); ++n_) asm volatile("" : "+v"(wf[n_])); \
  }
#define W_SUM(n0, n1)                                                                    \
  if (!SPARE) {                                                                          \
    _Pragma("unroll") for (int n_ = (n0); n_ < (n1); ++n_) {                             \
      cs0 = sum2(wf[n_][0], cs0); cs1 = sum2(wf[n_][1], cs1);                            \
      cs0 = sum2(wf[n_][2], cs0); cs1 = sum2(wf[n_][3], cs1);                            \
    }                                                                                    \
    asm volatile("" : "+v"(cs0), "+v"(cs1));                                             \
  }
  float fa_[4], fb_[4];
  asm volatile("s_waitcnt vmcnt(2)" : "+v"(fraw), "+v"(junk)::"memory");
  *(LAS v4u*)(size_t)(L0 + L_F + (w & 1) * 4096 + (w >> 1) * 1024 + lane * 16) = fraw;
  BAR();
  FLAG(0)
  W_READ(0, 7)
  {
    const unsigned fa = L0 + L_F + (unsigned)((16 * w3 + 4 * q4 + par) * 32 + ((kb <= 224 ? kb : 224) - fc0) * 2);
    float sc = SCALE_B * 0.125f;
    if (kb == 0 || kb == 224) sc = 0.5f * SCALE_B * 0.125f;
    if (kb > 224) sc = 0.f;
#pragma unroll
    for (int i = 0; i < 4; ++i) {
      fa_[i] = blo(*(LAS const unsigned short*)(size_t)(fa + (2 * (i & 1) + 64 * (i >> 1)) * 32)) * sc;
      fb_[i] = blo(*(LAS const unsigned short*)(size_t)(fa + (2 * (i & 1) + 64 * (i >> 1)) * 32 + 4096)) * sc;
    }
#pragma unroll
    for (int i = 0; i < 4; ++i) asm volatile("" : "+v"(fa_[i]), "+v"(fb_[i]));
  }
  BAR();
  STAMPF(5)
  if (DLY_T) __builtin_amdgcn_s_sleep(DLY_T);
  const int dt = w < 7 ? w : 6;
  const int srow = 128 * r + s2;
  v4u ce[4], co[4], se[4], so[4];
  {
    const unsigned tso = (unsigned)((j1b * 16 + tileb) * 16384 + w * 1024);
    const unsigned tvo = tileb < 15 ? (unsigned)lane * 16 : OOB;
    v4u g0, g1;
    PHI();
    ald16<0>(g0, rws, tvo, tso); ald16<0>(g1, rws, tvo, tso + 8192);
    PLO();
    SB();
    {
      f32x2 m = 0.25f * cis((float)(4 * q4 * srow) * (1.0f / 2048.0f));
      const f32x2 ms = cis((float)srow * (1.0f / 2048.0f));
#pragma unroll
      for (int i = 0; i < 4; ++i) {
        mre[i] = pkh(m.x, -m.y); mim[i] = pkh(m.y, m.x);
        if (i < 3) m = crot(m, ms);
      }
      const float df = (float)(2 * (16 * dt + r) + 1) * (1.0f / 896.0f);
      const f32x2 st = cis(df), rk = cis(64.0f * df);
      const float c2 = 2.0f * st.x;
      f32x2 z0 = cis((float)(16 * q4) * df), z1 = crot(z0, st);
#pragma unroll
      for (int kc = 0; kc < 4; ++kc) {
        W_DMA(7 + kc, 8 + kc)
        f32x2 cs[16];
        cs[0] = z0; cs[1] = z1;
        if (kc < 3) { z0 = crot(z0, rk); z1 = crot(z1, rk); }
#pragma unroll
        for (int e = 2; e < 16; ++e) cs[e] = cheb(c2, cs[e - 1], cs[e - 2]);
#pragma unroll
        for (int j = 0; j < 4; ++j) {
          ce[kc][j] = pkh(cs[4 * j].x, cs[4 * j + 2].x); co[kc][j] = pkh(cs[4 * j + 1].x, cs[4 * j + 3].x);
          se[kc][j] = pkh(cs[4 * j].y, cs[4 * j + 2].y); so[kc][j] = pkh(cs[4 * j + 1].y, cs[4 * j + 3].y);
        }
        asm volatile("" : "+v"(ce[kc]), "+v"(co[kc]), "+v"(se[kc]), "+v"(so[kc]));
        SB();
      }
      asm volatile("" : "+v"(mre), "+v"(mim));
    }
    STAMPF(6)
    PHI();
    asm volatile("s_waitcnt vmcnt(4)" : "+v"(g0), "+v"(g1)::"memory");
    int spins = tileb < 15 ? 0 : SPIN_MAX;
    while (!__all(g0.y == tag && g0.w == tag && g1.y == tag && g1.w == tag) && ++spins <= SPIN_MAX) {
      const bool k0 = g0.y == tag && g0.w == tag, k1 = g1.y == tag && g1.w == tag;
      v4u n0, n1;
      if (TSLP) __builtin_amdgcn_s_sleep(TSLP);
      ald16<0>(n0, rws, k0 ? OOB : tvo, tso); ald16<0>(n1, rws, k1 ? OOB : tvo, tso + 8192);
      asm volatile("s_waitcnt vmcnt(0)" : "+v"(n0), "+v"(n1)::"memory");
      if (!k0) g0 = n0;
      if (!k1) g1 = n1;
    }
    CNT(20, tileb < 15 ? spins : 0)
    const int m = 8 * w + (lane >> 3);
    const unsigned o = L0 + L_BT + (unsigned)(((m >> 4) * 64 + ((m >> 2) & 3) * 16 + 4 * (lane & 3) + ((lane >> 2) & 1)) * 16 + (((m >> 1) & 1) + 2 * (m & 1)) * 4);
    *(LAS unsigned*)(size_t)o = hadd(g0.x, g1.x);
    *(LAS unsigned*)(size_t)(o + 32) = hadd(g0.z, g1.z);
    *(LAS unsigned*)(size_t)(o + 4096) = hsub(g0.x, g1.x);
    *(LAS unsigned*)(size_t)(o + 4096 + 32) = hsub(g0.z, g1.z);
    W_DMA(11, 13)
    STAMP(7)
    BAR();
    f32x4 vre = {0, 0, 0, 0}, vim = {0, 0, 0, 0};
    {
      v4u tf[4];
#pragma unroll
      for (int g = 0; g < 4; ++g) tf[g] = lds16(L0 + L_BT + par * 4096 + g * 1024 + lane * 16);
      W_DMA(13, 14)
#pragma unroll
      for (int g = 0; g < 4; ++g) { vre = mfma16h(fre[g], tf[g], vre); vim = mfma16h(fim[g], tf[g], vim); }
    }
    W_DMA(14, 15)
    float pr[4], pi[4];
#pragma unroll
    for (int i = 0; i < 4; ++i) { pr[i] = vre[i] * fa_[i] - vim[i] * fb_[i]; pi[i] = -(vre[i] * fb_[i] + vim[i] * fa_[i]); }
    const unsigned po = L0 + L_BP + w3 * 1024 + lane * 16 + par * 8;
    *(LAS v2u*)(size_t)po = v2u{pkh(pr[0] + pr[2], pi[0] + pi[2]), pkh(pr[1] + pr[3], pi[1] + pi[3])};
    *(LAS v2u*)(size_t)(po + 4096) = v2u{pkh(pr[0] - pr[2], pi[0] - pi[2]), pkh(pr[1] - pr[3], pi[1] - pi[3])};
    W_DMA(15, 17)
    BAR();
    f32x4 ure = {0, 0, 0, 0}, uim = {0, 0, 0, 0};
    {
      v4u pf[4];
#pragma unroll
      for (int g = 0; g < 4; ++g) pf[g] = lds16(L0 + L_BP + par * 4096 + g * 1024 + lane * 16);
      W_DMA(17, 18)
#pragma unroll
      for (int g = 0; g < 4; ++g) { ure = mfma16h(pf[g], fre[g], ure); uim = mfma16h(pf[g], fim[g], uim); }
    }
    const unsigned uo = tileb < 15 ? (unsigned)(arow * 16384 + q4 * 16) : OOB, uso = U_OFF + (unsigned)((tileb * 16 + j1b) * 64);
    st16(v4u{pkh(ure[0] * 0.125f, uim[0] * -0.125f), pkh(ure[1] * 0.125f, uim[1] * -0.125f), pkh(ure[2] * 0.125f, uim[2] * -0.125f),
             pkh(ure[3] * 0.125f, uim[3] * -0.125f)}, rfx, uo, uso);
  }
  STAMP(8)
  MEMB();
  PLO();
  W_DMA(18, 23)
  v4u xv0, xv1;
  {
    const int lc = l32 < 28 ? l32 : 27, row = 2 * w + (lane >> 5);
    const unsigned o = L0 + L_X + (unsigned)((lc * 16 + (row ^ (lc & 15))) * 16);
    *(LAS v4u*)(size_t)o = v4u{xa[0].x, xa[0].y, xa[2].x, xa[2].y};
    *(LAS v4u*)(size_t)(o + 7168) = v4u{xa[1].x, xa[1].y, xa[3].x, xa[3].y};
    VMW(6);
    BAR();
    if (b == 0 && tid == 0) __builtin_amdgcn_raw_buffer_store_b32(tag, rfx, 0, EP_OFF, 16);
    W_READ(7, 18)
    const int lr = 4 * dt + q4;
    const unsigned o2 = L0 + L_X + (unsigned)((lr * 16 + (r ^ (lr & 15))) * 16);
    xv0 = lds16(o2); xv1 = lds16(o2 + 7168);
    asm volatile("" : "+v"(xv0), "+v"(xv1));
    BAR();
  }
  W_DMA(23, 28)
  const rsrc_t rd = mkrs(wd, F_ * D_ * 2);
  const unsigned dvoff = (unsigned)((lane >> 3) * 896 + (((lane & 7) ^ (4 * ((lane >> 4) & 1))) * 16));
  const unsigned dsoff = (unsigned)(224 * w * 896 + nE * 128);
  int erot = 64 + (nE + 2 * (yb & 3)) % 7;
  asm volatile("" : "+s"(erot));
#define ER(i) ((i) + erot >= 71 ? (i) + erot - 71 : (i) + erot - 64)
#define WD_DMA1(j)                                                                                     \
  {                                                                                                    \
    if (SPARE) { if ((j) < 7) ast16(sent4, rfx, tid * 16, clr0 + (28 + (j)) * 8192); }                 \
    else dma(rd, dvoff, dsoff + (32 * ER((j) >> 2) + 8 * (((j) >> 1) & 1) + 16 * ((j) & 1)) * 896, L0 + (dslot(j) * 8 + w) * 1024); \
  }
#define WD_DMA(j0, j1) _Pragma("unroll") for (int j_ = (j0); j_ < (j1); ++j_) WD_DMA1(j_)
  STAMP(9)
  if (DLY_U) __builtin_amdgcn_s_sleep(DLY_U);
  {
    v2u ug[4];
    unsigned fl;
    const unsigned uso = U_OFF + (unsigned)((s2 * 16 + 2 * w) * 1024);
    const unsigned uo = (w == 7 && r >= 8) ? OOB : (unsigned)((r >> 3) * 1024 + q4 * 256 + (r & 7) * 8);
#define U_LOAD()                                                                                       \
  ald4(fl, rfx, l32 * 16, FL_OFF);                                                                      \
  ald8<0>(ug[0], rfx, uo, uso); ald8<64>(ug[1], rfx, uo, uso); ald8<128>(ug[2], rfx, uo, uso); ald8<192>(ug[3], rfx, uo, uso);
    PHI();
    U_LOAD()
    PLO();
    WD_DMA(0, 2)
    W_SUM(0, 18)
    VMW(7);
    BAR();
    FLAG(1)
    W_READ(18, 28)
    STAMP(10)
    PHI();
    asm volatile("s_waitcnt vmcnt(2)" : "+v"(ug[0]), "+v"(ug[1]), "+v"(ug[2]), "+v"(ug[3]), "+v"(fl)::"memory");
    int spins = 0;
#define U_BAD (max4(v4u{ug[0].x, ug[0].y, ug[1].x, ug[1].y}, max4(v4u{ug[2].x, ug[2].y, ug[3].x, ug[3].y}, 0)) == SENT || fl != tag)
    while (__any(U_BAD) && ++spins <= SPIN_MAX) {
      U_LOAD()
      asm volatile("s_waitcnt vmcnt(0)" : "+v"(ug[0]), "+v"(ug[1]), "+v"(ug[2]), "+v"(ug[3]), "+v"(fl)::"memory");
    }
    CNT(21, spins)
    STAMP(11)
    const f32x4 z4 = {0, 0, 0, 0};
#pragma unroll
    for (int u = 0; u < 2; ++u) {
      const v4u uf = {ug[0][u], ug[1][u], ug[2][u], ug[3][u]};
      const f32x4 yre = mfma16h(uf, mre, z4), yim = mfma16h(uf, mim, z4);
      const int c = 2 * w + (q4 >> 1);
      const unsigned o = L0 + L_C + u * 8192 + (c * 16 + (r ^ (c & 7))) * 16 + (q4 & 1) * 8;
      *(LAS v2u*)(size_t)o = v2u{pkh(yre[0], yre[1]), pkh(yre[2], yre[3])};
      *(LAS v2u*)(size_t)(o + 4096) = v2u{pkh(yim[0], yim[1]), pkh(yim[2], yim[3])};
    }
    BAR();
    WD_DMA(2, 4)
    f32x4 Ce = z4, Co = z4, Se = z4, So = z4;
#pragma unroll
    for (int kc = 0; kc < 4; ++kc) {
      const int c = 4 * kc + q4;
      const unsigned o = L0 + L_C + (c * 16 + (r ^ (c & 7))) * 16;
      Ce = mfma16h(ce[kc], lds16(o), Ce);
      Se = mfma16h(se[kc], lds16(o + 4096), Se);
      Co = mfma16h(co[kc], lds16(o + 8192), Co);
      So = mfma16h(so[kc], lds16(o + 12288), So);
    }
    v4u hq[2];
#pragma unroll
    for (int g = 0; g < 2; ++g) {
      const int gg = g ^ q;
      const float sg = gg ? 1.0f : -1.0f;
      const v4u xs = gg ? v4u{__builtin_amdgcn_alignbit(xv1.y, xv1.y, 16), __builtin_amdgcn_alignbit(xv1.x, xv1.x, 16),
                              __builtin_amdgcn_alignbit(xv1.w, xv1.w, 16), __builtin_amdgcn_alignbit(xv1.z, xv1.z, 16)} : xv0;
      float lo[4], hi[4];
#pragma unroll
      for (int i = 0; i < 4; ++i) {
        const float a = __builtin_fmaf(sg, Se[i], Ce[i]), bb = __builtin_fmaf(sg, Co[i], So[i]);
        const float x0 = (i & 1) ? bhi(xs[i >> 1]) : blo(xs[i >> 1]), x1 = (i & 1) ? bhi(xs[2 + (i >> 1)]) : blo(xs[2 + (i >> 1)]);
        lo[i] = __builtin_fmaf(a - bb, SCALE_Y, x0); hi[i] = __builtin_fmaf(a + bb, SCALE_Y, x1);
      }
      hq[g] = v4u{pkb(lo[0], lo[1]), pkb(lo[2], lo[3]), pkb(hi[0], hi[1]), pkb(hi[2], hi[3])};
      if (g == 0) {
        const unsigned lo_ = w < 7 ? (unsigned)((q4 * 64 + r) * 16) : OOB;
        const unsigned lso = LH_OFF + (unsigned)((s2 >> 2) * LH_STR + ((((s2 >> 1) & 1) * 28 + 4 * dt) * 64 + 16 * (s2 & 1) + 32 * q) * 16);
        st16(hq[0], rfx, lo_, lso);
        MEMB();
        STAMP(12)
        SB();
      }
    }
    PLO();
    WD_DMA(4, 8)
    float sum = 0.f;
#pragma unroll
    for (int g = 0; g < 2; ++g)
#pragma unroll
      for (int i = 0; i < 4; ++i) sum += blo(hq[g][i]) + bhi(hq[g][i]);
    sum = xor32_add(xor16_add(sum));
    const float lm = sum * (1.0f / 64.0f);
    float m2 = 0.f;
#pragma unroll
    for (int g = 0; g < 2; ++g)
#pragma unroll
      for (int i = 0; i < 4; ++i) {
        const float d0_ = blo(hq[g][i]) - lm, d1_ = bhi(hq[g][i]) - lm;
        m2 = __builtin_fmaf(d0_, d0_, m2); m2 = __builtin_fmaf(d1_, d1_, m2);
      }
    m2 = xor32_add(xor16_add(m2));
    if (q4 == 0) *(LAS f32x2*)(size_t)(L0 + L_ST + (r * 8 + w) * 8) = f32x2{lm, m2};
    BAR();
    if (w == 7 && q == 0) {
      PHI();
      f32x4 sv[4];
#pragma unroll
      for (int j = 0; j < 4; ++j) sv[j] = *(LAS const f32x4*)(size_t)(L0 + L_ST + r * 64 + j * 16);
      const float mean = (((sv[0].x + sv[0].z) + (sv[1].x + sv[1].z)) + ((sv[2].x + sv[2].z) + sv[3].x)) * (1.0f / 7.0f);
      float M2 = 0.f;
#pragma unroll
      for (int j = 0; j < 7; ++j) {
        const float dlt = sv[j >> 1][2 * (j & 1)] - mean;
        M2 += __builtin_fmaf(64.0f * dlt, dlt, sv[j >> 1][2 * (j & 1) + 1]);
      }
      const float rs = __builtin_amdgcn_rsqf(M2 * (1.0f / 448.0f) + 1e-5f);
      const float m0 = -mean, m1 = m0 - trunc16(m0), m2_ = m1 - trunc16(m1);
      st16(v4u{hi16(m0, m0), hi16(m0, m1), hi16(m1, m2_), hi16(rs, rs - trunc16(rs))}, rfx, q4 == 0 ? (unsigned)r * 16 : OOB, SS_OFF + s2 * 256);
      MEMB();
      PLO();
    }
  }
  if (SPARE) {
    VMW(0);
    BAR();
    FLAG(2)
  } else {
    WD_DMA(8, 12)
    v4u wdf[7][4];
    const unsigned tdb = L0 + (unsigned)(w * 1024 + (q4 >> 1) * 8192 + (4 * (q4 & 1) + ((lane >> 2) & 3)) * 128 + ((lane >> 3) & 1) * 64 + (lane & 3) * 16);
    unsigned td0 = tdb;
#define WD_READ(u)                                                                                     \
  {                                                                                                    \
    constexpr int sl_ = dslot(2 * (u));                                                                \
    asm volatile("" : "+v"(td0));                                                                      \
    const unsigned ta_ = td0 + (sl_ & ~7) * 8192, tb_ = ta_ ^ 64;                                      \
    _Pragma("unroll") for (int nt = 0; nt < 4; ++nt) {                                                 \
      const v2u t_ = ldtr(((nt >> 1) ? tb_ : ta_) + (sl_ & 7) * 8192 + (nt & 1) * 8);                  \
      wdf[(u) >> 1][nt][2 * ((u) & 1)] = t_.x; wdf[(u) >> 1][nt][2 * ((u) & 1) + 1] = t_.y;            \
    }                                                                                                  \
  }
    v4u csf;
    W_SUM(18, 28)
    {
      const float c0_ = xor32_add(cs0 + cs1), c1_ = c0_ - trunc16(c0_), c2_ = c1_ - trunc16(c1_);
      csf = v4u{hi16(c0_, c1_), hi16(c2_, c0_), hi16(c1_, c0_), 0u};
      if (lane >= 32) csf = v4u{0u, 0u, 0u, 0u};
      asm volatile("" : "+v"(csf));
    }
    VMW(4);
    WD_READ(0) WD_READ(1) WD_READ(2) WD_READ(3)
    BAR();
    STAMP(13)
    if (DLY_L) __builtin_amdgcn_s_sleep(DLY_L);
    const unsigned lnso = LH_OFF + (unsigned)(2 * mG) * LH_STR;
    const unsigned la = L0 + lane * 16;
#define LN_DMA(i)                                                                                      \
  {                                                                                                    \
    const int pi_ = 8 * (i) + w, tt_ = pi_ / 28, j_ = pi_ - 28 * tt_;                                  \
    dma1(rfx, lane * 16, lnso + ((i) >= 7 ? LH_STR - 57344 : 0) + (28 * tt_ + KROT(j_)) * 1024, L0 + pi_ * 1024); \
  }
#define LN_MAX(i0, i1) \
  _Pragma("unroll") for (int i_ = (i0); i_ < (i1); ++i_) mx = max4(lds16((i_ < 7 ? la : la + 57344) + w * 1024 + (i_ < 7 ? i_ : i_ - 7) * 8192), mx);
#define LN_FIX(i0, i1)                                                                                 \
  if (__any(mx == SENT)) {                                                                             \
    int spins = 0;                                                                                     \
    do {                                                                                               \
      _Pragma("unroll") for (int i_ = (i0); i_ < (i1); ++i_) LN_DMA(i_)                                \
      VMW(0);                                                                                          \
      mx = 0;                                                                                          \
      LN_MAX(i0, i1)                                                                                   \
      lspins += 1 << (8 * ((i0) / 3));                                                                 \
    } while (__any(mx == SENT) && ++spins < SPIN_MAX);                                                 \
  }
    unsigned mx = 0;
    int lspins = 0;
    PHI();
#pragma unroll
    for (int i = 0; i < 4; ++i) LN_DMA(i)
    PLO();
    {
      const unsigned pb = (unsigned)(28672 + (224 * ((yb >> 3) % 3) + 28 * w + lane) * 128);
      ald4(junk, rfx, lane < 28 ? (pb >= 57344 ? pb + (LH_STR - 57344) : pb) : OOB, lnso);
    }
    VMW(1);
    LN_MAX(0, 4)
    LN_FIX(0, 4)
    BAR();
    STAMP(14)
    f32x16 acc, accp;
    v4u af[NAF], ho, stf;
    v4u hb[4][7];
    unsigned fl;
    float rsv = 0.f, rlv = 0.f;
    const unsigned hso = LH_OFF + (unsigned)((2 * mG + (nD >= 7)) * LH_STR + 57344 + (8 * (nD >= 7 ? nD - 7 : nD) + w) * 1024);
    unsigned hvo = (unsigned)(q4 * 512 + pE * 256 + r * 16);
    asm volatile("" : "+v"(hvo));
    const unsigned hlo = LH_OFF + (unsigned)((2 * mE + (w >> 2)) * LH_STR + 57344 + 14 * (w & 3) * 1024);
    const unsigned sso = SS_OFF + (unsigned)(mG * 2048);
#define H_LOAD(tt, i) hb[tt][i] = ld16(rfx, hvo, hlo + (tt) * 57344 + ER(i) * 2048);
#define H_TOUCH(tt) touch(junk, rfx, hvo, hlo + (tt) * 57344 + ER(0) * 2048);
#define LGK3() asm volatile("s_waitcnt lgkmcnt(3)" ::: "memory")
#define VMWS(n) asm volatile("s_waitcnt vmcnt(" #n ")" : "+v"(stf), "+v"(fl), "+v"(junk)::"memory")
#define FL_FIX(i)                                                                                      \
  if (__any(fl != tag)) {                                                                              \
    int spins = 0;                                                                                     \
    do {                                                                                               \
      ald4(fl, rfx, l32 * 16, FL_OFF + (i) * 512);                                                      \
      VMWS(0);                                                                                         \
      lspins += 1 << 24;                                                                               \
    } while (__any(fl != tag) && ++spins < SPIN_MAX);                                                  \
  }
    float ge = 0.f, gp = 0.f;
#define GLU_STEP(k)                                                                                    \
  {                                                                                                    \
    const int e_ = (k) >> 1;                                                                           \
    if (((k) & 1) == 0) {                                                                              \
      ge = __builtin_amdgcn_exp2f(accp[e_] * rlv);                                                     \
      asm volatile("" : "+v"(ge));                                                                     \
    } else {                                                                                           \
      const float p_ = __builtin_amdgcn_rcpf(1.0f + ge) * (accp[8 + e_] * rsv);                        \
      if (e_ & 1) { unsigned t_ = pkb(gp, p_); asm volatile("" : "+v"(t_)); ho[e_ >> 1] = t_; }        \
      else { gp = p_; asm volatile("" : "+v"(gp)); }                                                   \
    }                                                                                                  \
  }
    auto dpass = [&](auto TT) __attribute__((always_inline)) {
      constexpr int tt = decltype(TT)::value;
      unsigned lbt = la + tt * 28672;
      asm volatile("" : "+v"(lbt));
      __builtin_amdgcn_s_setprio(3);
#pragma unroll
      for (int i = 0; i < NAF - 1; ++i) af[i] = lds16(lbt + i * 1024);
#pragma unroll
      for (int i = 0; i < 16; ++i) acc[i] = 0.f;
      mx = 0;
      SB();
#pragma unroll
      for (int ks = 0; ks < 28; ++ks) {
        if (ks + NAF - 1 < 28) af[(ks + NAF - 1) % NAF] = lds16(lbt + (ks + NAF - 1) * 1024);
        acc = mfma32b(wf[ks], af[ks % NAF], acc);
        SB();
        if (ks == (tt ? 9 : 0)) ald16<tt * 512>(stf, rfx, l32 * 16, sso);
        if (tt > 0) {
          if (ks < 16) GLU_STEP(ks)
          if (ks == 16) { st16(ho, rfx, lane * 16, hso + (tt - 1) * 57344); MEMB(); }
        }
        if (ks == 9) __builtin_amdgcn_s_setprio(2);
        if (ks == 19 && tt < 3) __builtin_amdgcn_s_setprio(1);
        if (tt == 0) {
          if (ks == 1) ald4(fl, rfx, l32 * 16, FL_OFF + 512);
          if (ks >= 2 && ks < 22 && (ks & 1) == 0) LN_DMA(3 + (ks >> 1))
          if (ks == 21) WD_DMA1(12)
          if (ks == 22) WD_DMA1(13)
          if (ks == 4) WD_READ(4)
          if (ks == 7) WD_READ(5)
          if (ks == 24) { VMWS(9); LN_MAX(4, 7) }
        }
        if (tt == 1) {
          if (ks < 8 && (ks & 1) == 1) WD_DMA1(14 + (ks >> 1))
          if (ks == 10 || ks == 12) WD_DMA1(18 + ((ks - 10) >> 1))
          if (ks == 17) { VMW(8); WD_READ(6) }
          if (ks == 18) LN_MAX(7, 10)
          if (ks == 20) LN_MAX(10, 14)
          if (ks == 22) { LGK3(); WD_DMA1(20) }
          if (ks == 24) WD_DMA1(21)
          if (ks == 26) VMWS(5);
        }
        if (tt == 2) {
          if (ks < 8 && (ks & 1) == 0) WD_DMA1(22 + (ks >> 1))
          if (ks == 17) WD_READ(7)
          if (ks == 20) { LGK3(); WD_DMA1(26) }
          if (ks == 22) WD_DMA1(27)
          if (ks == 24) H_TOUCH(0)
          if (ks == 26) VMWS(4);
        }
        if (tt == 3) {
          if (ks == 1) H_TOUCH(1)
          if (ks >= 3 && ks <= 11 && (ks & 1) == 1) H_LOAD(0, (ks - 3) >> 1)
          if (ks == 15 || ks == 17) H_LOAD(0, (ks - 5) >> 1)
          if (ks >= 19 && ks <= 25 && (ks & 1) == 1) H_LOAD(1, (ks - 19) >> 1)
          if (ks == 9) ald4(fl, rfx, l32 * 16, FL_OFF + 1024);
          if (ks == 8) WD_READ(8)
          if (ks == 14) WD_READ(9)
          if (ks == 18) WD_READ(10)
          if (ks == 20) WD_READ(11)
          if (ks == 22) WD_READ(12)
          if (ks == 24) { VMWS(7); WD_READ(13) }
        }
        SB();
      }
      if (__any(max4(stf, 0) == SENT)) {
        int spins = 0;
        do {
          ald16<tt * 512>(stf, rfx, l32 * 16, sso);
          VMWS(0);
          lspins += 1 << 16;
        } while (__any(max4(stf, 0) == SENT) && ++spins < SPIN_MAX);
      }
      accp = mfma32b(csf, stf, acc);
      rsv = blo(stf.w) + bhi(stf.w);
      rlv = rsv * -1.44269504f;
      if (tt == 0) { LN_FIX(4, 7) FL_FIX(1) }
      if (tt == 1) LN_FIX(7, 14)
      if (tt == 3) FL_FIX(2)
      if (tt < 3) BAR();
      if (tt == 0) STAMP(15)
      if (tt == 2) STAMP(2)
    };
    dpass(IC<0>{}); dpass(IC<1>{}); dpass(IC<2>{}); dpass(IC<3>{});
    CNT(22, lspins)
    STAMP(16)
    int espins = 0;
    f32x4 ea[2][4];
    unsigned ln2 = la;
    asm volatile("" : "+v"(ln2));
    ln2 = (ln2 - L0) >> 4;
    unsigned hro, sel0, pa[2], ra, rb, ovo, hra;
    int hstep;
    v2u hr0, hr1;
    f32x4 rp[8];
    const unsigned hrs = LH_OFF + (unsigned)((2 * mE + (w >> 2)) * LH_STR + ((w >> 1) & 1) * 28672 + pE * 256);
    const rsrc_t ro = mkrs(out, S_ * D_ * 2);
#define HR_LOAD() { hr0 = ld8(rfx, hro, hrs); hr1 = ld8(rfx, hro + hstep, hrs); }
#define H_FIX(tt)                                                                                      \
  {                                                                                                    \
    int spins = 0;                                                                                     \
    do {                                                                                               \
      MEMB();                                                                                          \
      _Pragma("unroll") for (int i = 0; i < 7; ++i) H_LOAD(tt, i)                                      \
      mx = 0;                                                                                          \
      _Pragma("unroll") for (int i = 0; i < 7; ++i) mx = max4(hb[tt][i], mx);                          \
      espins += 1 << (8 * (tt));                                                                       \
    } while (__any(mx == SENT) && ++spins < SPIN_MAX);                                                 \
  }
#define E_ZERO(tt) _Pragma("unroll") for (int nt = 0; nt < 4; ++nt) ea[(tt) & 1][nt] = f32x4{0, 0, 0, 0};
#define E_MM(tt, k) ea[(tt) & 1][(k) & 3] = mfma16b(wdf[(k) >> 2][(k) & 3], hb[tt][(k) >> 2], ea[(tt) & 1][(k) & 3]);
#define E_STEP(tt, k) E_MM(tt, k)
#define E_END(tt)                                                                                      \
  if (__any(__builtin_isnan(ea[(tt) & 1][0][0]))) {                                                    \
    H_FIX(tt) E_ZERO(tt)                                                                               \
    _Pragma("unroll") for (int k = 0; k < 28; ++k) E_MM(tt, k)                                         \
  }
#define E_PUT(tt, nt)                                                                                  \
  {                                                                                                    \
    const int sl_ = pslot(tt, nt);                                                                     \
    *(LAS f32x4*)(size_t)((pa[(nt) & 1] + (sl_ & ~7) * 8192) + (sl_ & 7) * 8192) = ea[(tt) & 1][nt];         \
  }
#define R_READ(base, ww) rp[ww] = *(LAS const f32x4*)(size_t)((base) + (ww) * 1024);
#define R_FIN(h_, so)                                                                                  \
  {                                                                                                    \
    const f32x4 s_ = ((rp[0] + rp[1]) + (rp[2] + rp[3])) + ((rp[4] + rp[5]) + (rp[6] + rp[7]));        \
    const unsigned f0 = pkb(s_[0], s_[1]), f1 = pkb(s_[2], s_[3]);                                     \
    __builtin_amdgcn_raw_buffer_store_b64(v2u{pkb(blo(h_.x) + blo(f0), bhi(h_.x) + bhi(f0)), pkb(blo(h_.y) + blo(f1), bhi(h_.y) + bhi(f1))}, \
                                          ro, ovo, so, 16);                                            \
  }
    E_ZERO(0)
    SB();
#pragma unroll
    for (int k = 0; k < 28; ++k) {
      if (k < 16) GLU_STEP(k)
      if (k == 16) { st16(ho, rfx, lane * 16, hso + 3 * 57344); MEMB(); __builtin_amdgcn_s_setprio(1); }
      if (k < 6 && (k & 1) == 0) H_LOAD(1, 4 + (k >> 1))
      E_MM(0, k)
      if (k == 17) H_LOAD(2, 0)
      if (k == 24) {
#pragma unroll
        for (int nt = 0; nt < 2; ++nt) pa[nt] = L0 + w * 1024 + (ln2 >> 4) * 256 + ((ln2 + 2 * (ln2 >> 4) + 8 * nt) & 15) * 16;
        asm volatile("" : "+v"(pa[0]), "+v"(pa[1]));
      }
      if (k == 26) {
        const int c0 = 64 * nE + 32 * (w & 1) + 8 * (int)(ln2 >> 4);
        const int seg = ((c0 >> 4) * 37) >> 8, hi = seg >> 1, mir = seg & 1, cc = c0 - 224 * hi;
        const int ksa = mir ? (220 - cc) >> 2 : cc >> 2;
        hro = (unsigned)((ksa * 64 + 32 * mir + (ln2 & 15)) * 16 + 8 * hi);
        hstep = mir ? -1024 : 1024;
        sel0 = mir ? 0x05040706u : 0x03020100u;
        asm volatile("" : "+v"(hro), "+v"(hstep), "+v"(sel0));
      }
      SB();
    }
    E_END(0)
    STAMP(17)
    PLO();
    E_ZERO(1)
    SB();
#pragma unroll
    for (int k = 0; k < 28; ++k) {
      E_STEP(1, k)
      if (k < 24 && k % 6 == 2) E_PUT(0, (k / 6) & 3)
      if (k == 0) HR_LOAD()
      if (k >= 4 && k < 4 + 4 * H2N && (k & 3) == 0) H_LOAD(2, k >> 2)
      if (k == 23) {
        const int row = 8 * ((w >> 1) & 1) + (int)(ln2 >> 3), g = ln2 & 7, tl = w >> 2;
        const unsigned o = L0 + (unsigned)((g & 1) * 8192 + (g >> 1) * 256 + ((row + 2 * (g >> 1) + 8 * (g & 1)) & 15) * 16);
        ra = o + (unsigned)((tl ? pslot(1, 0) : pslot(0, 0)) + 2 * (w & 1)) * 8192;
        rb = o + (unsigned)(tl ? ((w & 1) ? pslot(3, 2) : pslot(3, 0)) : pslot(2, 0) + 2 * (w & 1)) * 8192;
        hra = L0 + 32768 + (unsigned)((2 * tl + (w & 1)) * 1024 + row * 64 + g * 8);
        ovo = (unsigned)(((128 * row + 8 * mE + 2 * tl + pE) * D_ + 64 * nE + 32 * (w & 1) + 4 * g) * 2);
        asm volatile("" : "+v"(ra), "+v"(rb), "+v"(hra), "+v"(ovo));
      }
      SB();
    }
    E_END(1)
    STAMPE(4)
    E_ZERO(2)
    SB();
#pragma unroll
    for (int k = 0; k < 28; ++k) {
      if (k < 2 * (6 - H2N) && (k & 1) == 0) H_LOAD(2, H2N + 1 + (k >> 1))
      if (k >= H3_AT && k < H3_AT + 7 * H3_ST && (k - H3_AT) % H3_ST == 0) H_LOAD(3, (k - H3_AT) / H3_ST)
      if (k < 24 && k % 6 == 2) E_PUT(1, (k / 6) & 3)
      if (k == 24) {
        if (__any(max(max(hr0.x, hr0.y), max(hr1.x, hr1.y)) == SENT)) {
          int spins = 0;
          do {
            MEMB();
            HR_LOAD()
          } while (__any(max(max(hr0.x, hr0.y), max(hr1.x, hr1.y)) == SENT) && ++spins < SPIN_MAX);
        }
        *(LAS v4u*)(size_t)(L0 + 32768 + w * 1024 + ((ln2 & 15) * 4 + (ln2 >> 4)) * 16) =
            v4u{__builtin_amdgcn_perm(hr0.y, hr0.x, sel0), __builtin_amdgcn_perm(hr0.y, hr0.x, sel0 ^ 0x04040404u),
                __builtin_amdgcn_perm(hr1.y, hr1.x, sel0), __builtin_amdgcn_perm(hr1.y, hr1.x, sel0 ^ 0x04040404u)};
      }
      E_STEP(2, k)
      SB();
    }
    E_END(2)
    STAMPE(5)
    BAR();
    STAMPE(6)
    E_ZERO(3)
    SB();
#pragma unroll
    for (int k = 0; k < 28; ++k) {
      if (k == 0) { hr0 = *(LAS const v2u*)(size_t)hra; hr1 = *(LAS const v2u*)(size_t)(hra + 4096); }
      if (k < 16 && (k & 1) == 0) R_READ(ra, k >> 1)
      if (k == 19) R_FIN(hr0, 0)
      if (k < 24 && k % 6 == 3) E_PUT(2, (k / 6) & 3)
      E_STEP(3, k)
      SB();
    }
    E_END(3)
    E_PUT(3, 0) E_PUT(3, 1) E_PUT(3, 2) E_PUT(3, 3)
    CNT(23, espins)
    STAMP(18)
    BAR();
#pragma unroll
    for (int ww = 0; ww < 8; ++ww) R_READ(rb, ww)
    R_FIN(hr1, 3584)
  }
}

__global__ __launch_bounds__(512, 2) void fused(const uint16_t* __restrict__ x, const uint16_t* __restrict__ wr,
                                                const uint16_t* __restrict__ wi, const uint16_t* __restrict__ wg,
                                                const uint16_t* __restrict__ wu, const uint16_t* __restrict__ wd,
                                                unsigned char* __restrict__ ws, uint16_t* __restrict__ out) {
  __shared__ __attribute__((aligned(16))) unsigned char smem[163840];
  const unsigned L0 = (unsigned)(size_t)smem;
  if (blockIdx.x < 224) body<false>(x, wr, wi, wg, wu, wd, ws, out, L0);
  else body<true>(x, wr, wi, wg, wu, wd, ws, out, L0);
}

extern "C" void kernel_launch(void* const* d_in, const int* in_sizes, int n_in, void* d_out, int out_size, void* d_ws,
                              size_t ws_size, hipStream_t stream) {
  fused<<<256, 512, 0, stream>>>((const uint16_t*)d_in[0], (const uint16_t*)d_in[1], (const uint16_t*)d_in[2],
                                 (const uint16_t*)d_in[4], (const uint16_t*)d_in[5], (const uint16_t*)d_in[6],
                                 (unsigned char*)d_ws, (uint16_t*)d_out);
}
```

```cpp
#include <hip/hip_runtime.h>
#include <hip/hip_bf16.h>
#include <stdint.h>

typedef unsigned v4u __attribute__((ext_vector_type(4)));
typedef unsigned v2u __attribute__((ext_vector_type(2)));
typedef float f32x2 __attribute__((ext_vector_type(2)));
typedef float f32x4 __attribute__((ext_vector_type(4)));
typedef float f32x16 __attribute__((ext_vector_type(16)));
typedef _Float16 h8 __attribute__((ext_vector_type(8)));
typedef _Float16 h2 __attribute__((ext_vector_type(2)));
typedef __bf16 b8 __attribute__((ext_vector_type(8)));
typedef __bf16 b2 __attribute__((ext_vector_type(2)));
typedef short s4v __attribute__((ext_vector_type(4)));
typedef __amdgpu_buffer_rsrc_t rsrc_t;
#define DI __device__ __forceinline__
template <int N> struct IC { static constexpr int value = N; };
#define LAS __attribute__((address_space(3)))

constexpr int S_ = 2048, D_ = 448, F_ = 1792, DF_ = 225;
constexpr unsigned MB = 1u << 20;
constexpr unsigned U_OFF = 0;
constexpr unsigned EP_OFF = 2 * MB;
constexpr unsigned FL_OFF = 2 * MB + 4096;
constexpr unsigned SS_OFF = 2 * MB + 65536;
constexpr unsigned LH_OFF = 3 * MB;
constexpr unsigned LH_STR = 5 * 57344;
constexpr unsigned FIX_BYTES = 16 * MB;
constexpr unsigned REG_BYTES = 4 * MB;
#define NROT 8
constexpr unsigned OOB = 0x80000000u;
constexpr unsigned SENT = 0xffffffffu;
constexpr int SPIN_MAX = 20000;
constexpr unsigned L_SP = 17 * 8192;
constexpr unsigned L_A = L_SP, L_BT = L_SP + 8192, L_BP = L_SP + 16384, L_F = L_BP, L_X = L_SP, L_C = L_SP, L_ST = L_SP + 23552;
constexpr int wslot(int n) { return n < 17 ? n : n - 17; }
constexpr int dslot(int j) {
  return j < 2 ? 11 + j : j < 8 ? j - 2 : j < 14 ? j + 6 : j < 18 ? j : j < 20 ? j - 18 : j < 22 ? j - 2 : j < 26 ? j - 20 : j - 12;
}
constexpr int pslot(int tt, int nt) { return tt == 0 ? 6 + nt : tt == 1 ? nt : tt == 2 ? 14 + nt : nt < 2 ? 11 + nt : 16 + nt; }

#define ROTX 2
#define CPF 2
#define TMASK 1
#define TSLP 0
#define EV 0
#define H2N 4
#define H3_AT 7
#define H3_ST 3
#define SBM 1
#define NAF 3
#define DLY_T 0
#define DLY_U 0
#define DLY_L 0

#define STAMP_TID 0
#define STAMP(i) {}
#define CNT(i, v)

#define STAMPF(i) STAMP(i)
#define STAMPE(i) {}
DI unsigned pkh(float a, float b) { h2 v = {(_Float16)a, (_Float16)b}; return __builtin_bit_cast(unsigned, v); }
DI unsigned pkb(float a, float b) { b2 v = {(__bf16)a, (__bf16)b}; return __builtin_bit_cast(unsigned, v); }
DI float blo(unsigned u) { return __builtin_bit_cast(float, u << 16); }
DI float bhi(unsigned u) { return __builtin_bit_cast(float, u & 0xffff0000u); }
DI float cosr(float x) { return __builtin_amdgcn_cosf(x); }
DI float sinr(float x) { return __builtin_amdgcn_sinf(x); }
DI f32x2 cis(float x) { return f32x2{cosr(x), sinr(x)}; }
DI f32x2 crot(f32x2 a, f32x2 b) {
  return __builtin_elementwise_fma(f32x2{a.y, a.y}, f32x2{-b.y, b.x}, f32x2{a.x, a.x} * b);
}
DI f32x2 cheb(float c2, f32x2 a1, f32x2 a0) { return __builtin_elementwise_fma(f32x2{c2, c2}, a1, -a0); }
DI unsigned hadd(unsigned a, unsigned b) { return __builtin_bit_cast(unsigned, __builtin_bit_cast(h2, a) + __builtin_bit_cast(h2, b)); }
DI unsigned hsub(unsigned a, unsigned b) { return __builtin_bit_cast(unsigned, __builtin_bit_cast(h2, a) - __builtin_bit_cast(h2, b)); }
DI f32x4 mfma16h(v4u a, v4u b, f32x4 c) {
  return __builtin_amdgcn_mfma_f32_16x16x32_f16(__builtin_bit_cast(h8, a), __builtin_bit_cast(h8, b), c, 0, 0, 0);
}
DI f32x4 mfma16b(v4u a, v4u b, f32x4 c) {
  return __builtin_amdgcn_mfma_f32_16x16x32_bf16(__builtin_bit_cast(b8, a), __builtin_bit_cast(b8, b), c, 0, 0, 0);
}
DI f32x16 mfma32b(v4u a, v4u b, f32x16 c) {
  return __builtin_amdgcn_mfma_f32_32x32x16_bf16(__builtin_bit_cast(b8, a), __builtin_bit_cast(b8, b), c, 0, 0, 0);
}
template <int CTRL> DI float dpp_add(float v) {
  return v + __builtin_bit_cast(float, __builtin_amdgcn_update_dpp(0, __builtin_bit_cast(int, v), CTRL, 0xf, 0xf, true));
}
DI float xor16_add(float v) {
  float a = v, b = v;
  asm("s_nop 1\n\tv_permlane16_swap_b32 %0, %1" : "+v"(a), "+v"(b));
  return a + b;
}
DI float xor32_add(float v) {
  float a = v, b = v;
  asm("s_nop 1\n\tv_permlane32_swap_b32 %0, %1" : "+v"(a), "+v"(b));
  return a + b;
}
DI float sum32(float v) {
  v = dpp_add<0xB1>(v); v = dpp_add<0x4E>(v); v = dpp_add<0x141>(v); v = dpp_add<0x140>(v);
  return xor16_add(v);
}
DI rsrc_t mkrs(const void* p, unsigned bytes) { return __builtin_amdgcn_make_buffer_rsrc((void*)p, (short)0, (int)bytes, 0x00020000); }
DI void dma(rsrc_t rs, unsigned voff, unsigned soff, unsigned lds) {
  asm volatile("s_mov_b32 m0, %0\n\ts_nop 0\n\tbuffer_load_dwordx4 %1, %2, %3 offen lds" ::"s"(lds), "v"(voff), "s"(rs), "s"(soff) : "memory");
}
DI void dma1(rsrc_t rs, unsigned voff, unsigned soff, unsigned lds) {
  asm volatile("s_mov_b32 m0, %0\n\ts_nop 0\n\tbuffer_load_dwordx4 %1, %2, %3 offen sc1 lds" ::"s"(lds), "v"(voff), "s"(rs), "s"(soff) : "memory");
}
template <int OFF> DI void ald8(v2u& d, rsrc_t rs, unsigned voff, unsigned soff) {
  asm volatile("buffer_load_dwordx2 %0, %1, %2, %3 offen offset:%4 sc1" : "=&v"(d) : "v"(voff), "s"(rs), "s"(soff), "n"(OFF) : "memory");
}
template <int OFF> DI void ald16(v4u& d, rsrc_t rs, unsigned voff, unsigned soff) {
  asm volatile("buffer_load_dwordx4 %0, %1, %2, %3 offen offset:%4 sc1" : "=&v"(d) : "v"(voff), "s"(rs), "s"(soff), "n"(OFF) : "memory");
}
DI v4u ld16(rsrc_t rs, unsigned voff, unsigned soff) { return __builtin_amdgcn_raw_buffer_load_b128(rs, voff, soff, 16); }
DI v2u ld8(rsrc_t rs, unsigned voff, unsigned soff) { return __builtin_amdgcn_raw_buffer_load_b64(rs, voff, soff, 16); }
DI void st16(v4u v, rsrc_t rs, unsigned voff, unsigned soff) { __builtin_amdgcn_raw_buffer_store_b128(v, rs, voff, soff, 16); }
DI v4u lds16(unsigned a) { return *(LAS const v4u*)(size_t)a; }
DI v2u ldtr(unsigned a) { return __builtin_bit_cast(v2u, __builtin_amdgcn_ds_read_tr16_b64_v4i16((LAS s4v*)(size_t)a)); }
DI unsigned max4(v4u v, unsigned m) { return max(max(max(max(m, v[0]), v[1]), v[2]), v[3]); }
#define BAR() asm volatile("s_waitcnt lgkmcnt(0)\n\ts_barrier" ::: "memory")
#define VMW(n) asm volatile("s_waitcnt vmcnt(" #n ")" ::: "memory")
#define SB() __builtin_amdgcn_sched_barrier(0)
#define MEMB() asm volatile("" ::: "memory")
#define PLO() __builtin_amdgcn_s_setprio(0)
#define PHI() __builtin_amdgcn_s_setprio(2)

constexpr float SCALE_B = 1.0f / 16.0f;
constexpr float SCALE_Y = 512.0f / (917504.0f * SCALE_B);

DI unsigned glu2(float g0, float g1, float u0, float u1, float rs, float rl) {
  const float s0 = __builtin_amdgcn_rcpf(1.0f + __builtin_amdgcn_exp2f(g0 * rl));
  const float s1 = __builtin_amdgcn_rcpf(1.0f + __builtin_amdgcn_exp2f(g1 * rl));
  return pkb(s0 * (u0 * rs), s1 * (u1 * rs));
}
DI void ald4(unsigned& d, rsrc_t rs, unsigned voff, unsigned soff) {
  asm volatile("buffer_load_dword %0, %1, %2, %3 offen sc1" : "=&v"(d) : "v"(voff), "s"(rs), "s"(soff) : "memory");
}
DI void touch(unsigned& d, rsrc_t rs, unsigned voff, unsigned soff) {
  asm volatile("buffer_load_dword %0, %1, %2, %3 offen sc1" : "+v"(d) : "v"(voff), "s"(rs), "s"(soff) : "memory");
}
DI float sum2(unsigned u, float c) {
  const b2 one2 = {(__bf16)1.0f, (__bf16)1.0f};
  return __builtin_amdgcn_fdot2_f32_bf16(__builtin_bit_cast(b2, u), one2, c, false);
}
DI void ast16(v4u v, rsrc_t rs, unsigned voff, unsigned soff) {
  asm volatile("buffer_store_dwordx4 %0, %1, %2, %3 offen sc1" ::"v"(v), "v"(voff), "s"(rs), "s"(soff) : "memory");
}
DI float trunc16(float v) { return bhi(__builtin_bit_cast(unsigned, v)); }
DI unsigned hi16(float a, float b) { return __builtin_amdgcn_perm(__builtin_bit_cast(unsigned, b), __builtin_bit_cast(unsigned, a), 0x07060302u); }

template <bool SPARE>
DI void body(const uint16_t* __restrict__ x, const uint16_t* __restrict__ wr, const uint16_t* __restrict__ wi,
             const uint16_t* __restrict__ wg, const uint16_t* __restrict__ wu, const uint16_t* __restrict__ wd,
             unsigned char* __restrict__ ws, uint16_t* __restrict__ out, const unsigned L0) {
  STAMP(0)
  const int b = blockIdx.x, tid = threadIdx.x, lane = tid & 63;
  const int w = __builtin_amdgcn_readfirstlane(tid >> 6);
  const int r = lane & 15, q4 = lane >> 4;
  const int s2 = (b & 7) + 8 * (b >> 4), q = (b >> 3) & 1;
  const int j1b = b & 15, tileb = b >> 4;
  const int yb = b >> 3;
  const int cel = 28 * (b & 3) + yb, mG = 8 * ((b >> 2) & 1) + (cel & 7), nD = cel >> 3;
  const int mE = 2 * (b & 7) + (yb & 1), pE = (yb >> 1) & 1, nE = yb >> 2;
  const int sbk = 4 * (b & 7) + (yb & 3);
  const rsrc_t rx = mkrs(x, S_ * D_ * 2), rfx = mkrs(ws, FIX_BYTES);
  PHI();

  v2u xa[4];
  unsigned ep;
  const int l32 = lane & 31;
  {
    const int lc = l32 < 28 ? l32 : 27;
    const unsigned rowo = (unsigned)(128 * (2 * w + (lane >> 5)) + s2) * 896u;
    const unsigned o1 = rowo + 8 * lc, o2 = rowo + 440 - 8 * lc;
    asm volatile("buffer_load_dwordx2 %0, %5, %7, 0 offen\n\t"
                 "buffer_load_dwordx2 %1, %6, %7, 0 offen\n\t"
                 "buffer_load_dwordx2 %2, %5, %7, 0 offen offset:448\n\t"
                 "buffer_load_dwordx2 %3, %6, %7, 0 offen offset:448\n\t"
                 "s_load_dword %4, %8, 0"
                 : "=&v"(xa[0]), "=&v"(xa[1]), "=&v"(xa[2]), "=&v"(xa[3]), "=&s"(ep)
                 : "v"(o1), "v"(o2), "s"(rx), "s"(ws + EP_OFF)
                 : "memory");
  }
  PLO();
  const v4u sent4 = {SENT, SENT, SENT, SENT};
  unsigned junk;
  {
    const int i5 = (lane * 52) >> 8, l5 = lane - 5 * i5;
    const unsigned tf = lane < 40 ? (unsigned)((((b & 7) + 8 * (8 * yb + i5)) * 450 + (l5 < 4 ? 128 * l5 : 446)) & ~3) : OOB;
    const unsigned td = lane < 49 ? (unsigned)(49 * b + lane) * 128 : OOB;
    const uint16_t* pc;
    int clen;
    asm volatile("s_getpc_b64 %0\n.Lcs_%2:\n\ts_mov_b32 %1, .Lce_%2-.Lcs_%2" : "=s"(pc), "=s"(clen) : "n"(SPARE ? 1 : 0));
    clen = (SPARE || yb < CPF) ? max(clen, 0) : 0;
    ald4(junk, mkrs(w == 0 ? wr : w == 1 ? wi : w == 2 ? wd : pc, w < 2 ? S_ * DF_ * 2 : w == 2 ? F_ * D_ * 2 : clen),
         w < 2 ? tf : w == 2 ? td : (unsigned)(tid - 192) * 128, 0);
    ast16(sent4, rfx, tid * 16, U_OFF + (2 * s2 + q) * 8192);
    if (SPARE) ast16(sent4, rfx, lane < 8 ? (unsigned)(w * 8 + lane) * 16 : OOB, SS_OFF + sbk * 1024);
  }
  const int wmat = w & 1, wrange = w >> 1;
  const rsrc_t rw = mkrs(wmat ? wu : wg, D_ * F_ * 2);
  const unsigned wvoff = (unsigned)(((wrange >> 1) ? 3 - q4 : q4) * 3584 + ((r ^ (2 * (q4 + 4 * wmat))) * 16));
  const unsigned wsoff0 = (unsigned)((224 * (wrange & 1) + ((wrange >> 1) ? 220 : 0)) * 3584 + nD * 256);
  const int wstep = (wrange >> 1) ? -4 * 3584 : 4 * 3584;
  int rot = 64 + ((nD == 3 || nD == 10) ? 7 * (cel & 3) + 3 * ((cel >> 2) & 1) : (7 * (cel & 7)) >> 1) + ROTX * ((b >> 2) & 1);
  asm volatile("" : "+s"(rot));
  const unsigned clr0 = LH_OFF + (unsigned)sbk * LH_STR;
#define KROT(j) ((j) + rot >= 92 ? (j) + rot - 92 : (j) + rot - 64)
#define W_DMA(n0, n1)                                                                                  \
  _Pragma("unroll") for (int n_ = (n0); n_ < (n1); ++n_) {                                             \
    if (SPARE) ast16(sent4, rfx, tid * 16, clr0 + n_ * 8192);                                          \
    else dma(rw, wvoff, wsoff0 + KROT(n_) * wstep, L0 + (wslot(n_) * 8 + w) * 1024);                   \
  }
#define FLAG(i) if (SPARE && tid == 0) __builtin_amdgcn_raw_buffer_store_b32(tag, rfx, sbk * 16, FL_OFF + (i) * 512, 16);
  const int par = w >> 2, w3 = w & 3;
  const int kb = 16 * tileb + r;
  const int fc0 = tileb < 14 ? 16 * tileb : 209;
  SB();
  v4u bc[4], bs[4], mre, mim;
  {
    const int k = 2 * (16 * w + 8 * ((r >> 1) & 1) + 2 * (r >> 2) + (r & 1)) + q;
    const float kf = (float)k * (1.0f / 896.0f);
    const f32x2 st = cis(-2.0f * kf), rk = cis(-64.0f * kf);
    const float c2 = 2.0f * st.x;
    f32x2 z0 = cis((float)(-16 * q4 - 1) * kf), z1 = crot(z0, st);
#pragma unroll
    for (int kc = 0; kc < 4; ++kc) {
      f32x2 cs[8];
      cs[0] = z0; cs[1] = z1;
      if (kc < 3) { z0 = crot(z0, rk); z1 = crot(z1, rk); }
#pragma unroll
      for (int e = 2; e < 8; ++e) cs[e] = cheb(c2, cs[e - 1], cs[e - 2]);
#pragma unroll
      for (int j = 0; j < 4; ++j) { bc[kc][j] = pkh(cs[2 * j].x, cs[2 * j + 1].x); bs[kc][j] = pkh(cs[2 * j].y, cs[2 * j + 1].y); }
    }
    f32x2 m = cis((float)(r * (512 * q4 + s2)) * (1.0f / 2048.0f));
    const f32x2 ms = cis((float)r * (1.0f / 16.0f));
#pragma unroll
    for (int i = 0; i < 4; ++i) {
      mre[i] = pkh(m.x, m.y); mim[i] = pkh(-m.y, m.x);
      if (i < 3) m = crot(m, ms);
    }
#pragma unroll
    for (int kc = 0; kc < 4; ++kc) asm volatile("" : "+v"(bc[kc]), "+v"(bs[kc]));
    asm volatile("" : "+v"(mre), "+v"(mim));
  }
  W_DMA(0, 4)
  PHI();
  STAMP(1)
  asm volatile("s_waitcnt vmcnt(6) lgkmcnt(0)" : "+v"(xa[0]), "+v"(xa[1]), "+v"(xa[2]), "+v"(xa[3]), "+s"(ep)::"memory");
  const unsigned tag = ep * 1664525u + 1013904223u;
  const rsrc_t rws = mkrs(ws + FIX_BYTES + (size_t)(tag & (NROT - 1)) * REG_BYTES, REG_BYTES);
  v4u fraw;
  {
    const unsigned c0 = __builtin_amdgcn_readlane(xa[0].x, 0), c1 = __builtin_amdgcn_readlane(xa[0].x, 32);
    const float cc = 2.0f * blo(lane < 32 ? c0 : c1);
    float s1[4], sb[4], d1[4], d2[4];
    float sum = 0.f, sq = 0.f;
#pragma unroll
    for (int i = 0; i < 4; ++i) {
      const unsigned ua = xa[0][i >> 1], ub = xa[1][1 - (i >> 1)], ue = xa[2][i >> 1], uf = xa[3][1 - (i >> 1)];
      const float a = (i & 1) ? bhi(ua) : blo(ua), e = (i & 1) ? bhi(ue) : blo(ue);
      const float bb = (i & 1) ? blo(ub) : bhi(ub), f = (i & 1) ? blo(uf) : bhi(uf);
      s1[i] = (a + f) - cc; sb[i] = (bb + e) - cc; d1[i] = a - f; d2[i] = e - bb;
      sum += s1[i] + sb[i];
      sq = __builtin_fmaf(s1[i], s1[i], sq); sq = __builtin_fmaf(sb[i], sb[i], sq);
      sq = __builtin_fmaf(d1[i], d1[i], sq); sq = __builtin_fmaf(d2[i], d2[i], sq);
    }
    if (l32 >= 28) { sum = 0.f; sq = 0.f; }
    asm volatile("" : "+v"(sum), "+v"(sq));
    W_DMA(4, 5)
    sum = sum32(sum); sq = sum32(sq);
    asm volatile("" : "+v"(sum), "+v"(sq));
    W_DMA(5, 6)
    const float mu = sum * (1.0f / 448.0f);
    const float var = __builtin_fmaxf(__builtin_fmaf(-mu, mu, sq * (0.5f / 448.0f)), 0.f);
    float rs = __builtin_amdgcn_rsqf(var + 1e-5f);
    if (l32 >= 28) rs = 0.f;
    const float sgn = q ? -1.0f : 1.0f;
    const float off = q ? 0.f : -4.0f * mu * rs;
    float P[4], Q[4];
#pragma unroll
    for (int i = 0; i < 4; ++i) {
      P[i] = __builtin_fmaf(__builtin_fmaf(sgn, sb[i], s1[i]), rs, off);
      Q[i] = __builtin_fmaf(sgn, d2[i], d1[i]) * rs;
    }
    const int c = l32 >> 1, row = 2 * w + (lane >> 5);
    const unsigned o = L0 + L_A + (c * 16 + (row ^ (c & 7))) * 16 + (l32 & 1) * 8;
    *(LAS v2u*)(size_t)o = v2u{pkh(P[0], P[1]), pkh(P[2], P[3])};
    *(LAS v2u*)(size_t)(o + 4096) = v2u{pkh(Q[0], Q[1]), pkh(Q[2], Q[3])};
  }
  W_DMA(6, 7)
  {
    const rsrc_t rf = mkrs((w & 1) ? wi : wr, S_ * DF_ * 2);
    const unsigned fo = (unsigned)(((j1b + 16 * (32 * (w >> 1) + (lane >> 1))) * DF_ + fc0 + 8 * (lane & 1)) * 2);
    asm volatile("buffer_load_dwordx4 %0, %1, %2, 0 offen" : "=&v"(fraw) : "v"(fo), "s"(rf) : "memory");
  }
  VMW(8);
  BAR();
  STAMPF(3)
  {
    f32x4 zre = {0, 0, 0, 0}, zim = {0, 0, 0, 0};
#pragma unroll
    for (int kc = 0; kc < 4; ++kc) {
      const int c = 4 * kc + q4;
      const unsigned o = L0 + L_A + (c * 16 + (r ^ (c & 7))) * 16;
      zre = mfma16h(lds16(o), bc[kc], zre);
      zim = mfma16h(lds16(o + 4096), bs[kc], zim);
    }
    v4u zop;
#pragma unroll
    for (int i = 0; i < 4; ++i) zop[i] = pkh(zre[i], zim[i]);
    const f32x4 z4 = {0, 0, 0, 0};
    const f32x4 tre = mfma16h(zop, mre, z4), tim = mfma16h(zop, mim, z4);
    const unsigned to = (unsigned)(r * 16 * 128 * 128 + q4 * 16);
    const unsigned tso = (unsigned)((2 * w * 128 + s2) * 128 + q * 64);
    st16(v4u{pkh(tre[0], tim[0]), tag, pkh(tre[1], tim[1]), tag}, rws, to, tso);
    st16(v4u{pkh(tre[2], tim[2]), tag, pkh(tre[3], tim[3]), tag}, rws, w == 7 ? OOB : to, tso + 128 * 128);
  }
  STAMPF(4)
  MEMB();
  PLO();
  const int arow = 16 * w3 + 4 * (r >> 2) + 2 * (r & 1) + 64 * ((r >> 1) & 1) + par;
  v4u fre[4], fim[4];
  {
    const float af = (float)arow * (1.0f / 128.0f);
    const f32x2 s1 = cis(af), s16 = cis(16.0f * af);
    f32x2 z[4][4];
    z[0][0] = cis((float)(4 * q4) * af);
    z[0][1] = crot(z[0][0], s1);
    z[0][2] = cheb(2.0f * s1.x, z[0][1], z[0][0]);
    z[0][3] = cheb(2.0f * s1.x, z[0][2], z[0][1]);
#pragma unroll
    for (int e = 0; e < 4; ++e) {
      z[1][e] = crot(z[0][e], s16);
      z[2][e] = cheb(2.0f * s16.x, z[1][e], z[0][e]);
      z[3][e] = cheb(2.0f * s16.x, z[2][e], z[1][e]);
    }
#pragma unroll
    for (int g = 0; g < 4; ++g)
#pragma unroll
      for (int i = 0; i < 4; ++i) {
        const f32x2 v = z[g][2 * (i & 1) + (i >> 1)];
        fre[g][i] = pkh(v.x, v.y); fim[g][i] = pkh(-v.y, v.x);
      }
#pragma unroll
    for (int g = 0; g < 4; ++g) asm volatile("" : "+v"(fre[g]), "+v"(fim[g]));
  }
  v4u wf[28];
  float cs0 = 0.f, cs1 = 0.f;
  const unsigned trb = L0 + (unsigned)((((lane >> 5) * 4 + ((lane >> 4) & 1)) * 1024) + ((lane >> 2) & 3) * 256 +
                                       (((2 * w + ((lane >> 1) & 1)) ^ (2 * (((lane >> 2) & 3) + 4 * ((lane >> 4) & 1)))) * 16) + (lane & 1) * 8);
#define W_READ(n0, n1)                                                                   \
  if (!SPARE) {                                                                          \
    unsigned t0 = trb, t1 = trb + 65536, t2 = trb + 131072;                              \
    asm volatile("" : "+v"(t0), "+v"(t1), "+v"(t2));                                     \
    _Pragma("unroll") for (int n_ = (n0); n_ < (n1); ++n_) {                             \
      const int sl = wslot(n_);                                                          \
      const unsigned a = (sl < 8 ? t0 : sl < 16 ? t1 : t2) + (sl & 7) * 8192;            \
      const v2u lo = ldtr(a), hi = ldtr(a + 2048);                                       \
      wf[n_] = v4u{lo.x, lo.y, hi.x, hi.y};                                              \
    }                                                                                    \
    _Pragma("unroll") for (int n_ = (n0); n_ < (n1); ++n_) asm volatile("" : "+v"(wf[n_])); \
  }
#define W_SUM(n0, n1)                                                                    \
  if (!SPARE) {                                                                          \
    _Pragma("unroll") for (int n_ = (n0); n_ < (n1); ++n_) {                             \
      cs0 = sum2(wf[n_][0], cs0); cs1 = sum2(wf[n_][1], cs1);                            \
      cs0 = sum2(wf[n_][2], cs0); cs1 = sum2(wf[n_][3], cs1);                            \
    }                                                                                    \
    asm volatile("" : "+v"(cs0), "+v"(cs1));                                             \
  }
  float fa_[4], fb_[4];
  asm volatile("s_waitcnt vmcnt(2)" : "+v"(fraw), "+v"(junk)::"memory");
  *(LAS v4u*)(size_t)(L0 + L_F + (w & 1) * 4096 + (w >> 1) * 1024 + lane * 16) = fraw;
  BAR();
  FLAG(0)
  W_READ(0, 7)
  {
    const unsigned fa = L0 + L_F + (unsigned)((16 * w3 + 4 * q4 + par) * 32 + ((kb <= 224 ? kb : 224) - fc0) * 2);
    float sc = SCALE_B * 0.125f;
    if (kb == 0 || kb == 224) sc = 0.5f * SCALE_B * 0.125f;
    if (kb > 224) sc = 0.f;
#pragma unroll
    for (int i = 0; i < 4; ++i) {
      fa_[i] = blo(*(LAS const unsigned short*)(size_t)(fa + (2 * (i & 1) + 64 * (i >> 1)) * 32)) * sc;
      fb_[i] = blo(*(LAS const unsigned short*)(size_t)(fa + (2 * (i & 1) + 64 * (i >> 1)) * 32 + 4096)) * sc;
    }
#pragma unroll
    for (int i = 0; i < 4; ++i) asm volatile("" : "+v"(fa_[i]), "+v"(fb_[i]));
  }
  BAR();
  STAMPF(5)
  if (DLY_T) __builtin_amdgcn_s_sleep(DLY_T);
  const int dt = w < 7 ? w : 6;
  const int srow = 128 * r + s2;
  v4u ce[4], co[4], se[4], so[4];
  {
    const unsigned tso = (unsigned)((j1b * 16 + tileb) * 16384 + w * 1024);
    const unsigned tvo = tileb < 15 ? (unsigned)lane * 16 : OOB;
    v4u g0, g1;
    PHI();
    ald16<0>(g0, rws, tvo, tso); ald16<0>(g1, rws, tvo, tso + 8192);
    PLO();
    SB();
    {
      f32x2 m = 0.25f * cis((float)(4 * q4 * srow) * (1.0f / 2048.0f));
      const f32x2 ms = cis((float)srow * (1.0f / 2048.0f));
#pragma unroll
      for (int i = 0; i < 4; ++i) {
        mre[i] = pkh(m.x, -m.y); mim[i] = pkh(m.y, m.x);
        if (i < 3) m = crot(m, ms);
      }
      const float df = (float)(2 * (16 * dt + r) + 1) * (1.0f / 896.0f);
      const f32x2 st = cis(df), rk = cis(64.0f * df);
      const float c2 = 2.0f * st.x;
      f32x2 z0 = cis((float)(16 * q4) * df), z1 = crot(z0, st);
#pragma unroll
      for (int kc = 0; kc < 4; ++kc) {
        W_DMA(7 + kc, 8 + kc)
        f32x2 cs[16];
        cs[0] = z0; cs[1] = z1;
        if (kc < 3) { z0 = crot(z0, rk); z1 = crot(z1, rk); }
#pragma unroll
        for (int e = 2; e < 16; ++e) cs[e] = cheb(c2, cs[e - 1], cs[e - 2]);
#pragma unroll
        for (int j = 0; j < 4; ++j) {
          ce[kc][j] = pkh(cs[4 * j].x, cs[4 * j + 2].x); co[kc][j] = pkh(cs[4 * j + 1].x, cs[4 * j + 3].x);
          se[kc][j] = pkh(cs[4 * j].y, cs[4 * j + 2].y); so[kc][j] = pkh(cs[4 * j + 1].y, cs[4 * j + 3].y);
        }
        asm volatile("" : "+v"(ce[kc]), "+v"(co[kc]), "+v"(se[kc]), "+v"(so[kc]));
        SB();
      }
      asm volatile("" : "+v"(mre), "+v"(mim));
    }
    STAMPF(6)
    PHI();
    asm volatile("s_waitcnt vmcnt(4)" : "+v"(g0), "+v"(g1)::"memory");
    int spins = tileb < 15 ? 0 : SPIN_MAX;
    while (!__all(g0.y == tag && g0.w == tag && g1.y == tag && g1.w == tag) && ++spins <= SPIN_MAX) {
      const bool k0 = g0.y == tag && g0.w == tag, k1 = g1.y == tag && g1.w == tag;
      v4u n0, n1;
      if (TSLP) __builtin_amdgcn_s_sleep(TSLP);
      ald16<0>(n0, rws, k0 ? OOB : tvo, tso); ald16<0>(n1, rws, k1 ? OOB : tvo, tso + 8192);
      asm volatile("s_waitcnt vmcnt(0)" : "+v"(n0), "+v"(n1)::"memory");
      if (!k0) g0 = n0;
      if (!k1) g1 = n1;
    }
    CNT(20, tileb < 15 ? spins : 0)
    const int m = 8 * w + (lane >> 3);
    const unsigned o = L0 + L_BT + (unsigned)(((m >> 4) * 64 + ((m >> 2) & 3) * 16 + 4 * (lane & 3) + ((lane >> 2) & 1)) * 16 + (((m >> 1) & 1) + 2 * (m & 1)) * 4);
    *(LAS unsigned*)(size_t)o = hadd(g0.x, g1.x);
    *(LAS unsigned*)(size_t)(o + 32) = hadd(g0.z, g1.z);
    *(LAS unsigned*)(size_t)(o + 4096) = hsub(g0.x, g1.x);
    *(LAS unsigned*)(size_t)(o + 4096 + 32) = hsub(g0.z, g1.z);
    W_DMA(11, 13)
    STAMP(7)
    BAR();
    f32x4 vre = {0, 0, 0, 0}, vim = {0, 0, 0, 0};
    {
      v4u tf[4];
#pragma unroll
      for (int g = 0; g < 4; ++g) tf[g] = lds16(L0 + L_BT + par * 4096 + g * 1024 + lane * 16);
      W_DMA(13, 14)
#pragma unroll
      for (int g = 0; g < 4; ++g) { vre = mfma16h(fre[g], tf[g], vre); vim = mfma16h(fim[g], tf[g], vim); }
    }
    W_DMA(14, 15)
    float pr[4], pi[4];
#pragma unroll
    for (int i = 0; i < 4; ++i) { pr[i] = vre[i] * fa_[i] - vim[i] * fb_[i]; pi[i] = -(vre[i] * fb_[i] + vim[i] * fa_[i]); }
    const unsigned po = L0 + L_BP + w3 * 1024 + lane * 16 + par * 8;
    *(LAS v2u*)(size_t)po = v2u{pkh(pr[0] + pr[2], pi[0] + pi[2]), pkh(pr[1] + pr[3], pi[1] + pi[3])};
    *(LAS v2u*)(size_t)(po + 4096) = v2u{pkh(pr[0] - pr[2], pi[0] - pi[2]), pkh(pr[1] - pr[3], pi[1] - pi[3])};
    W_DMA(15, 17)
    BAR();
    f32x4 ure = {0, 0, 0, 0}, uim = {0, 0, 0, 0};
    {
      v4u pf[4];
#pragma unroll
      for (int g = 0; g < 4; ++g) pf[g] = lds16(L0 + L_BP + par * 4096 + g * 1024 + lane * 16);
      W_DMA(17, 18)
#pragma unroll
      for (int g = 0; g < 4; ++g) { ure = mfma16h(pf[g], fre[g], ure); uim = mfma16h(pf[g], fim[g], uim); }
    }
    const unsigned uo = tileb < 15 ? (unsigned)(arow * 16384 + q4 * 16) : OOB, uso = U_OFF + (unsigned)((tileb * 16 + j1b) * 64);
    st16(v4u{pkh(ure[0] * 0.125f, uim[0] * -0.125f), pkh(ure[1] * 0.125f, uim[1] * -0.125f), pkh(ure[2] * 0.125f, uim[2] * -0.125f),
             pkh(ure[3] * 0.125f, uim[3] * -0.125f)}, rfx, uo, uso);
  }
  STAMP(8)
  MEMB();
  PLO();
  W_DMA(18, 23)
  v4u xv0, xv1;
  {
    const int lc = l32 < 28 ? l32 : 27, row = 2 * w + (lane >> 5);
    const unsigned o = L0 + L_X + (unsigned)((lc * 16 + (row ^ (lc & 15))) * 16);
    *(LAS v4u*)(size_t)o = v4u{xa[0].x, xa[0].y, xa[2].x, xa[2].y};
    *(LAS v4u*)(size_t)(o + 7168) = v4u{xa[1].x, xa[1].y, xa[3].x, xa[3].y};
    VMW(6);
    BAR();
    if (b == 0 && tid == 0) __builtin_amdgcn_raw_buffer_store_b32(tag, rfx, 0, EP_OFF, 16);
    W_READ(7, 18)
    const int lr = 4 * dt + q4;
    const unsigned o2 = L0 + L_X + (unsigned)((lr * 16 + (r ^ (lr & 15))) * 16);
    xv0 = lds16(o2); xv1 = lds16(o2 + 7168);
    asm volatile("" : "+v"(xv0), "+v"(xv1));
    BAR();
  }
  W_DMA(23, 28)
  const rsrc_t rd = mkrs(wd, F_ * D_ * 2);
  const unsigned dvoff = (unsigned)((lane >> 3) * 896 + (((lane & 7) ^ (4 * ((lane >> 4) & 1))) * 16));
  const unsigned dsoff = (unsigned)(224 * w * 896 + nE * 128);
  int erot = 64 + (nE + 2 * (yb & 3)) % 7;
  asm volatile("" : "+s"(erot));
#define ER(i) ((i) + erot >= 71 ? (i) + erot - 71 : (i) + erot - 64)
#define WD_DMA1(j)                                                                                     \
  {                                                                                                    \
    if (SPARE) { if ((j) < 7) ast16(sent4, rfx, tid * 16, clr0 + (28 + (j)) * 8192); }                 \
    else dma(rd, dvoff, dsoff + (32 * ER((j) >> 2) + 8 * (((j) >> 1) & 1) + 16 * ((j) & 1)) * 896, L0 + (dslot(j) * 8 + w) * 1024); \
  }
#define WD_DMA(j0, j1) _Pragma("unroll") for (int j_ = (j0); j_ < (j1); ++j_) WD_DMA1(j_)
  STAMP(9)
  if (DLY_U) __builtin_amdgcn_s_sleep(DLY_U);
  {
    v2u ug[4];
    unsigned fl;
    const unsigned uso = U_OFF + (unsigned)((s2 * 16 + 2 * w) * 1024);
    const unsigned uo = (w == 7 && r >= 8) ? OOB : (unsigned)((r >> 3) * 1024 + q4 * 256 + (r & 7) * 8);
#define U_LOAD()                                                                                       \
  ald4(fl, rfx, l32 * 16, FL_OFF);                                                                      \
  ald8<0>(ug[0], rfx, uo, uso); ald8<64>(ug[1], rfx, uo, uso); ald8<128>(ug[2], rfx, uo, uso); ald8<192>(ug[3], rfx, uo, uso);
    PHI();
    U_LOAD()
    PLO();
    WD_DMA(0, 2)
    W_SUM(0, 18)
    VMW(7);
    BAR();
    FLAG(1)
    W_READ(18, 28)
    STAMP(10)
    PHI();
    asm volatile("s_waitcnt vmcnt(2)" : "+v"(ug[0]), "+v"(ug[1]), "+v"(ug[2]), "+v"(ug[3]), "+v"(fl)::"memory");
    int spins = 0;
#define U_BAD (max4(v4u{ug[0].x, ug[0].y, ug[1].x, ug[1].y}, max4(v4u{ug[2].x, ug[2].y, ug[3].x, ug[3].y}, 0)) == SENT || fl != tag)
    while (__any(U_BAD) && ++spins <= SPIN_MAX) {
      U_LOAD()
      asm volatile("s_waitcnt vmcnt(0)" : "+v"(ug[0]), "+v"(ug[1]), "+v"(ug[2]), "+v"(ug[3]), "+v"(fl)::"memory");
    }
    CNT(21, spins)
    STAMP(11)
    const f32x4 z4 = {0, 0, 0, 0};
#pragma unroll
    for (int u = 0; u < 2; ++u) {
      const v4u uf = {ug[0][u], ug[1][u], ug[2][u], ug[3][u]};
      const f32x4 yre = mfma16h(uf, mre, z4), yim = mfma16h(uf, mim, z4);
      const int c = 2 * w + (q4 >> 1);
      const unsigned o = L0 + L_C + u * 8192 + (c * 16 + (r ^ (c & 7))) * 16 + (q4 & 1) * 8;
      *(LAS v2u*)(size_t)o = v2u{pkh(yre[0], yre[1]), pkh(yre[2], yre[3])};
      *(LAS v2u*)(size_t)(o + 4096) = v2u{pkh(yim[0], yim[1]), pkh(yim[2], yim[3])};
    }
    BAR();
    WD_DMA(2, 4)
    f32x4 Ce = z4, Co = z4, Se = z4, So = z4;
#pragma unroll
    for (int kc = 0; kc < 4; ++kc) {
      const int c = 4 * kc + q4;
      const unsigned o = L0 + L_C + (c * 16 + (r ^ (c & 7))) * 16;
      Ce = mfma16h(ce[kc], lds16(o), Ce);
      Se = mfma16h(se[kc], lds16(o + 4096), Se);
      Co = mfma16h(co[kc], lds16(o + 8192), Co);
      So = mfma16h(so[kc], lds16(o + 12288), So);
    }
    v4u hq[2];
#pragma unroll
    for (int g = 0; g < 2; ++g) {
      const int gg = g ^ q;
      const float sg = gg ? 1.0f : -1.0f;
      const v4u xs = gg ? v4u{__builtin_amdgcn_alignbit(xv1.y, xv1.y, 16), __builtin_amdgcn_alignbit(xv1.x, xv1.x, 16),
                              __builtin_amdgcn_alignbit(xv1.w, xv1.w, 16), __builtin_amdgcn_alignbit(xv1.z, xv1.z, 16)} : xv0;
      float lo[4], hi[4];
#pragma unroll
      for (int i = 0; i < 4; ++i) {
        const float a = __builtin_fmaf(sg, Se[i], Ce[i]), bb = __builtin_fmaf(sg, Co[i], So[i]);
        const float x0 = (i & 1) ? bhi(xs[i >> 1]) : blo(xs[i >> 1]), x1 = (i & 1) ? bhi(xs[2 + (i >> 1)]) : blo(xs[2 + (i >> 1)]);
        lo[i] = __builtin_fmaf(a - bb, SCALE_Y, x0); hi[i] = __builtin_fmaf(a + bb, SCALE_Y, x1);
      }
      hq[g] = v4u{pkb(lo[0], lo[1]), pkb(lo[2], lo[3]), pkb(hi[0], hi[1]), pkb(hi[2], hi[3])};
      if (g == 0) {
        const unsigned lo_ = w < 7 ? (unsigned)((q4 * 64 + r) * 16) : OOB;
        const unsigned lso = LH_OFF + (unsigned)((s2 >> 2) * LH_STR + ((((s2 >> 1) & 1) * 28 + 4 * dt) * 64 + 16 * (s2 & 1) + 32 * q) * 16);
        st16(hq[0], rfx, lo_, lso);
        MEMB();
        STAMP(12)
        SB();
      }
    }
    PLO();
    WD_DMA(4, 8)
    float sum = 0.f;
#pragma unroll
    for (int g = 0; g < 2; ++g)
#pragma unroll
      for (int i = 0; i < 4; ++i) sum += blo(hq[g][i]) + bhi(hq[g][i]);
    sum = xor32_add(xor16_add(sum));
    const float lm = sum * (1.0f / 64.0f);
    float m2 = 0.f;
#pragma unroll
    for (int g = 0; g < 2; ++g)
#pragma unroll
      for (int i = 0; i < 4; ++i) {
        const float d0_ = blo(hq[g][i]) - lm, d1_ = bhi(hq[g][i]) - lm;
        m2 = __builtin_fmaf(d0_, d0_, m2); m2 = __builtin_fmaf(d1_, d1_, m2);
      }
    m2 = xor32_add(xor16_add(m2));
    if (q4 == 0) *(LAS f32x2*)(size_t)(L0 + L_ST + (r * 8 + w) * 8) = f32x2{lm, m2};
    BAR();
    if (w == 7 && q == 0) {
      PHI();
      f32x4 sv[4];
#pragma unroll
      for (int j = 0; j < 4; ++j) sv[j] = *(LAS const f32x4*)(size_t)(L0 + L_ST + r * 64 + j * 16);
      const float mean = (((sv[0].x + sv[0].z) + (sv[1].x + sv[1].z)) + ((sv[2].x + sv[2].z) + sv[3].x)) * (1.0f / 7.0f);
      float M2 = 0.f;
#pragma unroll
      for (int j = 0; j < 7; ++j) {
        const float dlt = sv[j >> 1][2 * (j & 1)] - mean;
        M2 += __builtin_fmaf(64.0f * dlt, dlt, sv[j >> 1][2 * (j & 1) + 1]);
      }
      const float rs = __builtin_amdgcn_rsqf(M2 * (1.0f / 448.0f) + 1e-5f);
      const float m0 = -mean, m1 = m0 - trunc16(m0), m2_ = m1 - trunc16(m1);
      st16(v4u{hi16(m0, m0), hi16(m0, m1), hi16(m1, m2_), hi16(rs, rs - trunc16(rs))}, rfx, q4 == 0 ? (unsigned)r * 16 : OOB, SS_OFF + s2 * 256);
      MEMB();
      PLO();
    }
  }
  if (SPARE) {
    VMW(0);
    BAR();
    FLAG(2)
  } else {
    WD_DMA(8, 12)
    v4u wdf[7][4];
    const unsigned tdb = L0 + (unsigned)(w * 1024 + (q4 >> 1) * 8192 + (4 * (q4 & 1) + ((lane >> 2) & 3)) * 128 + ((lane >> 3) & 1) * 64 + (lane & 3) * 16);
    unsigned td0 = tdb;
#define WD_READ(u)                                                                                     \
  {                                                                                                    \
    constexpr int sl_ = dslot(2 * (u));                                                                \
    asm volatile("" : "+v"(td0));                                                                      \
    const unsigned ta_ = td0 + (sl_ & ~7) * 8192, tb_ = ta_ ^ 64;                                      \
    _Pragma("unroll") for (int nt = 0; nt < 4; ++nt) {                                                 \
      const v2u t_ = ldtr(((nt >> 1) ? tb_ : ta_) + (sl_ & 7) * 8192 + (nt & 1) * 8);                  \
      wdf[(u) >> 1][nt][2 * ((u) & 1)] = t_.x; wdf[(u) >> 1][nt][2 * ((u) & 1) + 1] = t_.y;            \
    }                                                                                                  \
  }
    v4u csf;
    W_SUM(18, 28)
    {
      const float c0_ = xor32_add(cs0 + cs1), c1_ = c0_ - trunc16(c0_), c2_ = c1_ - trunc16(c1_);
      csf = v4u{hi16(c0_, c1_), hi16(c2_, c0_), hi16(c1_, c0_), 0u};
      if (lane >= 32) csf = v4u{0u, 0u, 0u, 0u};
      asm volatile("" : "+v"(csf));
    }
    VMW(4);
    WD_READ(0) WD_READ(1) WD_READ(2) WD_READ(3)
    BAR();
    STAMP(13)
    if (DLY_L) __builtin_amdgcn_s_sleep(DLY_L);
    const unsigned lnso = LH_OFF + (unsigned)(2 * mG) * LH_STR;
    const unsigned la = L0 + lane * 16;
#define LN_DMA(i)                                                                                      \
  {                                                                                                    \
    const int pi_ = 8 * (i) + w, tt_ = pi_ / 28, j_ = pi_ - 28 * tt_;                                  \
    dma1(rfx, lane * 16, lnso + ((i) >= 7 ? LH_STR - 57344 : 0) + (28 * tt_ + KROT(j_)) * 1024, L0 + pi_ * 1024); \
  }
#define LN_MAX(i0, i1) \
  _Pragma("unroll") for (int i_ = (i0); i_ < (i1); ++i_) mx = max4(lds16((i_ < 7 ? la : la + 57344) + w * 1024 + (i_ < 7 ? i_ : i_ - 7) * 8192), mx);
#define LN_FIX(i0, i1)                                                                                 \
  if (__any(mx == SENT)) {                                                                             \
    int spins = 0;                                                                                     \
    do {                                                                                               \
      _Pragma("unroll") for (int i_ = (i0); i_ < (i1); ++i_) LN_DMA(i_)                                \
      VMW(0);                                                                                          \
      mx = 0;                                                                                          \
      LN_MAX(i0, i1)                                                                                   \
      lspins += 1 << (8 * ((i0) / 3));                                                                 \
    } while (__any(mx == SENT) && ++spins < SPIN_MAX);                                                 \
  }
    unsigned mx = 0;
    int lspins = 0;
    PHI();
#pragma unroll
    for (int i = 0; i < 4; ++i) LN_DMA(i)
    PLO();
    {
      const unsigned pb = (unsigned)(28672 + (224 * ((yb >> 3) % 3) + 28 * w + lane) * 128);
      ald4(junk, rfx, lane < 28 ? (pb >= 57344 ? pb + (LH_STR - 57344) : pb) : OOB, lnso);
    }
    VMW(1);
    LN_MAX(0, 4)
    LN_FIX(0, 4)
    BAR();
    STAMP(14)
    f32x16 acc, accp;
    v4u af[NAF], ho, stf;
    v4u hb[4][7];
    unsigned fl;
    float rsv = 0.f, rlv = 0.f;
    const unsigned hso = LH_OFF + (unsigned)((2 * mG + (nD >= 7)) * LH_STR + 57344 + (8 * (nD >= 7 ? nD - 7 : nD) + w) * 1024);
    unsigned hvo = (unsigned)(q4 * 512 + pE * 256 + r * 16);
    asm volatile("" : "+v"(hvo));
    const unsigned hlo = LH_OFF + (unsigned)((2 * mE + (w >> 2)) * LH_STR + 57344 + 14 * (w & 3) * 1024);
    const unsigned sso = SS_OFF + (unsigned)(mG * 2048);
#define H_LOAD(tt, i) hb[tt][i] = ld16(rfx, hvo, hlo + (tt) * 57344 + ER(i) * 2048);
#define H_TOUCH(tt) touch(junk, rfx, hvo, hlo + (tt) * 57344 + ER(0) * 2048);
#define LGK3() asm volatile("s_waitcnt lgkmcnt(3)" ::: "memory")
#define VMWS(n) asm volatile("s_waitcnt vmcnt(" #n ")" : "+v"(stf), "+v"(fl), "+v"(junk)::"memory")
#define FL_FIX(i)                                                                                      \
  if (__any(fl != tag)) {                                                                              \
    int spins = 0;                                                                                     \
    do {                                                                                               \
      ald4(fl, rfx, l32 * 16, FL_OFF + (i) * 512);                                                      \
      VMWS(0);                                                                                         \
      lspins += 1 << 24;                                                                               \
    } while (__any(fl != tag) && ++spins < SPIN_MAX);                                                  \
  }
    float ge = 0.f, gp = 0.f;
#define GLU_STEP(k)                                                                                    \
  {                                                                                                    \
    const int e_ = (k) >> 1;                                                                           \
    if (((k) & 1) == 0) {                                                                              \
      ge = __builtin_amdgcn_exp2f(accp[e_] * rlv);                                                     \
      asm volatile("" : "+v"(ge));                                                                     \
    } else {                                                                                           \
      const float p_ = __builtin_amdgcn_rcpf(1.0f + ge) * (accp[8 + e_] * rsv);                        \
      if (e_ & 1) { unsigned t_ = pkb(gp, p_); asm volatile("" : "+v"(t_)); ho[e_ >> 1] = t_; }        \
      else { gp = p_; asm volatile("" : "+v"(gp)); }                                                   \
    }                                                                                                  \
  }
    auto dpass = [&](auto TT) __attribute__((always_inline)) {
      constexpr int tt = decltype(TT)::value;
      unsigned lbt = la + tt * 28672;
      asm volatile("" : "+v"(lbt));
      __builtin_amdgcn_s_setprio(3);
#pragma unroll
      for (int i = 0; i < NAF - 1; ++i) af[i] = lds16(lbt + i * 1024);
#pragma unroll
      for (int i = 0; i < 16; ++i) acc[i] = 0.f;
      mx = 0;
      SB();
#pragma unroll
      for (int ks = 0; ks < 28; ++ks) {
        if (ks + NAF - 1 < 28) af[(ks + NAF - 1) % NAF] = lds16(lbt + (ks + NAF - 1) * 1024);
        acc = mfma32b(wf[ks], af[ks % NAF], acc);
        SB();
        if (ks == (tt ? 9 : 0)) ald16<tt * 512>(stf, rfx, l32 * 16, sso);
        if (tt > 0) {
          if (ks < 16) GLU_STEP(ks)
          if (ks == 16) { st16(ho, rfx, lane * 16, hso + (tt - 1) * 57344); MEMB(); }
        }
        if (ks == 9) __builtin_amdgcn_s_setprio(2);
        if (ks == 19 && tt < 3) __builtin_amdgcn_s_setprio(1);
        if (tt == 0) {
          if (ks == 1) ald4(fl, rfx, l32 * 16, FL_OFF + 512);
          if (ks >= 2 && ks < 22 && (ks & 1) == 0) LN_DMA(3 + (ks >> 1))
          if (ks == 21) WD_DMA1(12)
          if (ks == 22) WD_DMA1(13)
          if (ks == 4) WD_READ(4)
          if (ks == 7) WD_READ(5)
          if (ks == 24) { VMWS(9); LN_MAX(4, 7) }
        }
        if (tt == 1) {
          if (ks < 8 && (ks & 1) == 1) WD_DMA1(14 + (ks >> 1))
          if (ks == 10 || ks == 12) WD_DMA1(18 + ((ks - 10) >> 1))
          if (ks == 17) { VMW(8); WD_READ(6) }
          if (ks == 18) LN_MAX(7, 10)
          if (ks == 20) LN_MAX(10, 14)
          if (ks == 22) { LGK3(); WD_DMA1(20) }
          if (ks == 24) WD_DMA1(21)
          if (ks == 26) VMWS(5);
        }
        if (tt == 2) {
          if (ks < 8 && (ks & 1) == 0) WD_DMA1(22 + (ks >> 1))
          if (ks == 17) WD_READ(7)
          if (ks == 20) { LGK3(); WD_DMA1(26) }
          if (ks == 22) WD_DMA1(27)
          if (ks == 24) H_TOUCH(0)
          if (ks == 26) VMWS(4);
        }
        if (tt == 3) {
          if (ks == 1) H_TOUCH(1)
          if (ks >= 3 && ks <= 11 && (ks & 1) == 1) H_LOAD(0, (ks - 3) >> 1)
          if (ks == 15 || ks == 17) H_LOAD(0, (ks - 5) >> 1)
          if (ks >= 19 && ks <= 25 && (ks & 1) == 1) H_LOAD(1, (ks - 19) >> 1)
          if (ks == 9) ald4(fl, rfx, l32 * 16, FL_OFF + 1024);
          if (ks == 8) WD_READ(8)
          if (ks == 14) WD_READ(9)
          if (ks == 18) WD_READ(10)
          if (ks == 20) WD_READ(11)
          if (ks == 22) WD_READ(12)
          if (ks == 24) { VMWS(7); WD_READ(13) }
        }
        SB();
      }
      if (__any(max4(stf, 0) == SENT)) {
        int spins = 0;
        do {
          ald16<tt * 512>(stf, rfx, l32 * 16, sso);
          VMWS(0);
          lspins += 1 << 16;
        } while (__any(max4(stf, 0) == SENT) && ++spins < SPIN_MAX);
      }
      accp = mfma32b(csf, stf, acc);
      rsv = blo(stf.w) + bhi(stf.w);
      rlv = rsv * -1.44269504f;
      if (tt == 0) { LN_FIX(4, 7) FL_FIX(1) }
      if (tt == 1) LN_FIX(7, 14)
      if (tt == 3) FL_FIX(2)
      if (tt < 3) BAR();
      if (tt == 0) STAMP(15)
      if (tt == 2) STAMP(2)
    };
    dpass(IC<0>{}); dpass(IC<1>{}); dpass(IC<2>{}); dpass(IC<3>{});
    CNT(22, lspins)
    STAMP(16)
    int espins = 0;
    f32x4 ea[2][4];
    unsigned ln2 = la;
    asm volatile("" : "+v"(ln2));
    ln2 = (ln2 - L0) >> 4;
    unsigned hro, sel0, pa[2], ra, rb, ovo, hra;
    int hstep;
    v2u hr0, hr1;
    f32x4 rp[8];
    const unsigned hrs = LH_OFF + (unsigned)((2 * mE + (w >> 2)) * LH_STR + ((w >> 1) & 1) * 28672 + pE * 256);
    const rsrc_t ro = mkrs(out, S_ * D_ * 2);
#define HR_LOAD() { hr0 = ld8(rfx, hro, hrs); hr1 = ld8(rfx, hro + hstep, hrs); }
#define H_FIX(tt)                                                                                      \
  {                                                                                                    \
    int spins = 0;                                                                                     \
    do {                                                                                               \
      MEMB();                                                                                          \
      _Pragma("unroll") for (int i = 0; i < 7; ++i) H_LOAD(tt, i)                                      \
      mx = 0;                                                                                          \
      _Pragma("unroll") for (int i = 0; i < 7; ++i) mx = max4(hb[tt][i], mx);                          \
      espins += 1 << (8 * (tt));                                                                       \
    } while (__any(mx == SENT) && ++spins < SPIN_MAX);                                                 \
  }
#define E_ZERO(tt) _Pragma("unroll") for (int nt = 0; nt < 4; ++nt) ea[(tt) & 1][nt] = f32x4{0, 0, 0, 0};
#define E_MM(tt, k) ea[(tt) & 1][(k) & 3] = mfma16b(wdf[(k) >> 2][(k) & 3], hb[tt][(k) >> 2], ea[(tt) & 1][(k) & 3]);
#define E_STEP(tt, k) E_MM(tt, k)
#define E_END(tt)                                                                                      \
  if (__any(__builtin_isnan(ea[(tt) & 1][0][0]))) {                                                    \
    H_FIX(tt) E_ZERO(tt)                                                                               \
    _Pragma("unroll") for (int k = 0; k < 28; ++k) E_MM(tt, k)                                         \
  }
#define E_PUT(tt, nt)                                                                                  \
  {                                                                                                    \
    const int sl_ = pslot(tt, nt);                                                                     \
    *(LAS f32x4*)(size_t)((pa[(nt) & 1] + (sl_ & ~7) * 8192) + (sl_ & 7) * 8192) = ea[(tt) & 1][nt];         \
  }
#define R_READ(base, ww) rp[ww] = *(LAS const f32x4*)(size_t)((base) + (ww) * 1024);
#define R_FIN(h_, so)                                                                                  \
  {                                                                                                    \
    const f32x4 s_ = ((rp[0] + rp[1]) + (rp[2] + rp[3])) + ((rp[4] + rp[5]) + (rp[6] + rp[7]));        \
    const unsigned f0 = pkb(s_[0], s_[1]), f1 = pkb(s_[2], s_[3]);                                     \
    __builtin_amdgcn_raw_buffer_store_b64(v2u{pkb(blo(h_.x) + blo(f0), bhi(h_.x) + bhi(f0)), pkb(blo(h_.y) + blo(f1), bhi(h_.y) + bhi(f1))}, \
                                          ro, ovo, so, 16);                                            \
  }
    E_ZERO(0)
    SB();
#pragma unroll
    for (int k = 0; k < 28; ++k) {
      if (k < 16) GLU_STEP(k)
      if (k == 16) { st16(ho, rfx, lane * 16, hso + 3 * 57344); MEMB(); __builtin_amdgcn_s_setprio(1); }
      if (k < 6 && (k & 1) == 0) H_LOAD(1, 4 + (k >> 1))
      E_MM(0, k)
      if (k == 17) H_LOAD(2, 0)
      if (k == 24) {
#pragma unroll
        for (int nt = 0; nt < 2; ++nt) pa[nt] = L0 + w * 1024 + (ln2 >> 4) * 256 + ((ln2 + 2 * (ln2 >> 4) + 8 * nt) & 15) * 16;
        asm volatile("" : "+v"(pa[0]), "+v"(pa[1]));
      }
      if (k == 26) {
        const int c0 = 64 * nE + 32 * (w & 1) + 8 * (int)(ln2 >> 4);
        const int seg = ((c0 >> 4) * 37) >> 8, hi = seg >> 1, mir = seg & 1, cc = c0 - 224 * hi;
        const int ksa = mir ? (220 - cc) >> 2 : cc >> 2;
        hro = (unsigned)((ksa * 64 + 32 * mir + (ln2 & 15)) * 16 + 8 * hi);
        hstep = mir ? -1024 : 1024;
        sel0 = mir ? 0x05040706u : 0x03020100u;
        asm volatile("" : "+v"(hro), "+v"(hstep), "+v"(sel0));
      }
      SB();
    }
    E_END(0)
    STAMP(17)
    PLO();
    E_ZERO(1)
    SB();
#pragma unroll
    for (int k = 0; k < 28; ++k) {
      E_STEP(1, k)
      if (k < 24 && k % 6 == 2) E_PUT(0, (k / 6) & 3)
      if (k == 0) HR_LOAD()
      if (k >= 4 && k < 4 + 4 * H2N && (k & 3) == 0) H_LOAD(2, k >> 2)
      if (k == 23) {
        const int row = 8 * ((w >> 1) & 1) + (int)(ln2 >> 3), g = ln2 & 7, tl = w >> 2;
        const unsigned o = L0 + (unsigned)((g & 1) * 8192 + (g >> 1) * 256 + ((row + 2 * (g >> 1) + 8 * (g & 1)) & 15) * 16);
        ra = o + (unsigned)((tl ? pslot(1, 0) : pslot(0, 0)) + 2 * (w & 1)) * 8192;
        rb = o + (unsigned)(tl ? ((w & 1) ? pslot(3, 2) : pslot(3, 0)) : pslot(2, 0) + 2 * (w & 1)) * 8192;
        hra = L0 + 32768 + (unsigned)((2 * tl + (w & 1)) * 1024 + row * 64 + g * 8);
        ovo = (unsigned)(((128 * row + 8 * mE + 2 * tl + pE) * D_ + 64 * nE + 32 * (w & 1) + 4 * g) * 2);
        asm volatile("" : "+v"(ra), "+v"(rb), "+v"(hra), "+v"(ovo));
      }
      SB();
    }
    E_END(1)
    STAMPE(4)
    E_ZERO(2)
    SB();
#pragma unroll
    for (int k = 0; k < 28; ++k) {
      if (k < 2 * (6 - H2N) && (k & 1) == 0) H_LOAD(2, H2N + 1 + (k >> 1))
      if (k >= H3_AT && k < H3_AT + 7 * H3_ST && (k - H3_AT) % H3_ST == 0) H_LOAD(3, (k - H3_AT) / H3_ST)
      if (k < 24 && k % 6 == 2) E_PUT(1, (k / 6) & 3)
      if (k == 24) {
        if (__any(max(max(hr0.x, hr0.y), max(hr1.x, hr1.y)) == SENT)) {
          int spins = 0;
          do {
            MEMB();
            HR_LOAD()
          } while (__any(max(max(hr0.x, hr0.y), max(hr1.x, hr1.y)) == SENT) && ++spins < SPIN_MAX);
        }
        *(LAS v4u*)(size_t)(L0 + 32768 + w * 1024 + ((ln2 & 15) * 4 + (ln2 >> 4)) * 16) =
            v4u{__builtin_amdgcn_perm(hr0.y, hr0.x, sel0), __builtin_amdgcn_perm(hr0.y, hr0.x, sel0 ^ 0x04040404u),
                __builtin_amdgcn_perm(hr1.y, hr1.x, sel0), __builtin_amdgcn_perm(hr1.y, hr1.x, sel0 ^ 0x04040404u)};
      }
      E_STEP(2, k)
      SB();
    }
    E_END(2)
    STAMPE(5)
    BAR();
    STAMPE(6)
    E_ZERO(3)
    SB();
#pragma unroll
    for (int k = 0; k < 28; ++k) {
      if (k == 0) { hr0 = *(LAS const v2u*)(size_t)hra; hr1 = *(LAS const v2u*)(size_t)(hra + 4096); }
      if (k < 16 && (k & 1) == 0) R_READ(ra, k >> 1)
      if (k == 19) R_FIN(hr0, 0)
      if (k < 24 && k % 6 == 3) E_PUT(2, (k / 6) & 3)
      E_STEP(3, k)
      SB();
    }
    E_END(3)
    E_PUT(3, 0) E_PUT(3, 1) E_PUT(3, 2) E_PUT(3, 3)
    CNT(23, espins)
    STAMP(18)
    BAR();
#pragma unroll
    for (int ww = 0; ww < 8; ++ww) R_READ(rb, ww)
    R_FIN(hr1, 3584)
  }
  asm volatile(".Lce_%0:" ::"n"(SPARE ? 1 : 0) : "memory");
}

__global__ __launch_bounds__(512, 2) void fused(const uint16_t* __restrict__ x, const uint16_t* __restrict__ wr,
                                                const uint16_t* __restrict__ wi, const uint16_t* __restrict__ wg,
                                                const uint16_t* __restrict__ wu, const uint16_t* __restrict__ wd,
                                                unsigned char* __restrict__ ws, uint16_t* __restrict__ out) {
  __shared__ __attribute__((aligned(16))) unsigned char smem[163840];
  const unsigned L0 = (unsigned)(size_t)smem;
  if (blockIdx.x < 224) body<false>(x, wr, wi, wg, wu, wd, ws, out, L0);
  else body<true>(x, wr, wi, wg, wu, wd, ws, out, L0);
}

extern "C" void kernel_launch(void* const* d_in, const int* in_sizes, int n_in, void* d_out, int out_size, void* d_ws,
                              size_t ws_size, hipStream_t stream) {
  fused<<<256, 512, 0, stream>>>((const uint16_t*)d_in[0], (const uint16_t*)d_in[1], (const uint16_t*)d_in[2],
                                 (const uint16_t*)d_in[4], (const uint16_t*)d_in[5], (const uint16_t*)d_in[6],
                                 (unsigned char*)d_ws, (uint16_t*)d_out);
}
```

```cpp
#include <hip/hip_runtime.h>
#include <hip/hip_bf16.h>
#include <stdint.h>

typedef unsigned v4u __attribute__((ext_vector_type(4)));
typedef unsigned v2u __attribute__((ext_vector_type(2)));
typedef float f32x2 __attribute__((ext_vector_type(2)));
typedef float f32x4 __attribute__((ext_vector_type(4)));
typedef float f32x16 __attribute__((ext_vector_type(16)));
typedef _Float16 h8 __attribute__((ext_vector_type(8)));
typedef _Float16 h2 __attribute__((ext_vector_type(2)));
typedef __bf16 b8 __attribute__((ext_vector_type(8)));
typedef __bf16 b2 __attribute__((ext_vector_type(2)));
typedef short s4v __attribute__((ext_vector_type(4)));
typedef __amdgpu_buffer_rsrc_t rsrc_t;
#define DI __device__ __forceinline__
template <int N> struct IC { static constexpr int value = N; };
#define LAS __attribute__((address_space(3)))

constexpr int S_ = 2048, D_ = 448, F_ = 1792, DF_ = 225;
constexpr unsigned MB = 1u << 20;
constexpr unsigned U_OFF = 0;
constexpr unsigned EP_OFF = 2 * MB;
constexpr unsigned FL_OFF = 2 * MB + 4096;
constexpr unsigned SS_OFF = 2 * MB + 65536;
constexpr unsigned LH_OFF = 3 * MB;
constexpr unsigned LH_STR = 5 * 57344;
constexpr unsigned FIX_BYTES = 16 * MB;
constexpr unsigned REG_BYTES = 4 * MB;
#define NROT 8
constexpr unsigned OOB = 0x80000000u;
constexpr unsigned SENT = 0xffffffffu;
constexpr int SPIN_MAX = 20000;
constexpr unsigned L_SP = 17 * 8192;
constexpr unsigned L_A = L_SP, L_BT = L_SP + 8192, L_BP = L_SP + 16384, L_F = L_BP, L_X = L_SP, L_C = L_SP, L_ST = L_SP + 23552;
constexpr int wslot(int n) { return n < 17 ? n : n - 17; }
constexpr int dslot(int j) {
  return j < 2 ? 11 + j : j < 8 ? j - 2 : j < 14 ? j + 6 : j < 18 ? j : j < 20 ? j - 18 : j < 22 ? j - 2 : j < 26 ? j - 20 : j - 12;
}
constexpr int pslot(int tt, int nt) { return tt == 0 ? 6 + nt : tt == 1 ? nt : tt == 2 ? 14 + nt : nt < 2 ? 11 + nt : 16 + nt; }

#define ROTX 2
#define CPF 2
#define TPF 2
#define TPW 6
#define TMASK 1
#define TSLP 0
#define EV 0
#define H2N 4
#define H3_AT 7
#define H3_ST 3
#define SBM 1
#define NAF 3
#define DLY_T 0
#define DLY_U 0
#define DLY_L 0

#define STAMP_TID 0
#define STAMP(i) {}
#define CNT(i, v)

#define STAMPF(i) STAMP(i)
#define STAMPE(i) {}
DI unsigned pkh(float a, float b) { h2 v = {(_Float16)a, (_Float16)b}; return __builtin_bit_cast(unsigned, v); }
DI unsigned pkb(float a, float b) { b2 v = {(__bf16)a, (__bf16)b}; return __builtin_bit_cast(unsigned, v); }
DI float blo(unsigned u) { return __builtin_bit_cast(float, u << 16); }
DI float bhi(unsigned u) { return __builtin_bit_cast(float, u & 0xffff0000u); }
DI float cosr(float x) { return __builtin_amdgcn_cosf(x); }
DI float sinr(float x) { return __builtin_amdgcn_sinf(x); }
DI f32x2 cis(float x) { return f32x2{cosr(x), sinr(x)}; }
DI f32x2 crot(f32x2 a, f32x2 b) {
  return __builtin_elementwise_fma(f32x2{a.y, a.y}, f32x2{-b.y, b.x}, f32x2{a.x, a.x} * b);
}
DI f32x2 cheb(float c2, f32x2 a1, f32x2 a0) { return __builtin_elementwise_fma(f32x2{c2, c2}, a1, -a0); }
DI unsigned hadd(unsigned a, unsigned b) { return __builtin_bit_cast(unsigned, __builtin_bit_cast(h2, a) + __builtin_bit_cast(h2, b)); }
DI unsigned hsub(unsigned a, unsigned b) { return __builtin_bit_cast(unsigned, __builtin_bit_cast(h2, a) - __builtin_bit_cast(h2, b)); }
DI f32x4 mfma16h(v4u a, v4u b, f32x4 c) {
  return __builtin_amdgcn_mfma_f32_16x16x32_f16(__builtin_bit_cast(h8, a), __builtin_bit_cast(h8, b), c, 0, 0, 0);
}
DI f32x4 mfma16b(v4u a, v4u b, f32x4 c) {
  return __builtin_amdgcn_mfma_f32_16x16x32_bf16(__builtin_bit_cast(b8, a), __builtin_bit_cast(b8, b), c, 0, 0, 0);
}
DI f32x16 mfma32b(v4u a, v4u b, f32x16 c) {
  return __builtin_amdgcn_mfma_f32_32x32x16_bf16(__builtin_bit_cast(b8, a), __builtin_bit_cast(b8, b), c, 0, 0, 0);
}
template <int CTRL> DI float dpp_add(float v) {
  return v + __builtin_bit_cast(float, __builtin_amdgcn_update_dpp(0, __builtin_bit_cast(int, v), CTRL, 0xf, 0xf, true));
}
DI float xor16_add(float v) {
  float a = v, b = v;
  asm("s_nop 1\n\tv_permlane16_swap_b32 %0, %1" : "+v"(a), "+v"(b));
  return a + b;
}
DI float xor32_add(float v) {
  float a = v, b = v;
  asm("s_nop 1\n\tv_permlane32_swap_b32 %0, %1" : "+v"(a), "+v"(b));
  return a + b;
}
DI float sum32(float v) {
  v = dpp_add<0xB1>(v); v = dpp_add<0x4E>(v); v = dpp_add<0x141>(v); v = dpp_add<0x140>(v);
  return xor16_add(v);
}
DI rsrc_t mkrs(const void* p, unsigned bytes) { return __builtin_amdgcn_make_buffer_rsrc((void*)p, (short)0, (int)bytes, 0x00020000); }
DI void dma(rsrc_t rs, unsigned voff, unsigned soff, unsigned lds) {
  asm volatile("s_mov_b32 m0, %0\n\ts_nop 0\n\tbuffer_load_dwordx4 %1, %2, %3 offen lds" ::"s"(lds), "v"(voff), "s"(rs), "s"(soff) : "memory");
}
DI void dma1(rsrc_t rs, unsigned voff, unsigned soff, unsigned lds) {
  asm volatile("s_mov_b32 m0, %0\n\ts_nop 0\n\tbuffer_load_dwordx4 %1, %2, %3 offen sc1 lds" ::"s"(lds), "v"(voff), "s"(rs), "s"(soff) : "memory");
}
template <int OFF> DI void ald8(v2u& d, rsrc_t rs, unsigned voff, unsigned soff) {
  asm volatile("buffer_load_dwordx2 %0, %1, %2, %3 offen offset:%4 sc1" : "=&v"(d) : "v"(voff), "s"(rs), "s"(soff), "n"(OFF) : "memory");
}
template <int OFF> DI void ald16(v4u& d, rsrc_t rs, unsigned voff, unsigned soff) {
  asm volatile("buffer_load_dwordx4 %0, %1, %2, %3 offen offset:%4 sc1" : "=&v"(d) : "v"(voff), "s"(rs), "s"(soff), "n"(OFF) : "memory");
}
DI v4u ld16(rsrc_t rs, unsigned voff, unsigned soff) { return __builtin_amdgcn_raw_buffer_load_b128(rs, voff, soff, 16); }
DI v2u ld8(rsrc_t rs, unsigned voff, unsigned soff) { return __builtin_amdgcn_raw_buffer_load_b64(rs, voff, soff, 16); }
DI void st16(v4u v, rsrc_t rs, unsigned voff, unsigned soff) { __builtin_amdgcn_raw_buffer_store_b128(v, rs, voff, soff, 16); }
DI v4u lds16(unsigned a) { return *(LAS const v4u*)(size_t)a; }
DI v2u ldtr(unsigned a) { return __builtin_bit_cast(v2u, __builtin_amdgcn_ds_read_tr16_b64_v4i16((LAS s4v*)(size_t)a)); }
DI unsigned max4(v4u v, unsigned m) { return max(max(max(max(m, v[0]), v[1]), v[2]), v[3]); }
#define BAR() asm volatile("s_waitcnt lgkmcnt(0)\n\ts_barrier" ::: "memory")
#define VMW(n) asm volatile("s_waitcnt vmcnt(" #n ")" ::: "memory")
#define SB() __builtin_amdgcn_sched_barrier(0)
#define MEMB() asm volatile("" ::: "memory")
#define PLO() __builtin_amdgcn_s_setprio(0)
#define PHI() __builtin_amdgcn_s_setprio(2)

constexpr float SCALE_B = 1.0f / 16.0f;
constexpr float SCALE_Y = 512.0f / (917504.0f * SCALE_B);

DI unsigned glu2(float g0, float g1, float u0, float u1, float rs, float rl) {
  const float s0 = __builtin_amdgcn_rcpf(1.0f + __builtin_amdgcn_exp2f(g0 * rl));
  const float s1 = __builtin_amdgcn_rcpf(1.0f + __builtin_amdgcn_exp2f(g1 * rl));
  return pkb(s0 * (u0 * rs), s1 * (u1 * rs));
}
DI void ald4(unsigned& d, rsrc_t rs, unsigned voff, unsigned soff) {
  asm volatile("buffer_load_dword %0, %1, %2, %3 offen sc1" : "=&v"(d) : "v"(voff), "s"(rs), "s"(soff) : "memory");
}
DI void touch(unsigned& d, rsrc_t rs, unsigned voff, unsigned soff) {
  asm volatile("buffer_load_dword %0, %1, %2, %3 offen sc1" : "+v"(d) : "v"(voff), "s"(rs), "s"(soff) : "memory");
}
DI float sum2(unsigned u, float c) {
  const b2 one2 = {(__bf16)1.0f, (__bf16)1.0f};
  return __builtin_amdgcn_fdot2_f32_bf16(__builtin_bit_cast(b2, u), one2, c, false);
}
DI void ast16(v4u v, rsrc_t rs, unsigned voff, unsigned soff) {
  asm volatile("buffer_store_dwordx4 %0, %1, %2, %3 offen sc1" ::"v"(v), "v"(voff), "s"(rs), "s"(soff) : "memory");
}
DI float trunc16(float v) { return bhi(__builtin_bit_cast(unsigned, v)); }
DI unsigned hi16(float a, float b) { return __builtin_amdgcn_perm(__builtin_bit_cast(unsigned, b), __builtin_bit_cast(unsigned, a), 0x07060302u); }

template <bool SPARE>
DI void body(const uint16_t* __restrict__ x, const uint16_t* __restrict__ wr, const uint16_t* __restrict__ wi,
             const uint16_t* __restrict__ wg, const uint16_t* __restrict__ wu, const uint16_t* __restrict__ wd,
             unsigned char* __restrict__ ws, uint16_t* __restrict__ out, const unsigned L0) {
  STAMP(0)
  const int b = blockIdx.x, tid = threadIdx.x, lane = tid & 63;
  const int w = __builtin_amdgcn_readfirstlane(tid >> 6);
  const int r = lane & 15, q4 = lane >> 4;
  const int s2 = (b & 7) + 8 * (b >> 4), q = (b >> 3) & 1;
  const int j1b = b & 15, tileb = b >> 4;
  const int yb = b >> 3;
  const int cel = 28 * (b & 3) + yb, mG = 8 * ((b >> 2) & 1) + (cel & 7), nD = cel >> 3;
  const int mE = 2 * (b & 7) + (yb & 1), pE = (yb >> 1) & 1, nE = yb >> 2;
  const int sbk = 4 * (b & 7) + (yb & 3);
  const rsrc_t rx = mkrs(x, S_ * D_ * 2), rfx = mkrs(ws, FIX_BYTES);
  PHI();

  v2u xa[4];
  unsigned ep;
  const int l32 = lane & 31;
  {
    const int lc = l32 < 28 ? l32 : 27;
    const unsigned rowo = (unsigned)(128 * (2 * w + (lane >> 5)) + s2) * 896u;
    const unsigned o1 = rowo + 8 * lc, o2 = rowo + 440 - 8 * lc;
    asm volatile("buffer_load_dwordx2 %0, %5, %7, 0 offen\n\t"
                 "buffer_load_dwordx2 %1, %6, %7, 0 offen\n\t"
                 "buffer_load_dwordx2 %2, %5, %7, 0 offen offset:448\n\t"
                 "buffer_load_dwordx2 %3, %6, %7, 0 offen offset:448\n\t"
                 "s_load_dword %4, %8, 0"
                 : "=&v"(xa[0]), "=&v"(xa[1]), "=&v"(xa[2]), "=&v"(xa[3]), "=&s"(ep)
                 : "v"(o1), "v"(o2), "s"(rx), "s"(ws + EP_OFF)
                 : "memory");
  }
  PLO();
  const v4u sent4 = {SENT, SENT, SENT, SENT};
  unsigned junk;
  {
    const int i5 = (lane * 52) >> 8, l5 = lane - 5 * i5;
    const unsigned tf = lane < 40 ? (unsigned)((((b & 7) + 8 * (8 * yb + i5)) * 450 + (l5 < 4 ? 128 * l5 : 446)) & ~3) : OOB;
    const unsigned td = lane < 49 ? (unsigned)(49 * b + lane) * 128 : OOB;
    const uint16_t* pc;
    int clen;
    asm volatile("s_getpc_b64 %0\n.Lcs_%2:\n\ts_mov_b32 %1, .Lce_%2-.Lcs_%2\n\ts_max_i32 %1, %1, .Lce_1-.Lcs_%2"
                 : "=s"(pc), "=s"(clen) : "n"(SPARE ? 1 : 0) : "scc");
    const bool cw = SPARE || yb < CPF, tw = yb < CPF + TPF && w < TPW;
    const void* p3 = cw ? (const void*)pc : w == 3 ? (const void*)out : (const void*)(ws + (w == 5 ? FIX_BYTES : 0));
    const unsigned st3 = cw ? 128 : w == 3 ? S_ * D_ * 2 / 64 : w == 4 ? FIX_BYTES / 64 : NROT * REG_BYTES / 64;
    const unsigned n3 = cw ? max(clen, 0) : tw ? 64 * st3 : 0;
    ald4(junk, mkrs(w == 0 ? wr : w == 1 ? wi : w == 2 ? wd : p3, w < 2 ? S_ * DF_ * 2 : w == 2 ? F_ * D_ * 2 : n3),
         w < 2 ? tf : w == 2 ? td : (unsigned)(cw ? tid - 192 : lane) * st3, 0);
    ast16(sent4, rfx, tid * 16, U_OFF + (2 * s2 + q) * 8192);
    if (SPARE) ast16(sent4, rfx, lane < 8 ? (unsigned)(w * 8 + lane) * 16 : OOB, SS_OFF + sbk * 1024);
  }
  const int wmat = w & 1, wrange = w >> 1;
  const rsrc_t rw = mkrs(wmat ? wu : wg, D_ * F_ * 2);
  const unsigned wvoff = (unsigned)(((wrange >> 1) ? 3 - q4 : q4) * 3584 + ((r ^ (2 * (q4 + 4 * wmat))) * 16));
  const unsigned wsoff0 = (unsigned)((224 * (wrange & 1) + ((wrange >> 1) ? 220 : 0)) * 3584 + nD * 256);
  const int wstep = (wrange >> 1) ? -4 * 3584 : 4 * 3584;
  int rot = 64 + ((nD == 3 || nD == 10) ? 7 * (cel & 3) + 3 * ((cel >> 2) & 1) : (7 * (cel & 7)) >> 1) + ROTX * ((b >> 2) & 1);
  asm volatile("" : "+s"(rot));
  const unsigned clr0 = LH_OFF + (unsigned)sbk * LH_STR;
#define KROT(j) ((j) + rot >= 92 ? (j) + rot - 92 : (j) + rot - 64)
#define W_DMA(n0, n1)                                                                                  \
  _Pragma("unroll") for (int n_ = (n0); n_ < (n1); ++n_) {                                             \
    if (SPARE) ast16(sent4, rfx, tid * 16, clr0 + n_ * 8192);                                          \
    else dma(rw, wvoff, wsoff0 + KROT(n_) * wstep, L0 + (wslot(n_) * 8 + w) * 1024);                   \
  }
#define FLAG(i) if (SPARE && tid == 0) __builtin_amdgcn_raw_buffer_store_b32(tag, rfx, sbk * 16, FL_OFF + (i) * 512, 16);
  const int par = w >> 2, w3 = w & 3;
  const int kb = 16 * tileb + r;
  const int fc0 = tileb < 14 ? 16 * tileb : 209;
  SB();
  v4u bc[4], bs[4], mre, mim;
  {
    const int k = 2 * (16 * w + 8 * ((r >> 1) & 1) + 2 * (r >> 2) + (r & 1)) + q;
    const float kf = (float)k * (1.0f / 896.0f);
    const f32x2 st = cis(-2.0f * kf), rk = cis(-64.0f * kf);
    const float c2 = 2.0f * st.x;
    f32x2 z0 = cis((float)(-16 * q4 - 1) * kf), z1 = crot(z0, st);
#pragma unroll
    for (int kc = 0; kc < 4; ++kc) {
      f32x2 cs[8];
      cs[0] = z0; cs[1] = z1;
      if (kc < 3) { z0 = crot(z0, rk); z1 = crot(z1, rk); }
#pragma unroll
      for (int e = 2; e < 8; ++e) cs[e] = cheb(c2, cs[e - 1], cs[e - 2]);
#pragma unroll
      for (int j = 0; j < 4; ++j) { bc[kc][j] = pkh(cs[2 * j].x, cs[2 * j + 1].x); bs[kc][j] = pkh(cs[2 * j].y, cs[2 * j + 1].y); }
    }
    f32x2 m = cis((float)(r * (512 * q4 + s2)) * (1.0f / 2048.0f));
    const f32x2 ms = cis((float)r * (1.0f / 16.0f));
#pragma unroll
    for (int i = 0; i < 4; ++i) {
      mre[i] = pkh(m.x, m.y); mim[i] = pkh(-m.y, m.x);
      if (i < 3) m = crot(m, ms);
    }
#pragma unroll
    for (int kc = 0; kc < 4; ++kc) asm volatile("" : "+v"(bc[kc]), "+v"(bs[kc]));
    asm volatile("" : "+v"(mre), "+v"(mim));
  }
  W_DMA(0, 4)
  PHI();
  STAMP(1)
  asm volatile("s_waitcnt vmcnt(6) lgkmcnt(0)" : "+v"(xa[0]), "+v"(xa[1]), "+v"(xa[2]), "+v"(xa[3]), "+s"(ep)::"memory");
  const unsigned tag = ep * 1664525u + 1013904223u;
  const rsrc_t rws = mkrs(ws + FIX_BYTES + (size_t)(tag & (NROT - 1)) * REG_BYTES, REG_BYTES);
  v4u fraw;
  {
    const unsigned c0 = __builtin_amdgcn_readlane(xa[0].x, 0), c1 = __builtin_amdgcn_readlane(xa[0].x, 32);
    const float cc = 2.0f * blo(lane < 32 ? c0 : c1);
    float s1[4], sb[4], d1[4], d2[4];
    float sum = 0.f, sq = 0.f;
#pragma unroll
    for (int i = 0; i < 4; ++i) {
      const unsigned ua = xa[0][i >> 1], ub = xa[1][1 - (i >> 1)], ue = xa[2][i >> 1], uf = xa[3][1 - (i >> 1)];
      const float a = (i & 1) ? bhi(ua) : blo(ua), e = (i & 1) ? bhi(ue) : blo(ue);
      const float bb = (i & 1) ? blo(ub) : bhi(ub), f = (i & 1) ? blo(uf) : bhi(uf);
      s1[i] = (a + f) - cc; sb[i] = (bb + e) - cc; d1[i] = a - f; d2[i] = e - bb;
      sum += s1[i] + sb[i];
      sq = __builtin_fmaf(s1[i], s1[i], sq); sq = __builtin_fmaf(sb[i], sb[i], sq);
      sq = __builtin_fmaf(d1[i], d1[i], sq); sq = __builtin_fmaf(d2[i], d2[i], sq);
    }
    if (l32 >= 28) { sum = 0.f; sq = 0.f; }
    asm volatile("" : "+v"(sum), "+v"(sq));
    W_DMA(4, 5)
    sum = sum32(sum); sq = sum32(sq);
    asm volatile("" : "+v"(sum), "+v"(sq));
    W_DMA(5, 6)
    const float mu = sum * (1.0f / 448.0f);
    const float var = __builtin_fmaxf(__builtin_fmaf(-mu, mu, sq * (0.5f / 448.0f)), 0.f);
    float rs = __builtin_amdgcn_rsqf(var + 1e-5f);
    if (l32 >= 28) rs = 0.f;
    const float sgn = q ? -1.0f : 1.0f;
    const float off = q ? 0.f : -4.0f * mu * rs;
    float P[4], Q[4];
#pragma unroll
    for (int i = 0; i < 4; ++i) {
      P[i] = __builtin_fmaf(__builtin_fmaf(sgn, sb[i], s1[i]), rs, off);
      Q[i] = __builtin_fmaf(sgn, d2[i], d1[i]) * rs;
    }
    const int c = l32 >> 1, row = 2 * w + (lane >> 5);
    const unsigned o = L0 + L_A + (c * 16 + (row ^ (c & 7))) * 16 + (l32 & 1) * 8;
    *(LAS v2u*)(size_t)o = v2u{pkh(P[0], P[1]), pkh(P[2], P[3])};
    *(LAS v2u*)(size_t)(o + 4096) = v2u{pkh(Q[0], Q[1]), pkh(Q[2], Q[3])};
  }
  W_DMA(6, 7)
  {
    const rsrc_t rf = mkrs((w & 1) ? wi : wr, S_ * DF_ * 2);
    const unsigned fo = (unsigned)(((j1b + 16 * (32 * (w >> 1) + (lane >> 1))) * DF_ + fc0 + 8 * (lane & 1)) * 2);
    asm volatile("buffer_load_dwordx4 %0, %1, %2, 0 offen" : "=&v"(fraw) : "v"(fo), "s"(rf) : "memory");
  }
  VMW(8);
  BAR();
  STAMPF(3)
  {
    f32x4 zre = {0, 0, 0, 0}, zim = {0, 0, 0, 0};
#pragma unroll
    for (int kc = 0; kc < 4; ++kc) {
      const int c = 4 * kc + q4;
      const unsigned o = L0 + L_A + (c * 16 + (r ^ (c & 7))) * 16;
      zre = mfma16h(lds16(o), bc[kc], zre);
      zim = mfma16h(lds16(o + 4096), bs[kc], zim);
    }
    v4u zop;
#pragma unroll
    for (int i = 0; i < 4; ++i) zop[i] = pkh(zre[i], zim[i]);
    const f32x4 z4 = {0, 0, 0, 0};
    const f32x4 tre = mfma16h(zop, mre, z4), tim = mfma16h(zop, mim, z4);
    const unsigned to = (unsigned)(r * 16 * 128 * 128 + q4 * 16);
    const unsigned tso = (unsigned)((2 * w * 128 + s2) * 128 + q * 64);
    st16(v4u{pkh(tre[0], tim[0]), tag, pkh(tre[1], tim[1]), tag}, rws, to, tso);
    st16(v4u{pkh(tre[2], tim[2]), tag, pkh(tre[3], tim[3]), tag}, rws, w == 7 ? OOB : to, tso + 128 * 128);
  }
  STAMPF(4)
  MEMB();
  PLO();
  const int arow = 16 * w3 + 4 * (r >> 2) + 2 * (r & 1) + 64 * ((r >> 1) & 1) + par;
  v4u fre[4], fim[4];
  {
    const float af = (float)arow * (1.0f / 128.0f);
    const f32x2 s1 = cis(af), s16 = cis(16.0f * af);
    f32x2 z[4][4];
    z[0][0] = cis((float)(4 * q4) * af);
    z[0][1] = crot(z[0][0], s1);
    z[0][2] = cheb(2.0f * s1.x, z[0][1], z[0][0]);
    z[0][3] = cheb(2.0f * s1.x, z[0][2], z[0][1]);
#pragma unroll
    for (int e = 0; e < 4; ++e) {
      z[1][e] = crot(z[0][e], s16);
      z[2][e] = cheb(2.0f * s16.x, z[1][e], z[0][e]);
      z[3][e] = cheb(2.0f * s16.x, z[2][e], z[1][e]);
    }
#pragma unroll
    for (int g = 0; g < 4; ++g)
#pragma unroll
      for (int i = 0; i < 4; ++i) {
        const f32x2 v = z[g][2 * (i & 1) + (i >> 1)];
        fre[g][i] = pkh(v.x, v.y); fim[g][i] = pkh(-v.y, v.x);
      }
#pragma unroll
    for (int g = 0; g < 4; ++g) asm volatile("" : "+v"(fre[g]), "+v"(fim[g]));
  }
  v4u wf[28];
  float cs0 = 0.f, cs1 = 0.f;
  const unsigned trb = L0 + (unsigned)((((lane >> 5) * 4 + ((lane >> 4) & 1)) * 1024) + ((lane >> 2) & 3) * 256 +
                                       (((2 * w + ((lane >> 1) & 1)) ^ (2 * (((lane >> 2) & 3) + 4 * ((lane >> 4) & 1)))) * 16) + (lane & 1) * 8);
#define W_READ(n0, n1)                                                                   \
  if (!SPARE) {                                                                          \
    unsigned t0 = trb, t1 = trb + 65536, t2 = trb + 131072;                              \
    asm volatile("" : "+v"(t0), "+v"(t1), "+v"(t2));                                     \
    _Pragma("unroll") for (int n_ = (n0); n_ < (n1); ++n_) {                             \
      const int sl = wslot(n_);                                                          \
      const unsigned a = (sl < 8 ? t0 : sl < 16 ? t1 : t2) + (sl & 7) * 8192;            \
      const v2u lo = ldtr(a), hi = ldtr(a + 2048);                                       \
      wf[n_] = v4u{lo.x, lo.y, hi.x, hi.y};                                              \
    }                                                                                    \
    _Pragma("unroll") for (int n_ = (n0); n_ < (n1); ++n_) asm volatile("" : "+v"(wf[n_])); \
  }
#define W_SUM(n0, n1)                                                                    \
  if (!SPARE) {                                                                          \
    _Pragma("unroll") for (int n_ = (n0); n_ < (n1); ++n_) {                             \
      cs0 = sum2(wf[n_][0], cs0); cs1 = sum2(wf[n_][1], cs1);                            \
      cs0 = sum2(wf[n_][2], cs0); cs1 = sum2(wf[n_][3], cs1);                            \
    }                                                                                    \
    asm volatile("" : "+v"(cs0), "+v"(cs1));                                             \
  }
  float fa_[4], fb_[4];
  asm volatile("s_waitcnt vmcnt(2)" : "+v"(fraw), "+v"(junk)::"memory");
  *(LAS v4u*)(size_t)(L0 + L_F + (w & 1) * 4096 + (w >> 1) * 1024 + lane * 16) = fraw;
  BAR();
  FLAG(0)
  W_READ(0, 7)
  {
    const unsigned fa = L0 + L_F + (unsigned)((16 * w3 + 4 * q4 + par) * 32 + ((kb <= 224 ? kb : 224) - fc0) * 2);
    float sc = SCALE_B * 0.125f;
    if (kb == 0 || kb == 224) sc = 0.5f * SCALE_B * 0.125f;
    if (kb > 224) sc = 0.f;
#pragma unroll
    for (int i = 0; i < 4; ++i) {
      fa_[i] = blo(*(LAS const unsigned short*)(size_t)(fa + (2 * (i & 1) + 64 * (i >> 1)) * 32)) * sc;
      fb_[i] = blo(*(LAS const unsigned short*)(size_t)(fa + (2 * (i & 1) + 64 * (i >> 1)) * 32 + 4096)) * sc;
    }
#pragma unroll
    for (int i = 0; i < 4; ++i) asm volatile("" : "+v"(fa_[i]), "+v"(fb_[i]));
  }
  BAR();
  STAMPF(5)
  if (DLY_T) __builtin_amdgcn_s_sleep(DLY_T);
  const int dt = w < 7 ? w : 6;
  const int srow = 128 * r + s2;
  v4u ce[4], co[4], se[4], so[4];
  {
    const unsigned tso = (unsigned)((j1b * 16 + tileb) * 16384 + w * 1024);
    const unsigned tvo = tileb < 15 ? (unsigned)lane * 16 : OOB;
    v4u g0, g1;
    PHI();
    ald16<0>(g0, rws, tvo, tso); ald16<0>(g1, rws, tvo, tso + 8192);
    PLO();
    SB();
    {
      f32x2 m = 0.25f * cis((float)(4 * q4 * srow) * (1.0f / 2048.0f));
      const f32x2 ms = cis((float)srow * (1.0f / 2048.0f));
#pragma unroll
      for (int i = 0; i < 4; ++i) {
        mre[i] = pkh(m.x, -m.y); mim[i] = pkh(m.y, m.x);
        if (i < 3) m = crot(m, ms);
      }
      const float df = (float)(2 * (16 * dt + r) + 1) * (1.0f / 896.0f);
      const f32x2 st = cis(df), rk = cis(64.0f * df);
      const float c2 = 2.0f * st.x;
      f32x2 z0 = cis((float)(16 * q4) * df), z1 = crot(z0, st);
#pragma unroll
      for (int kc = 0; kc < 4; ++kc) {
        W_DMA(7 + kc, 8 + kc)
        f32x2 cs[16];
        cs[0] = z0; cs[1] = z1;
        if (kc < 3) { z0 = crot(z0, rk); z1 = crot(z1, rk); }
#pragma unroll
        for (int e = 2; e < 16; ++e) cs[e] = cheb(c2, cs[e - 1], cs[e - 2]);
#pragma unroll
        for (int j = 0; j < 4; ++j) {
          ce[kc][j] = pkh(cs[4 * j].x, cs[4 * j + 2].x); co[kc][j] = pkh(cs[4 * j + 1].x, cs[4 * j + 3].x);
          se[kc][j] = pkh(cs[4 * j].y, cs[4 * j + 2].y); so[kc][j] = pkh(cs[4 * j + 1].y, cs[4 * j + 3].y);
        }
        asm volatile("" : "+v"(ce[kc]), "+v"(co[kc]), "+v"(se[kc]), "+v"(so[kc]));
        SB();
      }
      asm volatile("" : "+v"(mre), "+v"(mim));
    }
    STAMPF(6)
    PHI();
    asm volatile("s_waitcnt vmcnt(4)" : "+v"(g0), "+v"(g1)::"memory");
    int spins = tileb < 15 ? 0 : SPIN_MAX;
    while (!__all(g0.y == tag && g0.w == tag && g1.y == tag && g1.w == tag) && ++spins <= SPIN_MAX) {
      const bool k0 = g0.y == tag && g0.w == tag, k1 = g1.y == tag && g1.w == tag;
      v4u n0, n1;
      if (TSLP) __builtin_amdgcn_s_sleep(TSLP);
      ald16<0>(n0, rws, k0 ? OOB : tvo, tso); ald16<0>(n1, rws, k1 ? OOB : tvo, tso + 8192);
      asm volatile("s_waitcnt vmcnt(0)" : "+v"(n0), "+v"(n1)::"memory");
      if (!k0) g0 = n0;
      if (!k1) g1 = n1;
    }
    CNT(20, tileb < 15 ? spins : 0)
    const int m = 8 * w + (lane >> 3);
    const unsigned o = L0 + L_BT + (unsigned)(((m >> 4) * 64 + ((m >> 2) & 3) * 16 + 4 * (lane & 3) + ((lane >> 2) & 1)) * 16 + (((m >> 1) & 1) + 2 * (m & 1)) * 4);
    *(LAS unsigned*)(size_t)o = hadd(g0.x, g1.x);
    *(LAS unsigned*)(size_t)(o + 32) = hadd(g0.z, g1.z);
    *(LAS unsigned*)(size_t)(o + 4096) = hsub(g0.x, g1.x);
    *(LAS unsigned*)(size_t)(o + 4096 + 32) = hsub(g0.z, g1.z);
    W_DMA(11, 13)
    STAMP(7)
    BAR();
    f32x4 vre = {0, 0, 0, 0}, vim = {0, 0, 0, 0};
    {
      v4u tf[4];
#pragma unroll
      for (int g = 0; g < 4; ++g) tf[g] = lds16(L0 + L_BT + par * 4096 + g * 1024 + lane * 16);
      W_DMA(13, 14)
#pragma unroll
      for (int g = 0; g < 4; ++g) { vre = mfma16h(fre[g], tf[g], vre); vim = mfma16h(fim[g], tf[g], vim); }
    }
    W_DMA(14, 15)
    float pr[4], pi[4];
#pragma unroll
    for (int i = 0; i < 4; ++i) { pr[i] = vre[i] * fa_[i] - vim[i] * fb_[i]; pi[i] = -(vre[i] * fb_[i] + vim[i] * fa_[i]); }
    const unsigned po = L0 + L_BP + w3 * 1024 + lane * 16 + par * 8;
    *(LAS v2u*)(size_t)po = v2u{pkh(pr[0] + pr[2], pi[0] + pi[2]), pkh(pr[1] + pr[3], pi[1] + pi[3])};
    *(LAS v2u*)(size_t)(po + 4096) = v2u{pkh(pr[0] - pr[2], pi[0] - pi[2]), pkh(pr[1] - pr[3], pi[1] - pi[3])};
    W_DMA(15, 17)
    BAR();
    f32x4 ure = {0, 0, 0, 0}, uim = {0, 0, 0, 0};
    {
      v4u pf[4];
#pragma unroll
      for (int g = 0; g < 4; ++g) pf[g] = lds16(L0 + L_BP + par * 4096 + g * 1024 + lane * 16);
      W_DMA(17, 18)
#pragma unroll
      for (int g = 0; g < 4; ++g) { ure = mfma16h(pf[g], fre[g], ure); uim = mfma16h(pf[g], fim[g], uim); }
    }
    const unsigned uo = tileb < 15 ? (unsigned)(arow * 16384 + q4 * 16) : OOB, uso = U_OFF + (unsigned)((tileb * 16 + j1b) * 64);
    st16(v4u{pkh(ure[0] * 0.125f, uim[0] * -0.125f), pkh(ure[1] * 0.125f, uim[1] * -0.125f), pkh(ure[2] * 0.125f, uim[2] * -0.125f),
             pkh(ure[3] * 0.125f, uim[3] * -0.125f)}, rfx, uo, uso);
  }
  STAMP(8)
  MEMB();
  PLO();
  W_DMA(18, 23)
  v4u xv0, xv1;
  {
    const int lc = l32 < 28 ? l32 : 27, row = 2 * w + (lane >> 5);
    const unsigned o = L0 + L_X + (unsigned)((lc * 16 + (row ^ (lc & 15))) * 16);
    *(LAS v4u*)(size_t)o = v4u{xa[0].x, xa[0].y, xa[2].x, xa[2].y};
    *(LAS v4u*)(size_t)(o + 7168) = v4u{xa[1].x, xa[1].y, xa[3].x, xa[3].y};
    VMW(6);
    BAR();
    if (b == 0 && tid == 0) __builtin_amdgcn_raw_buffer_store_b32(tag, rfx, 0, EP_OFF, 16);
    W_READ(7, 18)
    const int lr = 4 * dt + q4;
    const unsigned o2 = L0 + L_X + (unsigned)((lr * 16 + (r ^ (lr & 15))) * 16);
    xv0 = lds16(o2); xv1 = lds16(o2 + 7168);
    asm volatile("" : "+v"(xv0), "+v"(xv1));
    BAR();
  }
  W_DMA(23, 28)
  const rsrc_t rd = mkrs(wd, F_ * D_ * 2);
  const unsigned dvoff = (unsigned)((lane >> 3) * 896 + (((lane & 7) ^ (4 * ((lane >> 4) & 1))) * 16));
  const unsigned dsoff = (unsigned)(224 * w * 896 + nE * 128);
  int erot = 64 + (nE + 2 * (yb & 3)) % 7;
  asm volatile("" : "+s"(erot));
#define ER(i) ((i) + erot >= 71 ? (i) + erot - 71 : (i) + erot - 64)
#define WD_DMA1(j)                                                                                     \
  {                                                                                                    \
    if (SPARE) { if ((j) < 7) ast16(sent4, rfx, tid * 16, clr0 + (28 + (j)) * 8192); }                 \
    else dma(rd, dvoff, dsoff + (32 * ER((j) >> 2) + 8 * (((j) >> 1) & 1) + 16 * ((j) & 1)) * 896, L0 + (dslot(j) * 8 + w) * 1024); \
  }
#define WD_DMA(j0, j1) _Pragma("unroll") for (int j_ = (j0); j_ < (j1); ++j_) WD_DMA1(j_)
  STAMP(9)
  if (DLY_U) __builtin_amdgcn_s_sleep(DLY_U);
  {
    v2u ug[4];
    unsigned fl;
    const unsigned uso = U_OFF + (unsigned)((s2 * 16 + 2 * w) * 1024);
    const unsigned uo = (w == 7 && r >= 8) ? OOB : (unsigned)((r >> 3) * 1024 + q4 * 256 + (r & 7) * 8);
#define U_LOAD()                                                                                       \
  ald4(fl, rfx, l32 * 16, FL_OFF);                                                                      \
  ald8<0>(ug[0], rfx, uo, uso); ald8<64>(ug[1], rfx, uo, uso); ald8<128>(ug[2], rfx, uo, uso); ald8<192>(ug[3], rfx, uo, uso);
    PHI();
    U_LOAD()
    PLO();
    WD_DMA(0, 2)
    W_SUM(0, 18)
    VMW(7);
    BAR();
    FLAG(1)
    W_READ(18, 28)
    STAMP(10)
    PHI();
    asm volatile("s_waitcnt vmcnt(2)" : "+v"(ug[0]), "+v"(ug[1]), "+v"(ug[2]), "+v"(ug[3]), "+v"(fl)::"memory");
    int spins = 0;
#define U_BAD (max4(v4u{ug[0].x, ug[0].y, ug[1].x, ug[1].y}, max4(v4u{ug[2].x, ug[2].y, ug[3].x, ug[3].y}, 0)) == SENT || fl != tag)
    while (__any(U_BAD) && ++spins <= SPIN_MAX) {
      U_LOAD()
      asm volatile("s_waitcnt vmcnt(0)" : "+v"(ug[0]), "+v"(ug[1]), "+v"(ug[2]), "+v"(ug[3]), "+v"(fl)::"memory");
    }
    CNT(21, spins)
    STAMP(11)
    const f32x4 z4 = {0, 0, 0, 0};
#pragma unroll
    for (int u = 0; u < 2; ++u) {
      const v4u uf = {ug[0][u], ug[1][u], ug[2][u], ug[3][u]};
      const f32x4 yre = mfma16h(uf, mre, z4), yim = mfma16h(uf, mim, z4);
      const int c = 2 * w + (q4 >> 1);
      const unsigned o = L0 + L_C + u * 8192 + (c * 16 + (r ^ (c & 7))) * 16 + (q4 & 1) * 8;
      *(LAS v2u*)(size_t)o = v2u{pkh(yre[0], yre[1]), pkh(yre[2], yre[3])};
      *(LAS v2u*)(size_t)(o + 4096) = v2u{pkh(yim[0], yim[1]), pkh(yim[2], yim[3])};
    }
    BAR();
    WD_DMA(2, 4)
    f32x4 Ce = z4, Co = z4, Se = z4, So = z4;
#pragma unroll
    for (int kc = 0; kc < 4; ++kc) {
      const int c = 4 * kc + q4;
      const unsigned o = L0 + L_C + (c * 16 + (r ^ (c & 7))) * 16;
      Ce = mfma16h(ce[kc], lds16(o), Ce);
      Se = mfma16h(se[kc], lds16(o + 4096), Se);
      Co = mfma16h(co[kc], lds16(o + 8192), Co);
      So = mfma16h(so[kc], lds16(o + 12288), So);
    }
    v4u hq[2];
#pragma unroll
    for (int g = 0; g < 2; ++g) {
      const int gg = g ^ q;
      const float sg = gg ? 1.0f : -1.0f;
      const v4u xs = gg ? v4u{__builtin_amdgcn_alignbit(xv1.y, xv1.y, 16), __builtin_amdgcn_alignbit(xv1.x, xv1.x, 16),
                              __builtin_amdgcn_alignbit(xv1.w, xv1.w, 16), __builtin_amdgcn_alignbit(xv1.z, xv1.z, 16)} : xv0;
      float lo[4], hi[4];
#pragma unroll
      for (int i = 0; i < 4; ++i) {
        const float a = __builtin_fmaf(sg, Se[i], Ce[i]), bb = __builtin_fmaf(sg, Co[i], So[i]);
        const float x0 = (i & 1) ? bhi(xs[i >> 1]) : blo(xs[i >> 1]), x1 = (i & 1) ? bhi(xs[2 + (i >> 1)]) : blo(xs[2 + (i >> 1)]);
        lo[i] = __builtin_fmaf(a - bb, SCALE_Y, x0); hi[i] = __builtin_fmaf(a + bb, SCALE_Y, x1);
      }
      hq[g] = v4u{pkb(lo[0], lo[1]), pkb(lo[2], lo[3]), pkb(hi[0], hi[1]), pkb(hi[2], hi[3])};
      if (g == 0) {
        const unsigned lo_ = w < 7 ? (unsigned)((q4 * 64 + r) * 16) : OOB;
        const unsigned lso = LH_OFF + (unsigned)((s2 >> 2) * LH_STR + ((((s2 >> 1) & 1) * 28 + 4 * dt) * 64 + 16 * (s2 & 1) + 32 * q) * 16);
        st16(hq[0], rfx, lo_, lso);
        MEMB();
        STAMP(12)
        SB();
      }
    }
    PLO();
    WD_DMA(4, 8)
    float sum = 0.f;
#pragma unroll
    for (int g = 0; g < 2; ++g)
#pragma unroll
      for (int i = 0; i < 4; ++i) sum += blo(hq[g][i]) + bhi(hq[g][i]);
    sum = xor32_add(xor16_add(sum));
    const float lm = sum * (1.0f / 64.0f);
    float m2 = 0.f;
#pragma unroll
    for (int g = 0; g < 2; ++g)
#pragma unroll
      for (int i = 0; i < 4; ++i) {
        const float d0_ = blo(hq[g][i]) - lm, d1_ = bhi(hq[g][i]) - lm;
        m2 = __builtin_fmaf(d0_, d0_, m2); m2 = __builtin_fmaf(d1_, d1_, m2);
      }
    m2 = xor32_add(xor16_add(m2));
    if (q4 == 0) *(LAS f32x2*)(size_t)(L0 + L_ST + (r * 8 + w) * 8) = f32x2{lm, m2};
    BAR();
    if (w == 7 && q == 0) {
      PHI();
      f32x4 sv[4];
#pragma unroll
      for (int j = 0; j < 4; ++j) sv[j] = *(LAS const f32x4*)(size_t)(L0 + L_ST + r * 64 + j * 16);
      const float mean = (((sv[0].x + sv[0].z) + (sv[1].x + sv[1].z)) + ((sv[2].x + sv[2].z) + sv[3].x)) * (1.0f / 7.0f);
      float M2 = 0.f;
#pragma unroll
      for (int j = 0; j < 7; ++j) {
        const float dlt = sv[j >> 1][2 * (j & 1)] - mean;
        M2 += __builtin_fmaf(64.0f * dlt, dlt, sv[j >> 1][2 * (j & 1) + 1]);
      }
      const float rs = __builtin_amdgcn_rsqf(M2 * (1.0f / 448.0f) + 1e-5f);
      const float m0 = -mean, m1 = m0 - trunc16(m0), m2_ = m1 - trunc16(m1);
      st16(v4u{hi16(m0, m0), hi16(m0, m1), hi16(m1, m2_), hi16(rs, rs - trunc16(rs))}, rfx, q4 == 0 ? (unsigned)r * 16 : OOB, SS_OFF + s2 * 256);
      MEMB();
      PLO();
    }
  }
  if (SPARE) {
    VMW(0);
    BAR();
    FLAG(2)
  } else {
    WD_DMA(8, 12)
    v4u wdf[7][4];
    const unsigned tdb = L0 + (unsigned)(w * 1024 + (q4 >> 1) * 8192 + (4 * (q4 & 1) + ((lane >> 2) & 3)) * 128 + ((lane >> 3) & 1) * 64 + (lane & 3) * 16);
    unsigned td0 = tdb;
#define WD_READ(u)                                                                                     \
  {                                                                                                    \
    constexpr int sl_ = dslot(2 * (u));                                                                \
    asm volatile("" : "+v"(td0));                                                                      \
    const unsigned ta_ = td0 + (sl_ & ~7) * 8192, tb_ = ta_ ^ 64;                                      \
    _Pragma("unroll") for (int nt = 0; nt < 4; ++nt) {                                                 \
      const v2u t_ = ldtr(((nt >> 1) ? tb_ : ta_) + (sl_ & 7) * 8192 + (nt & 1) * 8);                  \
      wdf[(u) >> 1][nt][2 * ((u) & 1)] = t_.x; wdf[(u) >> 1][nt][2 * ((u) & 1) + 1] = t_.y;            \
    }                                                                                                  \
  }
    v4u csf;
    W_SUM(18, 28)
    {
      const float c0_ = xor32_add(cs0 + cs1), c1_ = c0_ - trunc16(c0_), c2_ = c1_ - trunc16(c1_);
      csf = v4u{hi16(c0_, c1_), hi16(c2_, c0_), hi16(c1_, c0_), 0u};
      if (lane >= 32) csf = v4u{0u, 0u, 0u, 0u};
      asm volatile("" : "+v"(csf));
    }
    VMW(4);
    WD_READ(0) WD_READ(1) WD_READ(2) WD_READ(3)
    BAR();
    STAMP(13)
    if (DLY_L) __builtin_amdgcn_s_sleep(DLY_L);
    const unsigned lnso = LH_OFF + (unsigned)(2 * mG) * LH_STR;
    const unsigned la = L0 + lane * 16;
#define LN_DMA(i)                                                                                      \
  {                                                                                                    \
    const int pi_ = 8 * (i) + w, tt_ = pi_ / 28, j_ = pi_ - 28 * tt_;                                  \
    dma1(rfx, lane * 16, lnso + ((i) >= 7 ? LH_STR - 57344 : 0) + (28 * tt_ + KROT(j_)) * 1024, L0 + pi_ * 1024); \
  }
#define LN_MAX(i0, i1) \
  _Pragma("unroll") for (int i_ = (i0); i_ < (i1); ++i_) mx = max4(lds16((i_ < 7 ? la : la + 57344) + w * 1024 + (i_ < 7 ? i_ : i_ - 7) * 8192), mx);
#define LN_FIX(i0, i1)                                                                                 \
  if (__any(mx == SENT)) {                                                                             \
    int spins = 0;                                                                                     \
    do {                                                                                               \
      _Pragma("unroll") for (int i_ = (i0); i_ < (i1); ++i_) LN_DMA(i_)                                \
      VMW(0);                                                                                          \
      mx = 0;                                                                                          \
      LN_MAX(i0, i1)                                                                                   \
      lspins += 1 << (8 * ((i0) / 3));                                                                 \
    } while (__any(mx == SENT) && ++spins < SPIN_MAX);                                                 \
  }
    unsigned mx = 0;
    int lspins = 0;
    PHI();
#pragma unroll
    for (int i = 0; i < 4; ++i) LN_DMA(i)
    PLO();
    {
      const unsigned pb = (unsigned)(28672 + (224 * ((yb >> 3) % 3) + 28 * w + lane) * 128);
      ald4(junk, rfx, lane < 28 ? (pb >= 57344 ? pb + (LH_STR - 57344) : pb) : OOB, lnso);
    }
    VMW(1);
    LN_MAX(0, 4)
    LN_FIX(0, 4)
    BAR();
    STAMP(14)
    f32x16 acc, accp;
    v4u af[NAF], ho, stf;
    v4u hb[4][7];
    unsigned fl;
    float rsv = 0.f, rlv = 0.f;
    const unsigned hso = LH_OFF + (unsigned)((2 * mG + (nD >= 7)) * LH_STR + 57344 + (8 * (nD >= 7 ? nD - 7 : nD) + w) * 1024);
    unsigned hvo = (unsigned)(q4 * 512 + pE * 256 + r * 16);
    asm volatile("" : "+v"(hvo));
    const unsigned hlo = LH_OFF + (unsigned)((2 * mE + (w >> 2)) * LH_STR + 57344 + 14 * (w & 3) * 1024);
    const unsigned sso = SS_OFF + (unsigned)(mG * 2048);
#define H_LOAD(tt, i) hb[tt][i] = ld16(rfx, hvo, hlo + (tt) * 57344 + ER(i) * 2048);
#define H_TOUCH(tt) touch(junk, rfx, hvo, hlo + (tt) * 57344 + ER(0) * 2048);
#define LGK3() asm volatile("s_waitcnt lgkmcnt(3)" ::: "memory")
#define VMWS(n) asm volatile("s_waitcnt vmcnt(" #n ")" : "+v"(stf), "+v"(fl), "+v"(junk)::"memory")
#define FL_FIX(i)                                                                                      \
  if (__any(fl != tag)) {                                                                              \
    int spins = 0;                                                                                     \
    do {                                                                                               \
      ald4(fl, rfx, l32 * 16, FL_OFF + (i) * 512);                                                      \
      VMWS(0);                                                                                         \
      lspins += 1 << 24;                                                                               \
    } while (__any(fl != tag) && ++spins < SPIN_MAX);                                                  \
  }
    float ge = 0.f, gp = 0.f;
#define GLU_STEP(k)                                                                                    \
  {                                                                                                    \
    const int e_ = (k) >> 1;                                                                           \
    if (((k) & 1) == 0) {                                                                              \
      ge = __builtin_amdgcn_exp2f(accp[e_] * rlv);                                                     \
      asm volatile("" : "+v"(ge));                                                                     \
    } else {                                                                                           \
      const float p_ = __builtin_amdgcn_rcpf(1.0f + ge) * (accp[8 + e_] * rsv);                        \
      if (e_ & 1) { unsigned t_ = pkb(gp, p_); asm volatile("" : "+v"(t_)); ho[e_ >> 1] = t_; }        \
      else { gp = p_; asm volatile("" : "+v"(gp)); }                                                   \
    }                                                                                                  \
  }
    auto dpass = [&](auto TT) __attribute__((always_inline)) {
      constexpr int tt = decltype(TT)::value;
      unsigned lbt = la + tt * 28672;
      asm volatile("" : "+v"(lbt));
      __builtin_amdgcn_s_setprio(3);
#pragma unroll
      for (int i = 0; i < NAF - 1; ++i) af[i] = lds16(lbt + i * 1024);
#pragma unroll
      for (int i = 0; i < 16; ++i) acc[i] = 0.f;
      mx = 0;
      SB();
#pragma unroll
      for (int ks = 0; ks < 28; ++ks) {
        if (ks + NAF - 1 < 28) af[(ks + NAF - 1) % NAF] = lds16(lbt + (ks + NAF - 1) * 1024);
        acc = mfma32b(wf[ks], af[ks % NAF], acc);
        SB();
        if (ks == (tt ? 9 : 0)) ald16<tt * 512>(stf, rfx, l32 * 16, sso);
        if (tt > 0) {
          if (ks < 16) GLU_STEP(ks)
          if (ks == 16) { st16(ho, rfx, lane * 16, hso + (tt - 1) * 57344); MEMB(); }
        }
        if (ks == 9) __builtin_amdgcn_s_setprio(2);
        if (ks == 19 && tt < 3) __builtin_amdgcn_s_setprio(1);
        if (tt == 0) {
          if (ks == 1) ald4(fl, rfx, l32 * 16, FL_OFF + 512);
          if (ks >= 2 && ks < 22 && (ks & 1) == 0) LN_DMA(3 + (ks >> 1))
          if (ks == 21) WD_DMA1(12)
          if (ks == 22) WD_DMA1(13)
          if (ks == 4) WD_READ(4)
          if (ks == 7) WD_READ(5)
          if (ks == 24) { VMWS(9); LN_MAX(4, 7) }
        }
        if (tt == 1) {
          if (ks < 8 && (ks & 1) == 1) WD_DMA1(14 + (ks >> 1))
          if (ks == 10 || ks == 12) WD_DMA1(18 + ((ks - 10) >> 1))
          if (ks == 17) { VMW(8); WD_READ(6) }
          if (ks == 18) LN_MAX(7, 10)
          if (ks == 20) LN_MAX(10, 14)
          if (ks == 22) { LGK3(); WD_DMA1(20) }
          if (ks == 24) WD_DMA1(21)
          if (ks == 26) VMWS(5);
        }
        if (tt == 2) {
          if (ks < 8 && (ks & 1) == 0) WD_DMA1(22 + (ks >> 1))
          if (ks == 17) WD_READ(7)
          if (ks == 20) { LGK3(); WD_DMA1(26) }
          if (ks == 22) WD_DMA1(27)
          if (ks == 24) H_TOUCH(0)
          if (ks == 26) VMWS(4);
        }
        if (tt == 3) {
          if (ks == 1) H_TOUCH(1)
          if (ks >= 3 && ks <= 11 && (ks & 1) == 1) H_LOAD(0, (ks - 3) >> 1)
          if (ks == 15 || ks == 17) H_LOAD(0, (ks - 5) >> 1)
          if (ks >= 19 && ks <= 25 && (ks & 1) == 1) H_LOAD(1, (ks - 19) >> 1)
          if (ks == 9) ald4(fl, rfx, l32 * 16, FL_OFF + 1024);
          if (ks == 8) WD_READ(8)
          if (ks == 14) WD_READ(9)
          if (ks == 18) WD_READ(10)
          if (ks == 20) WD_READ(11)
          if (ks == 22) WD_READ(12)
          if (ks == 24) { VMWS(7); WD_READ(13) }
        }
        SB();
      }
      if (__any(max4(stf, 0) == SENT)) {
        int spins = 0;
        do {
          ald16<tt * 512>(stf, rfx, l32 * 16, sso);
          VMWS(0);
          lspins += 1 << 16;
        } while (__any(max4(stf, 0) == SENT) && ++spins < SPIN_MAX);
      }
      accp = mfma32b(csf, stf, acc);
      rsv = blo(stf.w) + bhi(stf.w);
      rlv = rsv * -1.44269504f;
      if (tt == 0) { LN_FIX(4, 7) FL_FIX(1) }
      if (tt == 1) LN_FIX(7, 14)
      if (tt == 3) FL_FIX(2)
      if (tt < 3) BAR();
      if (tt == 0) STAMP(15)
      if (tt == 2) STAMP(2)
    };
    dpass(IC<0>{}); dpass(IC<1>{}); dpass(IC<2>{}); dpass(IC<3>{});
    CNT(22, lspins)
    STAMP(16)
    int espins = 0;
    f32x4 ea[2][4];
    unsigned ln2 = la;
    asm volatile("" : "+v"(ln2));
    ln2 = (ln2 - L0) >> 4;
    unsigned hro, sel0, pa[2], ra, rb, ovo, hra;
    int hstep;
    v2u hr0, hr1;
    f32x4 rp[8];
    const unsigned hrs = LH_OFF + (unsigned)((2 * mE + (w >> 2)) * LH_STR + ((w >> 1) & 1) * 28672 + pE * 256);
    const rsrc_t ro = mkrs(out, S_ * D_ * 2);
#define HR_LOAD() { hr0 = ld8(rfx, hro, hrs); hr1 = ld8(rfx, hro + hstep, hrs); }
#define H_FIX(tt)                                                                                      \
  {                                                                                                    \
    int spins = 0;                                                                                     \
    do {                                                                                               \
      MEMB();                                                                                          \
      _Pragma("unroll") for (int i = 0; i < 7; ++i) H_LOAD(tt, i)                                      \
      mx = 0;                                                                                          \
      _Pragma("unroll") for (int i = 0; i < 7; ++i) mx = max4(hb[tt][i], mx);                          \
      espins += 1 << (8 * (tt));                                                                       \
    } while (__any(mx == SENT) && ++spins < SPIN_MAX);                                                 \
  }
#define E_ZERO(tt) _Pragma("unroll") for (int nt = 0; nt < 4; ++nt) ea[(tt) & 1][nt] = f32x4{0, 0, 0, 0};
#define E_MM(tt, k) ea[(tt) & 1][(k) & 3] = mfma16b(wdf[(k) >> 2][(k) & 3], hb[tt][(k) >> 2], ea[(tt) & 1][(k) & 3]);
#define E_STEP(tt, k) E_MM(tt, k)
#define E_END(tt)                                                                                      \
  if (__any(__builtin_isnan(ea[(tt) & 1][0][0]))) {                                                    \
    H_FIX(tt) E_ZERO(tt)                                                                               \
    _Pragma("unroll") for (int k = 0; k < 28; ++k) E_MM(tt, k)                                         \
  }
#define E_PUT(tt, nt)                                                                                  \
  {                                                                                                    \
    const int sl_ = pslot(tt, nt);                                                                     \
    *(LAS f32x4*)(size_t)((pa[(nt) & 1] + (sl_ & ~7) * 8192) + (sl_ & 7) * 8192) = ea[(tt) & 1][nt];         \
  }
#define R_READ(base, ww) rp[ww] = *(LAS const f32x4*)(size_t)((base) + (ww) * 1024);
#define R_FIN(h_, so)                                                                                  \
  {                                                                                                    \
    const f32x4 s_ = ((rp[0] + rp[1]) + (rp[2] + rp[3])) + ((rp[4] + rp[5]) + (rp[6] + rp[7]));        \
    const unsigned f0 = pkb(s_[0], s_[1]), f1 = pkb(s_[2], s_[3]);                                     \
    __builtin_amdgcn_raw_buffer_store_b64(v2u{pkb(blo(h_.x) + blo(f0), bhi(h_.x) + bhi(f0)), pkb(blo(h_.y) + blo(f1), bhi(h_.y) + bhi(f1))}, \
                                          ro, ovo, so, 16);                                            \
  }
    E_ZERO(0)
    SB();
#pragma unroll
    for (int k = 0; k < 28; ++k) {
      if (k < 16) GLU_STEP(k)
      if (k == 16) { st16(ho, rfx, lane * 16, hso + 3 * 57344); MEMB(); __builtin_amdgcn_s_setprio(1); }
      if (k < 6 && (k & 1) == 0) H_LOAD(1, 4 + (k >> 1))
      E_MM(0, k)
      if (k == 17) H_LOAD(2, 0)
      if (k == 24) {
#pragma unroll
        for (int nt = 0; nt < 2; ++nt) pa[nt] = L0 + w * 1024 + (ln2 >> 4) * 256 + ((ln2 + 2 * (ln2 >> 4) + 8 * nt) & 15) * 16;
        asm volatile("" : "+v"(pa[0]), "+v"(pa[1]));
      }
      if (k == 26) {
        const int c0 = 64 * nE + 32 * (w & 1) + 8 * (int)(ln2 >> 4);
        const int seg = ((c0 >> 4) * 37) >> 8, hi = seg >> 1, mir = seg & 1, cc = c0 - 224 * hi;
        const int ksa = mir ? (220 - cc) >> 2 : cc >> 2;
        hro = (unsigned)((ksa * 64 + 32 * mir + (ln2 & 15)) * 16 + 8 * hi);
        hstep = mir ? -1024 : 1024;
        sel0 = mir ? 0x05040706u : 0x03020100u;
        asm volatile("" : "+v"(hro), "+v"(hstep), "+v"(sel0));
      }
      SB();
    }
    E_END(0)
    STAMP(17)
    PLO();
    E_ZERO(1)
    SB();
#pragma unroll
    for (int k = 0; k < 28; ++k) {
      E_STEP(1, k)
      if (k < 24 && k % 6 == 2) E_PUT(0, (k / 6) & 3)
      if (k == 0) HR_LOAD()
      if (k >= 4 && k < 4 + 4 * H2N && (k & 3) == 0) H_LOAD(2, k >> 2)
      if (k == 23) {
        const int row = 8 * ((w >> 1) & 1) + (int)(ln2 >> 3), g = ln2 & 7, tl = w >> 2;
        const unsigned o = L0 + (unsigned)((g & 1) * 8192 + (g >> 1) * 256 + ((row + 2 * (g >> 1) + 8 * (g & 1)) & 15) * 16);
        ra = o + (unsigned)((tl ? pslot(1, 0) : pslot(0, 0)) + 2 * (w & 1)) * 8192;
        rb = o + (unsigned)(tl ? ((w & 1) ? pslot(3, 2) : pslot(3, 0)) : pslot(2, 0) + 2 * (w & 1)) * 8192;
        hra = L0 + 32768 + (unsigned)((2 * tl + (w & 1)) * 1024 + row * 64 + g * 8);
        ovo = (unsigned)(((128 * row + 8 * mE + 2 * tl + pE) * D_ + 64 * nE + 32 * (w & 1) + 4 * g) * 2);
        asm volatile("" : "+v"(ra), "+v"(rb), "+v"(hra), "+v"(ovo));
      }
      SB();
    }
    E_END(1)
    STAMPE(4)
    E_ZERO(2)
    SB();
#pragma unroll
    for (int k = 0; k < 28; ++k) {
      if (k < 2 * (6 - H2N) && (k & 1) == 0) H_LOAD(2, H2N + 1 + (k >> 1))
      if (k >= H3_AT && k < H3_AT + 7 * H3_ST && (k - H3_AT) % H3_ST == 0) H_LOAD(3, (k - H3_AT) / H3_ST)
      if (k < 24 && k % 6 == 2) E_PUT(1, (k / 6) & 3)
      if (k == 24) {
        if (__any(max(max(hr0.x, hr0.y), max(hr1.x, hr1.y)) == SENT)) {
          int spins = 0;
          do {
            MEMB();
            HR_LOAD()
          } while (__any(max(max(hr0.x, hr0.y), max(hr1.x, hr1.y)) == SENT) && ++spins < SPIN_MAX);
        }
        *(LAS v4u*)(size_t)(L0 + 32768 + w * 1024 + ((ln2 & 15) * 4 + (ln2 >> 4)) * 16) =
            v4u{__builtin_amdgcn_perm(hr0.y, hr0.x, sel0), __builtin_amdgcn_perm(hr0.y, hr0.x, sel0 ^ 0x04040404u),
                __builtin_amdgcn_perm(hr1.y, hr1.x, sel0), __builtin_amdgcn_perm(hr1.y, hr1.x, sel0 ^ 0x04040404u)};
      }
      E_STEP(2, k)
      SB();
    }
    E_END(2)
    STAMPE(5)
    BAR();
    STAMPE(6)
    E_ZERO(3)
    SB();
#pragma unroll
    for (int k = 0; k < 28; ++k) {
      if (k == 0) { hr0 = *(LAS const v2u*)(size_t)hra; hr1 = *(LAS const v2u*)(size_t)(hra + 4096); }
      if (k < 16 && (k & 1) == 0) R_READ(ra, k >> 1)
      if (k == 19) R_FIN(hr0, 0)
      if (k < 24 && k % 6 == 3) E_PUT(2, (k / 6) & 3)
      E_STEP(3, k)
      SB();
    }
    E_END(3)
    E_PUT(3, 0) E_PUT(3, 1) E_PUT(3, 2) E_PUT(3, 3)
    CNT(23, espins)
    STAMP(18)
    BAR();
#pragma unroll
    for (int ww = 0; ww < 8; ++ww) R_READ(rb, ww)
    R_FIN(hr1, 3584)
  }
  asm volatile(".Lce_%0:" ::"n"(SPARE ? 1 : 0) : "memory");
}

__global__ __launch_bounds__(512, 2) void fused(const uint16_t* __restrict__ x, const uint16_t* __restrict__ wr,
                                                const uint16_t* __restrict__ wi, const uint16_t* __restrict__ wg,
                                                const uint16_t* __restrict__ wu, const uint16_t* __restrict__ wd,
                                                unsigned char* __restrict__ ws, uint16_t* __restrict__ out) {
  __shared__ __attribute__((aligned(16))) unsigned char smem[163840];
  const unsigned L0 = (unsigned)(size_t)smem;
  if (__builtin_expect(blockIdx.x < 224, 1)) body<false>(x, wr, wi, wg, wu, wd, ws, out, L0);
  else body<true>(x, wr, wi, wg, wu, wd, ws, out, L0);
}

extern "C" void kernel_launch(void* const* d_in, const int* in_sizes, int n_in, void* d_out, int out_size, void* d_ws,
                              size_t ws_size, hipStream_t stream) {
  fused<<<256, 512, 0, stream>>>((const uint16_t*)d_in[0], (const uint16_t*)d_in[1], (const uint16_t*)d_in[2],
                                 (const uint16_t*)d_in[4], (const uint16_t*)d_in[5], (const uint16_t*)d_in[6],
                                 (unsigned char*)d_ws, (uint16_t*)d_out);
}
```

```cpp
#include <hip/hip_runtime.h>
#include <hip/hip_bf16.h>
#include <stdint.h>

typedef unsigned v4u __attribute__((ext_vector_type(4)));
typedef unsigned v2u __attribute__((ext_vector_type(2)));
typedef float f32x2 __attribute__((ext_vector_type(2)));
typedef float f32x4 __attribute__((ext_vector_type(4)));
typedef float f32x16 __attribute__((ext_vector_type(16)));
typedef _Float16 h8 __attribute__((ext_vector_type(8)));
typedef _Float16 h2 __attribute__((ext_vector_type(2)));
typedef __bf16 b8 __attribute__((ext_vector_type(8)));
typedef __bf16 b2 __attribute__((ext_vector_type(2)));
typedef short s4v __attribute__((ext_vector_type(4)));
typedef __amdgpu_buffer_rsrc_t rsrc_t;
#define DI __device__ __forceinline__
template <int N> struct IC { static constexpr int value = N; };
#define LAS __attribute__((address_space(3)))

constexpr int S_ = 2048, D_ = 448, F_ = 1792, DF_ = 225;
constexpr unsigned MB = 1u << 20;
constexpr unsigned U_OFF = 0;
constexpr unsigned EP_OFF = 2 * MB;
constexpr unsigned FL_OFF = 2 * MB + 4096;
constexpr unsigned SS_OFF = 2 * MB + 65536;
constexpr unsigned LH_OFF = 3 * MB;
constexpr unsigned LH_STR = 5 * 57344;
constexpr unsigned FIX_BYTES = 16 * MB;
constexpr unsigned REG_BYTES = 4 * MB;
#define NROT 8
constexpr unsigned OOB = 0x80000000u;
constexpr unsigned SENT = 0xffffffffu;
constexpr int SPIN_MAX = 20000;
constexpr unsigned L_SP = 17 * 8192;
constexpr unsigned L_A = L_SP, L_BT = L_SP + 8192, L_BP = L_SP + 16384, L_F = L_BP, L_X = L_SP, L_C = L_SP, L_ST = L_SP + 23552;
constexpr int wslot(int n) { return n < 17 ? n : n - 17; }
constexpr int dslot(int j) {
  return j < 2 ? 11 + j : j < 8 ? j - 2 : j < 14 ? j + 6 : j < 18 ? j : j < 20 ? j - 18 : j < 22 ? j - 2 : j < 26 ? j - 20 : j - 12;
}
constexpr int pslot(int tt, int nt) { return tt == 0 ? 6 + nt : tt == 1 ? nt : tt == 2 ? 14 + nt : nt < 2 ? 11 + nt : 16 + nt; }

#define ROTX 2
#define CPF 2
#define TMASK 1
#define TSLP 0
#define EV 0
#define H2N 4
#define H3_AT 7
#define H3_ST 3
#define SBM 1
#define NAF 3
#define DLY_T 0
#define DLY_U 0
#define DLY_L 0

#define STAMP_TID 0
#define STAMP(i) {}
#define CNT(i, v)

#define STAMPF(i) STAMP(i)
#define STAMPE(i) {}
DI unsigned pkh(float a, float b) { h2 v = {(_Float16)a, (_Float16)b}; return __builtin_bit_cast(unsigned, v); }
DI unsigned pkb(float a, float b) { b2 v = {(__bf16)a, (__bf16)b}; return __builtin_bit_cast(unsigned, v); }
DI float blo(unsigned u) { return __builtin_bit_cast(float, u << 16); }
DI float bhi(unsigned u) { return __builtin_bit_cast(float, u & 0xffff0000u); }
DI float cosr(float x) { return __builtin_amdgcn_cosf(x); }
DI float sinr(float x) { return __builtin_amdgcn_sinf(x); }
DI f32x2 cis(float x) { return f32x2{cosr(x), sinr(x)}; }
DI f32x2 crot(f32x2 a, f32x2 b) {
  return __builtin_elementwise_fma(f32x2{a.y, a.y}, f32x2{-b.y, b.x}, f32x2{a.x, a.x} * b);
}
DI f32x2 cheb(float c2, f32x2 a1, f32x2 a0) { return __builtin_elementwise_fma(f32x2{c2, c2}, a1, -a0); }
DI unsigned hadd(unsigned a, unsigned b) { return __builtin_bit_cast(unsigned, __builtin_bit_cast(h2, a) + __builtin_bit_cast(h2, b)); }
DI unsigned hsub(unsigned a, unsigned b) { return __builtin_bit_cast(unsigned, __builtin_bit_cast(h2, a) - __builtin_bit_cast(h2, b)); }
DI f32x4 mfma16h(v4u a, v4u b, f32x4 c) {
  return __builtin_amdgcn_mfma_f32_16x16x32_f16(__builtin_bit_cast(h8, a), __builtin_bit_cast(h8, b), c, 0, 0, 0);
}
DI f32x4 mfma16b(v4u a, v4u b, f32x4 c) {
  return __builtin_amdgcn_mfma_f32_16x16x32_bf16(__builtin_bit_cast(b8, a), __builtin_bit_cast(b8, b), c, 0, 0, 0);
}
DI f32x16 mfma32b(v4u a, v4u b, f32x16 c) {
  return __builtin_amdgcn_mfma_f32_32x32x16_bf16(__builtin_bit_cast(b8, a), __builtin_bit_cast(b8, b), c, 0, 0, 0);
}
template <int CTRL> DI float dpp_add(float v) {
  return v + __builtin_bit_cast(float, __builtin_amdgcn_update_dpp(0, __builtin_bit_cast(int, v), CTRL, 0xf, 0xf, true));
}
DI float xor16_add(float v) {
  float a = v, b = v;
  asm("s_nop 1\n\tv_permlane16_swap_b32 %0, %1" : "+v"(a), "+v"(b));
  return a + b;
}
DI float xor32_add(float v) {
  float a = v, b = v;
  asm("s_nop 1\n\tv_permlane32_swap_b32 %0, %1" : "+v"(a), "+v"(b));
  return a + b;
}
DI float sum32(float v) {
  v = dpp_add<0xB1>(v); v = dpp_add<0x4E>(v); v = dpp_add<0x141>(v); v = dpp_add<0x140>(v);
  return xor16_add(v);
}
DI rsrc_t mkrs(const void* p, unsigned bytes) { return __builtin_amdgcn_make_buffer_rsrc((void*)p, (short)0, (int)bytes, 0x00020000); }
DI void dma(rsrc_t rs, unsigned voff, unsigned soff, unsigned lds) {
  asm volatile("s_mov_b32 m0, %0\n\ts_nop 0\n\tbuffer_load_dwordx4 %1, %2, %3 offen lds" ::"s"(lds), "v"(voff), "s"(rs), "s"(soff) : "memory");
}
DI void dma1(rsrc_t rs, unsigned voff, unsigned soff, unsigned lds) {
  asm volatile("s_mov_b32 m0, %0\n\ts_nop 0\n\tbuffer_load_dwordx4 %1, %2, %3 offen sc1 lds" ::"s"(lds), "v"(voff), "s"(rs), "s"(soff) : "memory");
}
template <int OFF> DI void ald8(v2u& d, rsrc_t rs, unsigned voff, unsigned soff) {
  asm volatile("buffer_load_dwordx2 %0, %1, %2, %3 offen offset:%4 sc1" : "=&v"(d) : "v"(voff), "s"(rs), "s"(soff), "n"(OFF) : "memory");
}
template <int OFF> DI void ald16(v4u& d, rsrc_t rs, unsigned voff, unsigned soff) {
  asm volatile("buffer_load_dwordx4 %0, %1, %2, %3 offen offset:%4 sc1" : "=&v"(d) : "v"(voff), "s"(rs), "s"(soff), "n"(OFF) : "memory");
}
DI v4u ld16(rsrc_t rs, unsigned voff, unsigned soff) { return __builtin_amdgcn_raw_buffer_load_b128(rs, voff, soff, 16); }
DI v2u ld8(rsrc_t rs, unsigned voff, unsigned soff) { return __builtin_amdgcn_raw_buffer_load_b64(rs, voff, soff, 16); }
DI void st16(v4u v, rsrc_t rs, unsigned voff, unsigned soff) { __builtin_amdgcn_raw_buffer_store_b128(v, rs, voff, soff, 16); }
DI v4u lds16(unsigned a) { return *(LAS const v4u*)(size_t)a; }
DI v2u ldtr(unsigned a) { return __builtin_bit_cast(v2u, __builtin_amdgcn_ds_read_tr16_b64_v4i16((LAS s4v*)(size_t)a)); }
DI unsigned max4(v4u v, unsigned m) { return max(max(max(max(m, v[0]), v[1]), v[2]), v[3]); }
#define BAR() asm volatile("s_waitcnt lgkmcnt(0)\n\ts_barrier" ::: "memory")
#define VMW(n) asm volatile("s_waitcnt vmcnt(" #n ")" ::: "memory")
#define SB() __builtin_amdgcn_sched_barrier(0)
#define MEMB() asm volatile("" ::: "memory")
#define PLO() __builtin_amdgcn_s_setprio(0)
#define PHI() __builtin_amdgcn_s_setprio(2)

constexpr float SCALE_B = 1.0f / 16.0f;
constexpr float SCALE_Y = 512.0f / (917504.0f * SCALE_B);

DI unsigned glu2(float g0, float g1, float u0, float u1, float rs, float rl) {
  const float s0 = __builtin_amdgcn_rcpf(1.0f + __builtin_amdgcn_exp2f(g0 * rl));
  const float s1 = __builtin_amdgcn_rcpf(1.0f + __builtin_amdgcn_exp2f(g1 * rl));
  return pkb(s0 * (u0 * rs), s1 * (u1 * rs));
}
DI void ald4(unsigned& d, rsrc_t rs, unsigned voff, unsigned soff) {
  asm volatile("buffer_load_dword %0, %1, %2, %3 offen sc1" : "=&v"(d) : "v"(voff), "s"(rs), "s"(soff) : "memory");
}
DI void touch(unsigned& d, rsrc_t rs, unsigned voff, unsigned soff) {
  asm volatile("buffer_load_dword %0, %1, %2, %3 offen sc1" : "+v"(d) : "v"(voff), "s"(rs), "s"(soff) : "memory");
}
DI float sum2(unsigned u, float c) {
  const b2 one2 = {(__bf16)1.0f, (__bf16)1.0f};
  return __builtin_amdgcn_fdot2_f32_bf16(__builtin_bit_cast(b2, u), one2, c, false);
}
DI void ast16(v4u v, rsrc_t rs, unsigned voff, unsigned soff) {
  asm volatile("buffer_store_dwordx4 %0, %1, %2, %3 offen sc1" ::"v"(v), "v"(voff), "s"(rs), "s"(soff) : "memory");
}
DI float trunc16(float v) { return bhi(__builtin_bit_cast(unsigned, v)); }
DI unsigned hi16(float a, float b) { return __builtin_amdgcn_perm(__builtin_bit_cast(unsigned, b), __builtin_bit_cast(unsigned, a), 0x07060302u); }

template <bool SPARE>
DI void body(const uint16_t* __restrict__ x, const uint16_t* __restrict__ wr, const uint16_t* __restrict__ wi,
             const uint16_t* __restrict__ wg, const uint16_t* __restrict__ wu, const uint16_t* __restrict__ wd,
             unsigned char* __restrict__ ws, uint16_t* __restrict__ out, const unsigned L0) {
  STAMP(0)
  const int b = blockIdx.x, tid = threadIdx.x, lane = tid & 63;
  const int w = __builtin_amdgcn_readfirstlane(tid >> 6);
  const int r = lane & 15, q4 = lane >> 4;
  const int s2 = (b & 7) + 8 * (b >> 4), q = (b >> 3) & 1;
  const int j1b = b & 15, tileb = b >> 4;
  const int yb = b >> 3;
  const int cel = 28 * (b & 3) + yb, mG = 8 * ((b >> 2) & 1) + (cel & 7), nD = cel >> 3;
  const int mE = 2 * (b & 7) + (yb & 1), pE = (yb >> 1) & 1, nE = yb >> 2;
  const int sbk = 4 * (b & 7) + (yb & 3);
  const rsrc_t rx = mkrs(x, S_ * D_ * 2), rfx = mkrs(ws, FIX_BYTES);
  PHI();

  v2u xa[4];
  unsigned ep;
  const int l32 = lane & 31;
  {
    const int lc = l32 < 28 ? l32 : 27;
    const unsigned rowo = (unsigned)(128 * (2 * w + (lane >> 5)) + s2) * 896u;
    const unsigned o1 = rowo + 8 * lc, o2 = rowo + 440 - 8 * lc;
    asm volatile("buffer_load_dwordx2 %0, %5, %7, 0 offen\n\t"
                 "buffer_load_dwordx2 %1, %6, %7, 0 offen\n\t"
                 "buffer_load_dwordx2 %2, %5, %7, 0 offen offset:448\n\t"
                 "buffer_load_dwordx2 %3, %6, %7, 0 offen offset:448\n\t"
                 "s_load_dword %4, %8, 0"
                 : "=&v"(xa[0]), "=&v"(xa[1]), "=&v"(xa[2]), "=&v"(xa[3]), "=&s"(ep)
                 : "v"(o1), "v"(o2), "s"(rx), "s"(ws + EP_OFF)
                 : "memory");
  }
  PLO();
  const v4u sent4 = {SENT, SENT, SENT, SENT};
  unsigned junk;
  {
    const int i5 = (lane * 52) >> 8, l5 = lane - 5 * i5;
    const unsigned tf = lane < 40 ? (unsigned)((((b & 7) + 8 * (8 * yb + i5)) * 450 + (l5 < 4 ? 128 * l5 : 446)) & ~3) : OOB;
    const unsigned td = lane < 49 ? (unsigned)(49 * b + lane) * 128 : OOB;
    const uint16_t* pc;
    int clen;
    asm volatile("s_getpc_b64 %0\n.Lcs_%2:\n\ts_mov_b32 %1, .Lce_%2-.Lcs_%2\n\ts_max_i32 %1, %1, .Lce_1-.Lcs_%2"
                 : "=s"(pc), "=s"(clen) : "n"(SPARE ? 1 : 0) : "scc");
    clen = (SPARE || yb < CPF) ? max(clen, 0) : 0;
    ald4(junk, mkrs(w == 0 ? wr : w == 1 ? wi : w == 2 ? wd : pc, w < 2 ? S_ * DF_ * 2 : w == 2 ? F_ * D_ * 2 : clen),
         w < 2 ? tf : w == 2 ? td : (unsigned)(tid - 192) * 128, 0);
    ast16(sent4, rfx, tid * 16, U_OFF + (2 * s2 + q) * 8192);
    if (SPARE) ast16(sent4, rfx, lane < 8 ? (unsigned)(w * 8 + lane) * 16 : OOB, SS_OFF + sbk * 1024);
  }
  const int wmat = w & 1, wrange = w >> 1;
  const rsrc_t rw = mkrs(wmat ? wu : wg, D_ * F_ * 2);
  const unsigned wvoff = (unsigned)(((wrange >> 1) ? 3 - q4 : q4) * 3584 + ((r ^ (2 * (q4 + 4 * wmat))) * 16));
  const unsigned wsoff0 = (unsigned)((224 * (wrange & 1) + ((wrange >> 1) ? 220 : 0)) * 3584 + nD * 256);
  const int wstep = (wrange >> 1) ? -4 * 3584 : 4 * 3584;
  int rot = 64 + ((nD == 3 || nD == 10) ? 7 * (cel & 3) + 3 * ((cel >> 2) & 1) : (7 * (cel & 7)) >> 1) + ROTX * ((b >> 2) & 1);
  asm volatile("" : "+s"(rot));
  const unsigned clr0 = LH_OFF + (unsigned)sbk * LH_STR;
#define KROT(j) ((j) + rot >= 92 ? (j) + rot - 92 : (j) + rot - 64)
#define W_DMA(n0, n1)                                                                                  \
  _Pragma("unroll") for (int n_ = (n0); n_ < (n1); ++n_) {                                             \
    if (SPARE) ast16(sent4, rfx, tid * 16, clr0 + n_ * 8192);                                          \
    else dma(rw, wvoff, wsoff0 + KROT(n_) * wstep, L0 + (wslot(n_) * 8 + w) * 1024);                   \
  }
#define FLAG(i) if (SPARE && tid == 0) __builtin_amdgcn_raw_buffer_store_b32(tag, rfx, sbk * 16, FL_OFF + (i) * 512, 16);
  const int par = w >> 2, w3 = w & 3;
  const int kb = 16 * tileb + r;
  const int fc0 = tileb < 14 ? 16 * tileb : 209;
  SB();
  v4u bc[4], bs[4], mre, mim;
  {
    const int k = 2 * (16 * w + 8 * ((r >> 1) & 1) + 2 * (r >> 2) + (r & 1)) + q;
    const float kf = (float)k * (1.0f / 896.0f);
    const f32x2 st = cis(-2.0f * kf), rk = cis(-64.0f * kf);
    const float c2 = 2.0f * st.x;
    f32x2 z0 = cis((float)(-16 * q4 - 1) * kf), z1 = crot(z0, st);
#pragma unroll
    for (int kc = 0; kc < 4; ++kc) {
      f32x2 cs[8];
      cs[0] = z0; cs[1] = z1;
      if (kc < 3) { z0 = crot(z0, rk); z1 = crot(z1, rk); }
#pragma unroll
      for (int e = 2; e < 8; ++e) cs[e] = cheb(c2, cs[e - 1], cs[e - 2]);
#pragma unroll
      for (int j = 0; j < 4; ++j) { bc[kc][j] = pkh(cs[2 * j].x, cs[2 * j + 1].x); bs[kc][j] = pkh(cs[2 * j].y, cs[2 * j + 1].y); }
    }
    f32x2 m = cis((float)(r * (512 * q4 + s2)) * (1.0f / 2048.0f));
    const f32x2 ms = cis((float)r * (1.0f / 16.0f));
#pragma unroll
    for (int i = 0; i < 4; ++i) {
      mre[i] = pkh(m.x, m.y); mim[i] = pkh(-m.y, m.x);
      if (i < 3) m = crot(m, ms);
    }
#pragma unroll
    for (int kc = 0; kc < 4; ++kc) asm volatile("" : "+v"(bc[kc]), "+v"(bs[kc]));
    asm volatile("" : "+v"(mre), "+v"(mim));
  }
  W_DMA(0, 4)
  PHI();
  STAMP(1)
  asm volatile("s_waitcnt vmcnt(6) lgkmcnt(0)" : "+v"(xa[0]), "+v"(xa[1]), "+v"(xa[2]), "+v"(xa[3]), "+s"(ep)::"memory");
  const unsigned tag = ep * 1664525u + 1013904223u;
  const rsrc_t rws = mkrs(ws + FIX_BYTES + (size_t)(tag & (NROT - 1)) * REG_BYTES, REG_BYTES);
  v4u fraw;
  {
    const unsigned c0 = __builtin_amdgcn_readlane(xa[0].x, 0), c1 = __builtin_amdgcn_readlane(xa[0].x, 32);
    const float cc = 2.0f * blo(lane < 32 ? c0 : c1);
    float s1[4], sb[4], d1[4], d2[4];
    float sum = 0.f, sq = 0.f;
#pragma unroll
    for (int i = 0; i < 4; ++i) {
      const unsigned ua = xa[0][i >> 1], ub = xa[1][1 - (i >> 1)], ue = xa[2][i >> 1], uf = xa[3][1 - (i >> 1)];
      const float a = (i & 1) ? bhi(ua) : blo(ua), e = (i & 1) ? bhi(ue) : blo(ue);
      const float bb = (i & 1) ? blo(ub) : bhi(ub), f = (i & 1) ? blo(uf) : bhi(uf);
      s1[i] = (a + f) - cc; sb[i] = (bb + e) - cc; d1[i] = a - f; d2[i] = e - bb;
      sum += s1[i] + sb[i];
      sq = __builtin_fmaf(s1[i], s1[i], sq); sq = __builtin_fmaf(sb[i], sb[i], sq);
      sq = __builtin_fmaf(d1[i], d1[i], sq); sq = __builtin_fmaf(d2[i], d2[i], sq);
    }
    if (l32 >= 28) { sum = 0.f; sq = 0.f; }
    asm volatile("" : "+v"(sum), "+v"(sq));
    W_DMA(4, 5)
    sum = sum32(sum); sq = sum32(sq);
    asm volatile("" : "+v"(sum), "+v"(sq));
    W_DMA(5, 6)
    const float mu = sum * (1.0f / 448.0f);
    const float var = __builtin_fmaxf(__builtin_fmaf(-mu, mu, sq * (0.5f / 448.0f)), 0.f);
    float rs = __builtin_amdgcn_rsqf(var + 1e-5f);
    if (l32 >= 28) rs = 0.f;
    const float sgn = q ? -1.0f : 1.0f;
    const float off = q ? 0.f : -4.0f * mu * rs;
    float P[4], Q[4];
#pragma unroll
    for (int i = 0; i < 4; ++i) {
      P[i] = __builtin_fmaf(__builtin_fmaf(sgn, sb[i], s1[i]), rs, off);
      Q[i] = __builtin_fmaf(sgn, d2[i], d1[i]) * rs;
    }
    const int c = l32 >> 1, row = 2 * w + (lane >> 5);
    const unsigned o = L0 + L_A + (c * 16 + (row ^ (c & 7))) * 16 + (l32 & 1) * 8;
    *(LAS v2u*)(size_t)o = v2u{pkh(P[0], P[1]), pkh(P[2], P[3])};
    *(LAS v2u*)(size_t)(o + 4096) = v2u{pkh(Q[0], Q[1]), pkh(Q[2], Q[3])};
  }
  W_DMA(6, 7)
  {
    const rsrc_t rf = mkrs((w & 1) ? wi : wr, S_ * DF_ * 2);
    const unsigned fo = (unsigned)(((j1b + 16 * (32 * (w >> 1) + (lane >> 1))) * DF_ + fc0 + 8 * (lane & 1)) * 2);
    asm volatile("buffer_load_dwordx4 %0, %1, %2, 0 offen" : "=&v"(fraw) : "v"(fo), "s"(rf) : "memory");
  }
  VMW(8);
  BAR();
  STAMPF(3)
  {
    f32x4 zre = {0, 0, 0, 0}, zim = {0, 0, 0, 0};
#pragma unroll
    for (int kc = 0; kc < 4; ++kc) {
      const int c = 4 * kc + q4;
      const unsigned o = L0 + L_A + (c * 16 + (r ^ (c & 7))) * 16;
      zre = mfma16h(lds16(o), bc[kc], zre);
      zim = mfma16h(lds16(o + 4096), bs[kc], zim);
    }
    v4u zop;
#pragma unroll
    for (int i = 0; i < 4; ++i) zop[i] = pkh(zre[i], zim[i]);
    const f32x4 z4 = {0, 0, 0, 0};
    const f32x4 tre = mfma16h(zop, mre, z4), tim = mfma16h(zop, mim, z4);
    const unsigned to = (unsigned)(r * 16 * 128 * 128 + q4 * 16);
    const unsigned tso = (unsigned)((2 * w * 128 + s2) * 128 + q * 64);
    st16(v4u{pkh(tre[0], tim[0]), tag, pkh(tre[1], tim[1]), tag}, rws, to, tso);
    st16(v4u{pkh(tre[2], tim[2]), tag, pkh(tre[3], tim[3]), tag}, rws, w == 7 ? OOB : to, tso + 128 * 128);
  }
  STAMPF(4)
  MEMB();
  PLO();
  const int arow = 16 * w3 + 4 * (r >> 2) + 2 * (r & 1) + 64 * ((r >> 1) & 1) + par;
  v4u fre[4], fim[4];
  {
    const float af = (float)arow * (1.0f / 128.0f);
    const f32x2 s1 = cis(af), s16 = cis(16.0f * af);
    f32x2 z[4][4];
    z[0][0] = cis((float)(4 * q4) * af);
    z[0][1] = crot(z[0][0], s1);
    z[0][2] = cheb(2.0f * s1.x, z[0][1], z[0][0]);
    z[0][3] = cheb(2.0f * s1.x, z[0][2], z[0][1]);
#pragma unroll
    for (int e = 0; e < 4; ++e) {
      z[1][e] = crot(z[0][e], s16);
      z[2][e] = cheb(2.0f * s16.x, z[1][e], z[0][e]);
      z[3][e] = cheb(2.0f * s16.x, z[2][e], z[1][e]);
    }
#pragma unroll
    for (int g = 0; g < 4; ++g)
#pragma unroll
      for (int i = 0; i < 4; ++i) {
        const f32x2 v = z[g][2 * (i & 1) + (i >> 1)];
        fre[g][i] = pkh(v.x, v.y); fim[g][i] = pkh(-v.y, v.x);
      }
#pragma unroll
    for (int g = 0; g < 4; ++g) asm volatile("" : "+v"(fre[g]), "+v"(fim[g]));
  }
  v4u wf[28];
  float cs0 = 0.f, cs1 = 0.f;
  const unsigned trb = L0 + (unsigned)((((lane >> 5) * 4 + ((lane >> 4) & 1)) * 1024) + ((lane >> 2) & 3) * 256 +
                                       (((2 * w + ((lane >> 1) & 1)) ^ (2 * (((lane >> 2) & 3) + 4 * ((lane >> 4) & 1)))) * 16) + (lane & 1) * 8);
#define W_READ(n0, n1)                                                                   \
  if (!SPARE) {                                                                          \
    unsigned t0 = trb, t1 = trb + 65536, t2 = trb + 131072;                              \
    asm volatile("" : "+v"(t0), "+v"(t1), "+v"(t2));                                     \
    _Pragma("unroll") for (int n_ = (n0); n_ < (n1); ++n_) {                             \
      const int sl = wslot(n_);                                                          \
      const unsigned a = (sl < 8 ? t0 : sl < 16 ? t1 : t2) + (sl & 7) * 8192;            \
      const v2u lo = ldtr(a), hi = ldtr(a + 2048);                                       \
      wf[n_] = v4u{lo.x, lo.y, hi.x, hi.y};                                              \
    }                                                                                    \
    _Pragma("unroll") for (int n_ = (n0); n_ < (n1); ++n_) asm volatile("" : "+v"(wf[n_])); \
  }
#define W_SUM(n0, n1)                                                                    \
  if (!SPARE) {                                                                          \
    _Pragma("unroll") for (int n_ = (n0); n_ < (n1); ++n_) {                             \
      cs0 = sum2(wf[n_][0], cs0); cs1 = sum2(wf[n_][1], cs1);                            \
      cs0 = sum2(wf[n_][2], cs0); cs1 = sum2(wf[n_][3], cs1);                            \
    }                                                                                    \
    asm volatile("" : "+v"(cs0), "+v"(cs1));                                             \
  }
  float fa_[4], fb_[4];
  asm volatile("s_waitcnt vmcnt(2)" : "+v"(fraw), "+v"(junk)::"memory");
  *(LAS v4u*)(size_t)(L0 + L_F + (w & 1) * 4096 + (w >> 1) * 1024 + lane * 16) = fraw;
  BAR();
  FLAG(0)
  W_READ(0, 7)
  {
    const unsigned fa = L0 + L_F + (unsigned)((16 * w3 + 4 * q4 + par) * 32 + ((kb <= 224 ? kb : 224) - fc0) * 2);
    float sc = SCALE_B * 0.125f;
    if (kb == 0 || kb == 224) sc = 0.5f * SCALE_B * 0.125f;
    if (kb > 224) sc = 0.f;
#pragma unroll
    for (int i = 0; i < 4; ++i) {
      fa_[i] = blo(*(LAS const unsigned short*)(size_t)(fa + (2 * (i & 1) + 64 * (i >> 1)) * 32)) * sc;
      fb_[i] = blo(*(LAS const unsigned short*)(size_t)(fa + (2 * (i & 1) + 64 * (i >> 1)) * 32 + 4096)) * sc;
    }
#pragma unroll
    for (int i = 0; i < 4; ++i) asm volatile("" : "+v"(fa_[i]), "+v"(fb_[i]));
  }
  BAR();
  STAMPF(5)
  if (DLY_T) __builtin_amdgcn_s_sleep(DLY_T);
  const int dt = w < 7 ? w : 6;
  const int srow = 128 * r + s2;
  v4u ce[4], co[4], se[4], so[4];
  {
    const unsigned tso = (unsigned)((j1b * 16 + tileb) * 16384 + w * 1024);
    const unsigned tvo = tileb < 15 ? (unsigned)lane * 16 : OOB;
    v4u g0, g1;
    PHI();
    ald16<0>(g0, rws, tvo, tso); ald16<0>(g1, rws, tvo, tso + 8192);
    PLO();
    SB();
    {
      f32x2 m = 0.25f * cis((float)(4 * q4 * srow) * (1.0f / 2048.0f));
      const f32x2 ms = cis((float)srow * (1.0f / 2048.0f));
#pragma unroll
      for (int i = 0; i < 4; ++i) {
        mre[i] = pkh(m.x, -m.y); mim[i] = pkh(m.y, m.x);
        if (i < 3) m = crot(m, ms);
      }
      const float df = (float)(2 * (16 * dt + r) + 1) * (1.0f / 896.0f);
      const f32x2 st = cis(df), rk = cis(64.0f * df);
      const float c2 = 2.0f * st.x;
      f32x2 z0 = cis((float)(16 * q4) * df), z1 = crot(z0, st);
#pragma unroll
      for (int kc = 0; kc < 4; ++kc) {
        W_DMA(7 + kc, 8 + kc)
        f32x2 cs[16];
        cs[0] = z0; cs[1] = z1;
        if (kc < 3) { z0 = crot(z0, rk); z1 = crot(z1, rk); }
#pragma unroll
        for (int e = 2; e < 16; ++e) cs[e] = cheb(c2, cs[e - 1], cs[e - 2]);
#pragma unroll
        for (int j = 0; j < 4; ++j) {
          ce[kc][j] = pkh(cs[4 * j].x, cs[4 * j + 2].x); co[kc][j] = pkh(cs[4 * j + 1].x, cs[4 * j + 3].x);
          se[kc][j] = pkh(cs[4 * j].y, cs[4 * j + 2].y); so[kc][j] = pkh(cs[4 * j + 1].y, cs[4 * j + 3].y);
        }
        asm volatile("" : "+v"(ce[kc]), "+v"(co[kc]), "+v"(se[kc]), "+v"(so[kc]));
        SB();
      }
      asm volatile("" : "+v"(mre), "+v"(mim));
    }
    STAMPF(6)
    PHI();
    asm volatile("s_waitcnt vmcnt(4)" : "+v"(g0), "+v"(g1)::"memory");
    int spins = tileb < 15 ? 0 : SPIN_MAX;
    while (!__all(g0.y == tag && g0.w == tag && g1.y == tag && g1.w == tag) && ++spins <= SPIN_MAX) {
      const bool k0 = g0.y == tag && g0.w == tag, k1 = g1.y == tag && g1.w == tag;
      v4u n0, n1;
      if (TSLP) __builtin_amdgcn_s_sleep(TSLP);
      ald16<0>(n0, rws, k0 ? OOB : tvo, tso); ald16<0>(n1, rws, k1 ? OOB : tvo, tso + 8192);
      asm volatile("s_waitcnt vmcnt(0)" : "+v"(n0), "+v"(n1)::"memory");
      if (!k0) g0 = n0;
      if (!k1) g1 = n1;
    }
    CNT(20, tileb < 15 ? spins : 0)
    const int m = 8 * w + (lane >> 3);
    const unsigned o = L0 + L_BT + (unsigned)(((m >> 4) * 64 + ((m >> 2) & 3) * 16 + 4 * (lane & 3) + ((lane >> 2) & 1)) * 16 + (((m >> 1) & 1) + 2 * (m & 1)) * 4);
    *(LAS unsigned*)(size_t)o = hadd(g0.x, g1.x);
    *(LAS unsigned*)(size_t)(o + 32) = hadd(g0.z, g1.z);
    *(LAS unsigned*)(size_t)(o + 4096) = hsub(g0.x, g1.x);
    *(LAS unsigned*)(size_t)(o + 4096 + 32) = hsub(g0.z, g1.z);
    W_DMA(11, 13)
    STAMP(7)
    BAR();
    f32x4 vre = {0, 0, 0, 0}, vim = {0, 0, 0, 0};
    {
      v4u tf[4];
#pragma unroll
      for (int g = 0; g < 4; ++g) tf[g] = lds16(L0 + L_BT + par * 4096 + g * 1024 + lane * 16);
      W_DMA(13, 14)
#pragma unroll
      for (int g = 0; g < 4; ++g) { vre = mfma16h(fre[g], tf[g], vre); vim = mfma16h(fim[g], tf[g], vim); }
    }
    W_DMA(14, 15)
    float pr[4], pi[4];
#pragma unroll
    for (int i = 0; i < 4; ++i) { pr[i] = vre[i] * fa_[i] - vim[i] * fb_[i]; pi[i] = -(vre[i] * fb_[i] + vim[i] * fa_[i]); }
    const unsigned po = L0 + L_BP + w3 * 1024 + lane * 16 + par * 8;
    *(LAS v2u*)(size_t)po = v2u{pkh(pr[0] + pr[2], pi[0] + pi[2]), pkh(pr[1] + pr[3], pi[1] + pi[3])};
    *(LAS v2u*)(size_t)(po + 4096) = v2u{pkh(pr[0] - pr[2], pi[0] - pi[2]), pkh(pr[1] - pr[3], pi[1] - pi[3])};
    W_DMA(15, 17)
    BAR();
    f32x4 ure = {0, 0, 0, 0}, uim = {0, 0, 0, 0};
    {
      v4u pf[4];
#pragma unroll
      for (int g = 0; g < 4; ++g) pf[g] = lds16(L0 + L_BP + par * 4096 + g * 1024 + lane * 16);
      W_DMA(17, 18)
#pragma unroll
      for (int g = 0; g < 4; ++g) { ure = mfma16h(pf[g], fre[g], ure); uim = mfma16h(pf[g], fim[g], uim); }
    }
    const unsigned uo = tileb < 15 ? (unsigned)(arow * 16384 + q4 * 16) : OOB, uso = U_OFF + (unsigned)((tileb * 16 + j1b) * 64);
    st16(v4u{pkh(ure[0] * 0.125f, uim[0] * -0.125f), pkh(ure[1] * 0.125f, uim[1] * -0.125f), pkh(ure[2] * 0.125f, uim[2] * -0.125f),
             pkh(ure[3] * 0.125f, uim[3] * -0.125f)}, rfx, uo, uso);
  }
  STAMP(8)
  MEMB();
  PLO();
  W_DMA(18, 23)
  v4u xv0, xv1;
  {
    const int lc = l32 < 28 ? l32 : 27, row = 2 * w + (lane >> 5);
    const unsigned o = L0 + L_X + (unsigned)((lc * 16 + (row ^ (lc & 15))) * 16);
    *(LAS v4u*)(size_t)o = v4u{xa[0].x, xa[0].y, xa[2].x, xa[2].y};
    *(LAS v4u*)(size_t)(o + 7168) = v4u{xa[1].x, xa[1].y, xa[3].x, xa[3].y};
    VMW(6);
    BAR();
    if (b == 0 && tid == 0) __builtin_amdgcn_raw_buffer_store_b32(tag, rfx, 0, EP_OFF, 16);
    W_READ(7, 18)
    const int lr = 4 * dt + q4;
    const unsigned o2 = L0 + L_X + (unsigned)((lr * 16 + (r ^ (lr & 15))) * 16);
    xv0 = lds16(o2); xv1 = lds16(o2 + 7168);
    asm volatile("" : "+v"(xv0), "+v"(xv1));
    BAR();
  }
  W_DMA(23, 28)
  const rsrc_t rd = mkrs(wd, F_ * D_ * 2);
  const unsigned dvoff = (unsigned)((lane >> 3) * 896 + (((lane & 7) ^ (4 * ((lane >> 4) & 1))) * 16));
  const unsigned dsoff = (unsigned)(224 * w * 896 + nE * 128);
  int erot = 64 + (nE + 2 * (yb & 3)) % 7;
  asm volatile("" : "+s"(erot));
#define ER(i) ((i) + erot >= 71 ? (i) + erot - 71 : (i) + erot - 64)
#define WD_DMA1(j)                                                                                     \
  {                                                                                                    \
    if (SPARE) { if ((j) < 7) ast16(sent4, rfx, tid * 16, clr0 + (28 + (j)) * 8192); }                 \
    else dma(rd, dvoff, dsoff + (32 * ER((j) >> 2) + 8 * (((j) >> 1) & 1) + 16 * ((j) & 1)) * 896, L0 + (dslot(j) * 8 + w) * 1024); \
  }
#define WD_DMA(j0, j1) _Pragma("unroll") for (int j_ = (j0); j_ < (j1); ++j_) WD_DMA1(j_)
  STAMP(9)
  if (DLY_U) __builtin_amdgcn_s_sleep(DLY_U);
  {
    v2u ug[4];
    unsigned fl;
    const unsigned uso = U_OFF + (unsigned)((s2 * 16 + 2 * w) * 1024);
    const unsigned uo = (w == 7 && r >= 8) ? OOB : (unsigned)((r >> 3) * 1024 + q4 * 256 + (r & 7) * 8);
#define U_LOAD()                                                                                       \
  ald4(fl, rfx, l32 * 16, FL_OFF);                                                                      \
  ald8<0>(ug[0], rfx, uo, uso); ald8<64>(ug[1], rfx, uo, uso); ald8<128>(ug[2], rfx, uo, uso); ald8<192>(ug[3], rfx, uo, uso);
    PHI();
    U_LOAD()
    PLO();
    WD_DMA(0, 2)
    W_SUM(0, 18)
    VMW(7);
    BAR();
    FLAG(1)
    W_READ(18, 28)
    STAMP(10)
    PHI();
    asm volatile("s_waitcnt vmcnt(2)" : "+v"(ug[0]), "+v"(ug[1]), "+v"(ug[2]), "+v"(ug[3]), "+v"(fl)::"memory");
    int spins = 0;
#define U_BAD (max4(v4u{ug[0].x, ug[0].y, ug[1].x, ug[1].y}, max4(v4u{ug[2].x, ug[2].y, ug[3].x, ug[3].y}, 0)) == SENT || fl != tag)
    while (__any(U_BAD) && ++spins <= SPIN_MAX) {
      U_LOAD()
      asm volatile("s_waitcnt vmcnt(0)" : "+v"(ug[0]), "+v"(ug[1]), "+v"(ug[2]), "+v"(ug[3]), "+v"(fl)::"memory");
    }
    CNT(21, spins)
    STAMP(11)
    const f32x4 z4 = {0, 0, 0, 0};
#pragma unroll
    for (int u = 0; u < 2; ++u) {
      const v4u uf = {ug[0][u], ug[1][u], ug[2][u], ug[3][u]};
      const f32x4 yre = mfma16h(uf, mre, z4), yim = mfma16h(uf, mim, z4);
      const int c = 2 * w + (q4 >> 1);
      const unsigned o = L0 + L_C + u * 8192 + (c * 16 + (r ^ (c & 7))) * 16 + (q4 & 1) * 8;
      *(LAS v2u*)(size_t)o = v2u{pkh(yre[0], yre[1]), pkh(yre[2], yre[3])};
      *(LAS v2u*)(size_t)(o + 4096) = v2u{pkh(yim[0], yim[1]), pkh(yim[2], yim[3])};
    }
    BAR();
    WD_DMA(2, 4)
    f32x4 Ce = z4, Co = z4, Se = z4, So = z4;
#pragma unroll
    for (int kc = 0; kc < 4; ++kc) {
      const int c = 4 * kc + q4;
      const unsigned o = L0 + L_C + (c * 16 + (r ^ (c & 7))) * 16;
      Ce = mfma16h(ce[kc], lds16(o), Ce);
      Se = mfma16h(se[kc], lds16(o + 4096), Se);
      Co = mfma16h(co[kc], lds16(o + 8192), Co);
      So = mfma16h(so[kc], lds16(o + 12288), So);
    }
    v4u hq[2];
#pragma unroll
    for (int g = 0; g < 2; ++g) {
      const int gg = g ^ q;
      const float sg = gg ? 1.0f : -1.0f;
      const v4u xs = gg ? v4u{__builtin_amdgcn_alignbit(xv1.y, xv1.y, 16), __builtin_amdgcn_alignbit(xv1.x, xv1.x, 16),
                              __builtin_amdgcn_alignbit(xv1.w, xv1.w, 16), __builtin_amdgcn_alignbit(xv1.z, xv1.z, 16)} : xv0;
      float lo[4], hi[4];
#pragma unroll
      for (int i = 0; i < 4; ++i) {
        const float a = __builtin_fmaf(sg, Se[i], Ce[i]), bb = __builtin_fmaf(sg, Co[i], So[i]);
        const float x0 = (i & 1) ? bhi(xs[i >> 1]) : blo(xs[i >> 1]), x1 = (i & 1) ? bhi(xs[2 + (i >> 1)]) : blo(xs[2 + (i >> 1)]);
        lo[i] = __builtin_fmaf(a - bb, SCALE_Y, x0); hi[i] = __builtin_fmaf(a + bb, SCALE_Y, x1);
      }
      hq[g] = v4u{pkb(lo[0], lo[1]), pkb(lo[2], lo[3]), pkb(hi[0], hi[1]), pkb(hi[2], hi[3])};
      if (g == 0) {
        const unsigned lo_ = w < 7 ? (unsigned)((q4 * 64 + r) * 16) : OOB;
        const unsigned lso = LH_OFF + (unsigned)((s2 >> 2) * LH_STR + ((((s2 >> 1) & 1) * 28 + 4 * dt) * 64 + 16 * (s2 & 1) + 32 * q) * 16);
        st16(hq[0], rfx, lo_, lso);
        MEMB();
        STAMP(12)
        SB();
      }
    }
    PLO();
    WD_DMA(4, 8)
    float sum = 0.f;
#pragma unroll
    for (int g = 0; g < 2; ++g)
#pragma unroll
      for (int i = 0; i < 4; ++i) sum += blo(hq[g][i]) + bhi(hq[g][i]);
    sum = xor32_add(xor16_add(sum));
    const float lm = sum * (1.0f / 64.0f);
    float m2 = 0.f;
#pragma unroll
    for (int g = 0; g < 2; ++g)
#pragma unroll
      for (int i = 0; i < 4; ++i) {
        const float d0_ = blo(hq[g][i]) - lm, d1_ = bhi(hq[g][i]) - lm;
        m2 = __builtin_fmaf(d0_, d0_, m2); m2 = __builtin_fmaf(d1_, d1_, m2);
      }
    m2 = xor32_add(xor16_add(m2));
    if (q4 == 0) *(LAS f32x2*)(size_t)(L0 + L_ST + (r * 8 + w) * 8) = f32x2{lm, m2};
    BAR();
    if (w == 7 && q == 0) {
      PHI();
      f32x4 sv[4];
#pragma unroll
      for (int j = 0; j < 4; ++j) sv[j] = *(LAS const f32x4*)(size_t)(L0 + L_ST + r * 64 + j * 16);
      const float mean = (((sv[0].x + sv[0].z) + (sv[1].x + sv[1].z)) + ((sv[2].x + sv[2].z) + sv[3].x)) * (1.0f / 7.0f);
      float M2 = 0.f;
#pragma unroll
      for (int j = 0; j < 7; ++j) {
        const float dlt = sv[j >> 1][2 * (j & 1)] - mean;
        M2 += __builtin_fmaf(64.0f * dlt, dlt, sv[j >> 1][2 * (j & 1) + 1]);
      }
      const float rs = __builtin_amdgcn_rsqf(M2 * (1.0f / 448.0f) + 1e-5f);
      const float m0 = -mean, m1 = m0 - trunc16(m0), m2_ = m1 - trunc16(m1);
      st16(v4u{hi16(m0, m0), hi16(m0, m1), hi16(m1, m2_), hi16(rs, rs - trunc16(rs))}, rfx, q4 == 0 ? (unsigned)r * 16 : OOB, SS_OFF + s2 * 256);
      MEMB();
      PLO();
    }
  }
  if (SPARE) {
    VMW(0);
    BAR();
    FLAG(2)
  } else {
    WD_DMA(8, 12)
    v4u wdf[7][4];
    const unsigned tdb = L0 + (unsigned)(w * 1024 + (q4 >> 1) * 8192 + (4 * (q4 & 1) + ((lane >> 2) & 3)) * 128 + ((lane >> 3) & 1) * 64 + (lane & 3) * 16);
    unsigned td0 = tdb;
#define WD_READ(u)                                                                                     \
  {                                                                                                    \
    constexpr int sl_ = dslot(2 * (u));                                                                \
    asm volatile("" : "+v"(td0));                                                                      \
    const unsigned ta_ = td0 + (sl_ & ~7) * 8192, tb_ = ta_ ^ 64;                                      \
    _Pragma("unroll") for (int nt = 0; nt < 4; ++nt) {                                                 \
      const v2u t_ = ldtr(((nt >> 1) ? tb_ : ta_) + (sl_ & 7) * 8192 + (nt & 1) * 8);                  \
      wdf[(u) >> 1][nt][2 * ((u) & 1)] = t_.x; wdf[(u) >> 1][nt][2 * ((u) & 1) + 1] = t_.y;            \
    }                                                                                                  \
  }
    v4u csf;
    W_SUM(18, 28)
    {
      const float c0_ = xor32_add(cs0 + cs1), c1_ = c0_ - trunc16(c0_), c2_ = c1_ - trunc16(c1_);
      csf = v4u{hi16(c0_, c1_), hi16(c2_, c0_), hi16(c1_, c0_), 0u};
      if (lane >= 32) csf = v4u{0u, 0u, 0u, 0u};
      asm volatile("" : "+v"(csf));
    }
    VMW(4);
    WD_READ(0) WD_READ(1) WD_READ(2) WD_READ(3)
    BAR();
    STAMP(13)
    if (DLY_L) __builtin_amdgcn_s_sleep(DLY_L);
    const unsigned lnso = LH_OFF + (unsigned)(2 * mG) * LH_STR;
    const unsigned la = L0 + lane * 16;
#define LN_DMA(i)                                                                                      \
  {                                                                                                    \
    const int pi_ = 8 * (i) + w, tt_ = pi_ / 28, j_ = pi_ - 28 * tt_;                                  \
    dma1(rfx, lane * 16, lnso + ((i) >= 7 ? LH_STR - 57344 : 0) + (28 * tt_ + KROT(j_)) * 1024, L0 + pi_ * 1024); \
  }
#define LN_MAX(i0, i1) \
  _Pragma("unroll") for (int i_ = (i0); i_ < (i1); ++i_) mx = max4(lds16((i_ < 7 ? la : la + 57344) + w * 1024 + (i_ < 7 ? i_ : i_ - 7) * 8192), mx);
#define LN_FIX(i0, i1)                                                                                 \
  if (__any(mx == SENT)) {                                                                             \
    int spins = 0;                                                                                     \
    do {                                                                                               \
      _Pragma("unroll") for (int i_ = (i0); i_ < (i1); ++i_) LN_DMA(i_)                                \
      VMW(0);                                                                                          \
      mx = 0;                                                                                          \
      LN_MAX(i0, i1)                                                                                   \
      lspins += 1 << (8 * ((i0) / 3));                                                                 \
    } while (__any(mx == SENT) && ++spins < SPIN_MAX);                                                 \
  }
    unsigned mx = 0;
    int lspins = 0;
    PHI();
#pragma unroll
    for (int i = 0; i < 4; ++i) LN_DMA(i)
    PLO();
    {
      const unsigned pb = (unsigned)(28672 + (224 * ((yb >> 3) % 3) + 28 * w + lane) * 128);
      ald4(junk, rfx, lane < 28 ? (pb >= 57344 ? pb + (LH_STR - 57344) : pb) : OOB, lnso);
    }
    VMW(1);
    LN_MAX(0, 4)
    LN_FIX(0, 4)
    BAR();
    STAMP(14)
    f32x16 acc, accp;
    v4u af[NAF], ho, stf;
    v4u hb[4][7];
    unsigned fl;
    float rsv = 0.f, rlv = 0.f;
    const unsigned hso = LH_OFF + (unsigned)((2 * mG + (nD >= 7)) * LH_STR + 57344 + (8 * (nD >= 7 ? nD - 7 : nD) + w) * 1024);
    unsigned hvo = (unsigned)(q4 * 512 + pE * 256 + r * 16);
    asm volatile("" : "+v"(hvo));
    const unsigned hlo = LH_OFF + (unsigned)((2 * mE + (w >> 2)) * LH_STR + 57344 + 14 * (w & 3) * 1024);
    const unsigned sso = SS_OFF + (unsigned)(mG * 2048);
#define H_LOAD(tt, i) hb[tt][i] = ld16(rfx, hvo, hlo + (tt) * 57344 + ER(i) * 2048);
#define H_TOUCH(tt) touch(junk, rfx, hvo, hlo + (tt) * 57344 + ER(0) * 2048);
#define LGK3() asm volatile("s_waitcnt lgkmcnt(3)" ::: "memory")
#define VMWS(n) asm volatile("s_waitcnt vmcnt(" #n ")" : "+v"(stf), "+v"(fl), "+v"(junk)::"memory")
#define FL_FIX(i)                                                                                      \
  if (__any(fl != tag)) {                                                                              \
    int spins = 0;                                                                                     \
    do {                                                                                               \
      ald4(fl, rfx, l32 * 16, FL_OFF + (i) * 512);                                                      \
      VMWS(0);                                                                                         \
      lspins += 1 << 24;                                                                               \
    } while (__any(fl != tag) && ++spins < SPIN_MAX);                                                  \
  }
    float ge = 0.f, gp = 0.f;
#define GLU_STEP(k)                                                                                    \
  {                                                                                                    \
    const int e_ = (k) >> 1;                                                                           \
    if (((k) & 1) == 0) {                                                                              \
      ge = __builtin_amdgcn_exp2f(accp[e_] * rlv);                                                     \
      asm volatile("" : "+v"(ge));                                                                     \
    } else {                                                                                           \
      const float p_ = __builtin_amdgcn_rcpf(1.0f + ge) * (accp[8 + e_] * rsv);                        \
      if (e_ & 1) { unsigned t_ = pkb(gp, p_); asm volatile("" : "+v"(t_)); ho[e_ >> 1] = t_; }        \
      else { gp = p_; asm volatile("" : "+v"(gp)); }                                                   \
    }                                                                                                  \
  }
    auto dpass = [&](auto TT) __attribute__((always_inline)) {
      constexpr int tt = decltype(TT)::value;
      unsigned lbt = la + tt * 28672;
      asm volatile("" : "+v"(lbt));
      __builtin_amdgcn_s_setprio(3);
#pragma unroll
      for (int i = 0; i < NAF - 1; ++i) af[i] = lds16(lbt + i * 1024);
#pragma unroll
      for (int i = 0; i < 16; ++i) acc[i] = 0.f;
      mx = 0;
      SB();
#pragma unroll
      for (int ks = 0; ks < 28; ++ks) {
        if (ks + NAF - 1 < 28) af[(ks + NAF - 1) % NAF] = lds16(lbt + (ks + NAF - 1) * 1024);
        acc = mfma32b(wf[ks], af[ks % NAF], acc);
        SB();
        if (ks == (tt ? 9 : 0)) ald16<tt * 512>(stf, rfx, l32 * 16, sso);
        if (tt > 0) {
          if (ks < 16) GLU_STEP(ks)
          if (ks == 16) { st16(ho, rfx, lane * 16, hso + (tt - 1) * 57344); MEMB(); }
        }
        if (ks == 9) __builtin_amdgcn_s_setprio(2);
        if (ks == 19 && tt < 3) __builtin_amdgcn_s_setprio(1);
        if (tt == 0) {
          if (ks == 1) ald4(fl, rfx, l32 * 16, FL_OFF + 512);
          if (ks >= 2 && ks < 22 && (ks & 1) == 0) LN_DMA(3 + (ks >> 1))
          if (ks == 21) WD_DMA1(12)
          if (ks == 22) WD_DMA1(13)
          if (ks == 4) WD_READ(4)
          if (ks == 7) WD_READ(5)
          if (ks == 24) { VMWS(9); LN_MAX(4, 7) }
        }
        if (tt == 1) {
          if (ks < 8 && (ks & 1) == 1) WD_DMA1(14 + (ks >> 1))
          if (ks == 10 || ks == 12) WD_DMA1(18 + ((ks - 10) >> 1))
          if (ks == 17) { VMW(8); WD_READ(6) }
          if (ks == 18) LN_MAX(7, 10)
          if (ks == 20) LN_MAX(10, 14)
          if (ks == 22) { LGK3(); WD_DMA1(20) }
          if (ks == 24) WD_DMA1(21)
          if (ks == 26) VMWS(5);
        }
        if (tt == 2) {
          if (ks < 8 && (ks & 1) == 0) WD_DMA1(22 + (ks >> 1))
          if (ks == 17) WD_READ(7)
          if (ks == 20) { LGK3(); WD_DMA1(26) }
          if (ks == 22) WD_DMA1(27)
          if (ks == 24) H_TOUCH(0)
          if (ks == 26) VMWS(4);
        }
        if (tt == 3) {
          if (ks == 1) H_TOUCH(1)
          if (ks >= 3 && ks <= 11 && (ks & 1) == 1) H_LOAD(0, (ks - 3) >> 1)
          if (ks == 15 || ks == 17) H_LOAD(0, (ks - 5) >> 1)
          if (ks >= 19 && ks <= 25 && (ks & 1) == 1) H_LOAD(1, (ks - 19) >> 1)
          if (ks == 9) ald4(fl, rfx, l32 * 16, FL_OFF + 1024);
          if (ks == 8) WD_READ(8)
          if (ks == 14) WD_READ(9)
          if (ks == 18) WD_READ(10)
          if (ks == 20) WD_READ(11)
          if (ks == 22) WD_READ(12)
          if (ks == 24) { VMWS(7); WD_READ(13) }
        }
        SB();
      }
      if (__any(max4(stf, 0) == SENT)) {
        int spins = 0;
        do {
          ald16<tt * 512>(stf, rfx, l32 * 16, sso);
          VMWS(0);
          lspins += 1 << 16;
        } while (__any(max4(stf, 0) == SENT) && ++spins < SPIN_MAX);
      }
      accp = mfma32b(csf, stf, acc);
      rsv = blo(stf.w) + bhi(stf.w);
      rlv = rsv * -1.44269504f;
      if (tt == 0) { LN_FIX(4, 7) FL_FIX(1) }
      if (tt == 1) LN_FIX(7, 14)
      if (tt == 3) FL_FIX(2)
      if (tt < 3) BAR();
      if (tt == 0) STAMP(15)
      if (tt == 2) STAMP(2)
    };
    dpass(IC<0>{}); dpass(IC<1>{}); dpass(IC<2>{}); dpass(IC<3>{});
    CNT(22, lspins)
    STAMP(16)
    int espins = 0;
    f32x4 ea[2][4];
    unsigned ln2 = la;
    asm volatile("" : "+v"(ln2));
    ln2 = (ln2 - L0) >> 4;
    unsigned hro, sel0, pa[2], ra, rb, ovo, hra;
    int hstep;
    v2u hr0, hr1;
    f32x4 rp[8];
    const unsigned hrs = LH_OFF + (unsigned)((2 * mE + (w >> 2)) * LH_STR + ((w >> 1) & 1) * 28672 + pE * 256);
    const rsrc_t ro = mkrs(out, S_ * D_ * 2);
#define HR_LOAD() { hr0 = ld8(rfx, hro, hrs); hr1 = ld8(rfx, hro + hstep, hrs); }
#define H_FIX(tt)                                                                                      \
  {                                                                                                    \
    int spins = 0;                                                                                     \
    do {                                                                                               \
      MEMB();                                                                                          \
      _Pragma("unroll") for (int i = 0; i < 7; ++i) H_LOAD(tt, i)                                      \
      mx = 0;                                                                                          \
      _Pragma("unroll") for (int i = 0; i < 7; ++i) mx = max4(hb[tt][i], mx);                          \
      espins += 1 << (8 * (tt));                                                                       \
    } while (__any(mx == SENT) && ++spins < SPIN_MAX);                                                 \
  }
#define E_ZERO(tt) _Pragma("unroll") for (int nt = 0; nt < 4; ++nt) ea[(tt) & 1][nt] = f32x4{0, 0, 0, 0};
#define E_MM(tt, k) ea[(tt) & 1][(k) & 3] = mfma16b(wdf[(k) >> 2][(k) & 3], hb[tt][(k) >> 2], ea[(tt) & 1][(k) & 3]);
#define E_STEP(tt, k) E_MM(tt, k)
#define E_END(tt)                                                                                      \
  if (__any(__builtin_isnan(ea[(tt) & 1][0][0]))) {                                                    \
    H_FIX(tt) E_ZERO(tt)                                                                               \
    _Pragma("unroll") for (int k = 0; k < 28; ++k) E_MM(tt, k)                                         \
  }
#define E_PUT(tt, nt)                                                                                  \
  {                                                                                                    \
    const int sl_ = pslot(tt, nt);                                                                     \
    *(LAS f32x4*)(size_t)((pa[(nt) & 1] + (sl_ & ~7) * 8192) + (sl_ & 7) * 8192) = ea[(tt) & 1][nt];         \
  }
#define R_READ(base, ww) rp[ww] = *(LAS const f32x4*)(size_t)((base) + (ww) * 1024);
#define R_FIN(h_, so)                                                                                  \
  {                                                                                                    \
    const f32x4 s_ = ((rp[0] + rp[1]) + (rp[2] + rp[3])) + ((rp[4] + rp[5]) + (rp[6] + rp[7]));        \
    const unsigned f0 = pkb(s_[0], s_[1]), f1 = pkb(s_[2], s_[3]);                                     \
    __builtin_amdgcn_raw_buffer_store_b64(v2u{pkb(blo(h_.x) + blo(f0), bhi(h_.x) + bhi(f0)), pkb(blo(h_.y) + blo(f1), bhi(h_.y) + bhi(f1))}, \
                                          ro, ovo, so, 16);                                            \
  }
    E_ZERO(0)
    SB();
#pragma unroll
    for (int k = 0; k < 28; ++k) {
      if (k < 16) GLU_STEP(k)
      if (k == 16) { st16(ho, rfx, lane * 16, hso + 3 * 57344); MEMB(); __builtin_amdgcn_s_setprio(1); }
      if (k < 6 && (k & 1) == 0) H_LOAD(1, 4 + (k >> 1))
      E_MM(0, k)
      if (k == 17) H_LOAD(2, 0)
      if (k == 24) {
#pragma unroll
        for (int nt = 0; nt < 2; ++nt) pa[nt] = L0 + w * 1024 + (ln2 >> 4) * 256 + ((ln2 + 2 * (ln2 >> 4) + 8 * nt) & 15) * 16;
        asm volatile("" : "+v"(pa[0]), "+v"(pa[1]));
      }
      if (k == 26) {
        const int c0 = 64 * nE + 32 * (w & 1) + 8 * (int)(ln2 >> 4);
        const int seg = ((c0 >> 4) * 37) >> 8, hi = seg >> 1, mir = seg & 1, cc = c0 - 224 * hi;
        const int ksa = mir ? (220 - cc) >> 2 : cc >> 2;
        hro = (unsigned)((ksa * 64 + 32 * mir + (ln2 & 15)) * 16 + 8 * hi);
        hstep = mir ? -1024 : 1024;
        sel0 = mir ? 0x05040706u : 0x03020100u;
        asm volatile("" : "+v"(hro), "+v"(hstep), "+v"(sel0));
      }
      SB();
    }
    E_END(0)
    STAMP(17)
    PLO();
    E_ZERO(1)
    SB();
#pragma unroll
    for (int k = 0; k < 28; ++k) {
      E_STEP(1, k)
      if (k < 24 && k % 6 == 2) E_PUT(0, (k / 6) & 3)
      if (k == 0) HR_LOAD()
      if (k >= 4 && k < 4 + 4 * H2N && (k & 3) == 0) H_LOAD(2, k >> 2)
      if (k == 23) {
        const int row = 8 * ((w >> 1) & 1) + (int)(ln2 >> 3), g = ln2 & 7, tl = w >> 2;
        const unsigned o = L0 + (unsigned)((g & 1) * 8192 + (g >> 1) * 256 + ((row + 2 * (g >> 1) + 8 * (g & 1)) & 15) * 16);
        ra = o + (unsigned)((tl ? pslot(1, 0) : pslot(0, 0)) + 2 * (w & 1)) * 8192;
        rb = o + (unsigned)(tl ? ((w & 1) ? pslot(3, 2) : pslot(3, 0)) : pslot(2, 0) + 2 * (w & 1)) * 8192;
        hra = L0 + 32768 + (unsigned)((2 * tl + (w & 1)) * 1024 + row * 64 + g * 8);
        ovo = (unsigned)(((128 * row + 8 * mE + 2 * tl + pE) * D_ + 64 * nE + 32 * (w & 1) + 4 * g) * 2);
        asm volatile("" : "+v"(ra), "+v"(rb), "+v"(hra), "+v"(ovo));
      }
      SB();
    }
    E_END(1)
    STAMPE(4)
    E_ZERO(2)
    SB();
#pragma unroll
    for (int k = 0; k < 28; ++k) {
      if (k < 2 * (6 - H2N) && (k & 1) == 0) H_LOAD(2, H2N + 1 + (k >> 1))
      if (k >= H3_AT && k < H3_AT + 7 * H3_ST && (k - H3_AT) % H3_ST == 0) H_LOAD(3, (k - H3_AT) / H3_ST)
      if (k < 24 && k % 6 == 2) E_PUT(1, (k / 6) & 3)
      if (k == 24) {
        if (__any(max(max(hr0.x, hr0.y), max(hr1.x, hr1.y)) == SENT)) {
          int spins = 0;
          do {
            MEMB();
            HR_LOAD()
          } while (__any(max(max(hr0.x, hr0.y), max(hr1.x, hr1.y)) == SENT) && ++spins < SPIN_MAX);
        }
        *(LAS v4u*)(size_t)(L0 + 32768 + w * 1024 + ((ln2 & 15) * 4 + (ln2 >> 4)) * 16) =
            v4u{__builtin_amdgcn_perm(hr0.y, hr0.x, sel0), __builtin_amdgcn_perm(hr0.y, hr0.x, sel0 ^ 0x04040404u),
                __builtin_amdgcn_perm(hr1.y, hr1.x, sel0), __builtin_amdgcn_perm(hr1.y, hr1.x, sel0 ^ 0x04040404u)};
      }
      E_STEP(2, k)
      SB();
    }
    E_END(2)
    STAMPE(5)
    BAR();
    STAMPE(6)
    E_ZERO(3)
    SB();
#pragma unroll
    for (int k = 0; k < 28; ++k) {
      if (k == 0) { hr0 = *(LAS const v2u*)(size_t)hra; hr1 = *(LAS const v2u*)(size_t)(hra + 4096); }
      if (k < 16 && (k & 1) == 0) R_READ(ra, k >> 1)
      if (k == 19) R_FIN(hr0, 0)
      if (k < 24 && k % 6 == 3) E_PUT(2, (k / 6) & 3)
      E_STEP(3, k)
      SB();
    }
    E_END(3)
    E_PUT(3, 0) E_PUT(3, 1) E_PUT(3, 2) E_PUT(3, 3)
    CNT(23, espins)
    STAMP(18)
    BAR();
#pragma unroll
    for (int ww = 0; ww < 8; ++ww) R_READ(rb, ww)
    R_FIN(hr1, 3584)
  }
  asm volatile(".Lce_%0:" ::"n"(SPARE ? 1 : 0) : "memory");
}

__global__ __launch_bounds__(512, 2) void fused(const uint16_t* __restrict__ x, const uint16_t* __restrict__ wr,
                                                const uint16_t* __restrict__ wi, const uint16_t* __restrict__ wg,
                                                const uint16_t* __restrict__ wu, const uint16_t* __restrict__ wd,
                                                unsigned char* __restrict__ ws, uint16_t* __restrict__ out) {
  __shared__ __attribute__((aligned(16))) unsigned char smem[163840];
  const unsigned L0 = (unsigned)(size_t)smem;
  if (__builtin_expect(blockIdx.x < 224, 1)) body<false>(x, wr, wi, wg, wu, wd, ws, out, L0);
  else body<true>(x, wr, wi, wg, wu, wd, ws, out, L0);
}

extern "C" void kernel_launch(void* const* d_in, const int* in_sizes, int n_in, void* d_out, int out_size, void* d_ws,
                              size_t ws_size, hipStream_t stream) {
  fused<<<256, 512, 0, stream>>>((const uint16_t*)d_in[0], (const uint16_t*)d_in[1], (const uint16_t*)d_in[2],
                                 (const uint16_t*)d_in[4], (const uint16_t*)d_in[5], (const uint16_t*)d_in[6],
                                 (unsigned char*)d_ws, (uint16_t*)d_out);
}
```
